```python
import torch

def _softmax(x, axis=-1):
    return torch.softmax(x, dim=axis)
def _gelu(x):
    return torch.nn.functional.gelu(x, approximate="tanh")
def _silu(x):
    return torch.nn.functional.silu(x)
def _sigmoid(x):
    return torch.sigmoid(x)
def _layernorm(x, axis=-1, eps=1e-5):
    m = x.mean(dim=axis, keepdim=True)
    v = x.var(dim=axis, keepdim=True, unbiased=False)
    return (x - m) / torch.sqrt(v + eps)
def _scatter_add(tgt, idx, src, axis=0):
    return tgt.clone().index_add_(axis, idx, src)
def _segment_sum(src, seg, n):
    out = torch.zeros((n,) + src.shape[1:], dtype=src.dtype)
    return out.index_add_(0, seg, src)
def _pad(x, pw):
    flat = tuple(v for pair in reversed(pw) for v in pair)
    return torch.nn.functional.pad(x, flat)

kernel_name = 'concat_heads__mha_block_cached__gqa_block_cached__near_ridge'

def setup_inputs():

    _w = (lambda a: (lambda t: t.to(torch.bfloat16) if t.is_floating_point() else t)(torch.as_tensor(a)))
    def _A_setup():
        B, Sq, D = 8, 224, 448
        H, Sk, Dh = 16, 512, 256
        return {
            "x": _w((lambda *s: torch.randn(*s, dtype=torch.bfloat16) * 0.8)(B, Sq, D)),
            "k_cache": _w((lambda *s: torch.randn(*s, dtype=torch.bfloat16) * 0.8)(B, H, Sk, Dh)),
            "v_cache": _w((lambda *s: torch.randn(*s, dtype=torch.bfloat16) * 0.05)(B, H, Sk, Dh)),
            "wq": _w((lambda *s: torch.randn(*s, dtype=torch.bfloat16) * 0.8)(D, H * Dh) * (D ** -0.5)),
            "wo": _w((lambda *s: torch.randn(*s, dtype=torch.bfloat16) * 0.05)(H * Dh, D) * ((H * Dh) ** -0.5)),
        }
    _A_in = _A_setup()

    def _B_setup():
        B, Sq, D = 8, 224, 448
        Hq, Hkv, Sk, Dh = 16, 16, 512, 256
        assert Hq % Hkv == 0, (Hq, Hkv)
        return {
            "x": _w((lambda *s: torch.randn(*s, dtype=torch.bfloat16) * 0.8)(B, Sq, D)),
            "k_cache": _w((lambda *s: torch.randn(*s, dtype=torch.bfloat16) * 0.8)(B, Hkv, Sk, Dh)),
            "v_cache": _w((lambda *s: torch.randn(*s, dtype=torch.bfloat16) * 0.05)(B, Hkv, Sk, Dh)),
            "wq": _w((lambda *s: torch.randn(*s, dtype=torch.bfloat16) * 0.8)(D, Hq * Dh) * (D ** -0.5)),
            "wo": _w((lambda *s: torch.randn(*s, dtype=torch.bfloat16) * 0.05)(Hq * Dh, D) * ((Hq * Dh) ** -0.5)),
        }
    _B_in = _B_setup()

    return {
        "A_x": _A_in["x"],
        "A_k_cache": _A_in["k_cache"],
        "A_v_cache": _A_in["v_cache"],
        "A_wq": _A_in["wq"],
        "A_wo": _A_in["wo"],
        "B_x": _B_in["x"],
        "B_k_cache": _B_in["k_cache"],
        "B_v_cache": _B_in["v_cache"],
        "B_wq": _B_in["wq"],
        "B_wo": _B_in["wo"],
    }

def reference(A_x, A_k_cache, A_v_cache, A_wq, A_wo, B_x, B_k_cache, B_v_cache, B_wq, B_wo):
    def _A_ref(x, k_cache, v_cache, wq, wo):
        B, Sq, D = x.shape
        H, Dh = 16, 256
        q = (lambda x, *d: x.permute(*d))((x @ wq).reshape(B, Sq, H, Dh), 0, 2, 1, 3)
        scores = torch.einsum("bhqd,bhkd->bhqk", q, k_cache) * (Dh ** -0.5)
        p = _softmax(scores, axis=-1)
        o = torch.einsum("bhqk,bhkd->bhqd", p, v_cache)
        return (lambda x, *d: x.permute(*d))(o, 0, 2, 1, 3).reshape(B, Sq, H * Dh) @ wo

    def _B_ref(x, k_cache, v_cache, wq, wo):
        B, Sq, D = x.shape
        Hq, Dh = 16, 256
        Hkv = k_cache.shape[1]
        rep = Hq // Hkv
        q = (lambda x, *d: x.permute(*d))((x @ wq).reshape(B, Sq, Hq, Dh), 0, 2, 1, 3)
        k = (lambda x, n, axis: torch.repeat_interleave(x, n, dim=axis))(k_cache, rep, axis=1)
        v = (lambda x, n, axis: torch.repeat_interleave(x, n, dim=axis))(v_cache, rep, axis=1)
        scores = torch.einsum("bhqd,bhkd->bhqk", q, k) * (Dh ** -0.5)
        p = _softmax(scores, axis=-1)
        o = torch.einsum("bhqk,bhkd->bhqd", p, v)
        return (lambda x, *d: x.permute(*d))(o, 0, 2, 1, 3).reshape(B, Sq, Hq * Dh) @ wo

    a = _A_ref(A_x, A_k_cache, A_v_cache, A_wq, A_wo)
    b = _B_ref(B_x, B_k_cache, B_v_cache, B_wq, B_wo)
    return torch.concatenate([a, b], axis=-1)
```

```cpp
#include <hip/hip_runtime.h>
#include <hip/hip_bf16.h>
typedef unsigned short u16;
__device__ __forceinline__ float b2f(u16 v){ return __uint_as_float(((unsigned)v)<<16); }
__device__ __forceinline__ u16 f2b(float x){ unsigned u=__float_as_uint(x); u += 0x7fffu + ((u>>16)&1u); return (u16)(u>>16); }
__global__ void gemm_naive(const u16* A, const u16* B, u16* C, int M, int N, int K, int ldc){
  __shared__ float As[16][65], Bs[16][65];
  int tx=threadIdx.x&15, ty=threadIdx.x>>4;
  int m0=blockIdx.y*64, n0=blockIdx.x*64;
  float acc[4][4]={};
  for(int k0=0;k0<K;k0+=16){
    for(int i=threadIdx.x;i<1024;i+=256){ int r=i>>4,c=i&15; As[c][r]=b2f(A[(size_t)(m0+r)*K+k0+c]); int kr=i>>6, cc=i&63; Bs[kr][cc]=b2f(B[(size_t)(k0+kr)*N+n0+cc]); }
    __syncthreads();
    for(int k=0;k<16;k++){
      float a[4],b[4];
      for(int i=0;i<4;i++){a[i]=As[k][ty*4+i]; b[i]=Bs[k][tx*4+i];}
      for(int i=0;i<4;i++)for(int j=0;j<4;j++)acc[i][j]+=a[i]*b[j];
    }
    __syncthreads();
  }
  for(int i=0;i<4;i++)for(int j=0;j<4;j++) C[(size_t)(m0+ty*4+i)*ldc+n0+tx*4+j]=f2b(acc[i][j]);
}
__global__ void attn_naive(const u16* Q, const u16* Kc, const u16* Vc, u16* O){
  __shared__ float q[8][256]; __shared__ float s[8][512]; __shared__ float red[8];
  int qg=blockIdx.x, h=blockIdx.y, b=blockIdx.z; int t=threadIdx.x;
  for(int r=0;r<8;r++) q[r][t]=b2f(Q[(size_t)(b*224+qg*8+r)*4096+h*256+t]);
  __syncthreads();
  const u16* K=Kc+(size_t)(b*16+h)*512*256; const u16* V=Vc+(size_t)(b*16+h)*512*256;
  for(int kk=t;kk<512;kk+=256){ float a[8]={}; for(int d=0;d<256;d++){ float kv=b2f(K[kk*256+d]); for(int r=0;r<8;r++) a[r]+=q[r][d]*kv; } for(int r=0;r<8;r++) s[r][kk]=a[r]*0.0625f; }
  __syncthreads();
  if(t<8){ float m=-1e30f; for(int k=0;k<512;k++) m=fmaxf(m,s[t][k]); float l=0; for(int k=0;k<512;k++){ float e=__expf(s[t][k]-m); s[t][k]=e; l+=e;} red[t]=1.f/l; }
  __syncthreads();
  float o[8]={};
  for(int k=0;k<512;k++){ float v=b2f(V[k*256+t]); for(int r=0;r<8;r++) o[r]+=b2f(f2b(s[r][k]))*v; }
  for(int r=0;r<8;r++) O[(size_t)(b*224+qg*8+r)*4096+h*256+t]=f2b(o[r]*red[r]);
}
extern "C" void kernel_launch(void* const* d_in, const int* in_sizes, int n_in, void* d_out, int out_size, void* d_ws, size_t ws_size, hipStream_t stream){
  u16* Q=(u16*)d_ws; u16* O=Q+1792*4096;
  for(int br=0;br<2;br++){
    const u16* x=(const u16*)d_in[br*5+0]; const u16* k=(const u16*)d_in[br*5+1]; const u16* v=(const u16*)d_in[br*5+2]; const u16* wq=(const u16*)d_in[br*5+3]; const u16* wo=(const u16*)d_in[br*5+4];
    gemm_naive<<<dim3(64,28),256,0,stream>>>(x,wq,Q,1792,4096,448,4096);
    attn_naive<<<dim3(28,16,8),256,0,stream>>>(Q,k,v,O);
    gemm_naive<<<dim3(7,28),256,0,stream>>>(O,wo,(u16*)d_out+br*448,1792,448,4096,896);
  }
}
```

```cpp
#include <hip/hip_runtime.h>

__global__ __launch_bounds__(256, 1) __attribute__((aligned(4096))) void concat_heads_fused_kernel(
    const char* x0, const char* k0, const char* v0, const char* wq0, const char* wo0,
    const char* x1, const char* k1, const char* v1, const char* wq1, const char* wo1, char* out, char* ws) {
  const unsigned w = __builtin_amdgcn_readfirstlane(threadIdx.x >> 6);
  asm volatile(
"s_bfe_u32 s24, %1, 0x10002\n"
"s_mul_i32 s27, s24, 40\n"
"s_mov_b64 s[88:89], %0\n"
"s_add_u32 s88, s88, s27\n"
"s_addc_u32 s89, s89, 0\n"
"s_load_dwordx2 s[36:37], s[88:89], 0\n"
"s_load_dwordx2 s[40:41], s[88:89], 24\n"
"s_load_dwordx2 s[44:45], s[88:89], 8\n"
"s_load_dwordx2 s[48:49], s[88:89], 16\n"
"s_load_dwordx2 s[52:53], s[88:89], 32\n"
"s_load_dwordx2 s[64:65], %0, 80\n"
"s_load_dwordx2 s[56:57], %0, 88\n"
"s_lshr_b32 s28, %1, 3\n"
"s_and_b32 s22, %1, 3\n"
"s_lshl_b32 s22, s22, 2\n"
"s_and_b32 s27, s28, 3\n"
"s_or_b32 s22, s22, s27\n"
"s_lshr_b32 s23, s28, 2\n"
"s_lshr_b32 s25, s22, 2\n"
"s_lshl_b32 s25, s25, 1\n"
"s_add_u32 s25, s25, s23\n"
"s_mul_i32 s27, s25, 37\n"
"s_lshr_b32 s27, s27, 8\n"
"s_mul_i32 s27, s27, 7\n"
"s_sub_u32 s25, s25, s27\n"
"s_lshl_b32 s25, s25, 7\n"
"s_lshl_b32 s99, s23, 12\n"
"s_mul_i32 s29, %2, 28672\n"
"s_mov_b32 s38, 200704\n"
"s_mov_b32 s39, 0x20000\n"
"s_lshl_b32 s27, s22, 9\n"
"s_sub_u32 s42, 3670016, s27\n"
"s_mov_b32 s43, 0x20000\n"
"s_mov_b32 s11, 0\n"
"s_mul_i32 s17, s23, 200704\n"
"START_%=:\n"
"v_mbcnt_lo_u32_b32 v15, -1, 0\n"
"v_mbcnt_hi_u32_b32 v15, -1, v15\n"
"s_mov_b32 s95, s25\n"
"s_mul_i32 s79, %2, 4160\n"
"s_mul_i32 s30, s23, 2\n"
"s_add_u32 s30, s30, s22\n"
"s_and_b32 s30, s30, 15\n"
"s_mul_i32 s86, s30, 43\n"
"s_lshr_b32 s86, s86, 7\n"
"s_mul_i32 s86, s86, 3\n"
"s_sub_u32 s86, s30, s86\n"
"s_mul_i32 s87, %2, 3\n"
"s_add_u32 s72, s86, s87\n"
"s_mul_i32 s68, s72, 14336\n"
"s_add_u32 s86, s86, 1\n"
"s_cmp_eq_u32 s86, 3\n"
"s_cselect_b32 s86, 0, s86\n"
"s_add_u32 s74, s86, s87\n"
"s_mul_i32 s69, s74, 14336\n"
"s_add_u32 s86, s86, 1\n"
"s_cmp_eq_u32 s86, 3\n"
"s_cselect_b32 s86, 0, s86\n"
"s_add_u32 s76, s86, s87\n"
"s_mul_i32 s70, s76, 14336\n"
"s_lshr_b32 s71, %2, 1\n"
"s_mul_i32 s71, s71, 14336\n"
"s_add_u32 s71, s71, 172032\n"
"v_lshrrev_b32 v0, 3, v15\n"
"v_lshrrev_b32 v1, 4, v15\n"
"v_mul_u32_u24 v0, 896, v0\n"
"v_xor_b32 v1, v1, v15\n"
"v_and_b32 v1, 7, v1\n"
"v_lshl_add_u32 v224, v1, 4, v0\n"
"v_xor_b32 v225, 64, v224\n"
"v_add_u32 v225, 6144, v225\n"
"s_getpc_b64 s[12:13]\n"
"PC_%=:\n"
"s_mov_b32 s14, END_%=-PC_%=\n"
"s_mov_b32 s15, 0x20000\n"
"s_lshl_b32 s86, s28, 2\n"
"s_add_u32 s86, s86, %2\n"
"s_and_b32 s86, s86, 31\n"
"s_mul_i32 s86, s86, 2304\n"
"v_mul_u32_u24 v10, 36, v15\n"
"buffer_load_dword v234, v10, s[12:15], s86 offen\n"
"s_mov_b32 s96, s25\n"
"v_bfe_u32 v2, v15, 4, 1\n"
"v_lshl_add_u32 v2, v2, 2, %2\n"
"v_and_b32 v3, 32, v15\n"
"v_add_u32 v2, v2, v3\n"
"v_and_b32 v3, 15, v15\n"
"v_lshlrev_b32 v3, 4, v3\n"
"v_lshl_add_u32 v226, v2, 13, v3\n"
"v_add_u32 v227, 0x1fc00, v226\n"
"v_add_u32 v228, 0x10000, v226\n"
"v_add_u32 v229, 0x2fc00, v226\n"
"s_and_b32 s81, s22, 3\n"
"s_lshl_b32 s80, s25, 12\n"
"v_mov_b32 v12, 0x2c00\n"
"s_cmp_lg_u32 s11, 0\n"
"s_cbranch_scc1 REDO1_%=\n"
"s_waitcnt lgkmcnt(0)\n"
"s_add_u32 s36, s36, s17\n"
"s_addc_u32 s37, s37, 0\n"
"s_add_u32 s40, s40, s27\n"
"s_addc_u32 s41, s41, 0\n"
"REDO1_%=:\n"
"s_add_u32 s88, s40, s80\n"
"s_addc_u32 s89, s41, 0\n"
"s_bitcmp0_b32 s23, 0\n"
"s_cselect_b64 s[88:89], s[36:37], s[88:89]\n"
"global_load_dword v235, v12, s[88:89]\n"
"s_add_u32 m0, s79, 0x0\n"
"s_add_u32 s82, s95, s68\n"
"buffer_load_dwordx4 v224, s[36:39], s82 offen offset:0 lds\n"
"buffer_load_dwordx4 v225, s[36:39], s82 offen offset:1024 lds\n"
"s_addk_i32 m0, 0x800\n"
"s_add_u32 s83, s95, s69\n"
"buffer_load_dwordx4 v224, s[36:39], s83 offen offset:0 lds\n"
"buffer_load_dwordx4 v225, s[36:39], s83 offen offset:1024 lds\n"
"s_add_u32 m0, s79, 0x40e0\n"
"s_add_u32 s82, s95, s70\n"
"buffer_load_dwordx4 v224, s[36:39], s82 offen offset:0 lds\n"
"buffer_load_dwordx4 v225, s[36:39], s82 offen offset:1024 lds\n"
"s_addk_i32 m0, 0x800\n"
"s_add_u32 s83, s95, s71\n"
"buffer_load_dwordx4 v224, s[36:39], s83 offen offset:0 lds\n"
"buffer_load_dwordx4 v225, s[36:39], s83 offen offset:1024 lds\n"
"s_lshl_b32 s80, s96, 12\n"
"s_add_u32 m0, s79, 0x81c0\n"
"s_nop 0\n"
"buffer_load_dwordx4 v226, s[40:43], s80 offen offset:0 lds\n"
"buffer_load_dwordx4 v227, s[40:43], s80 offen offset:1024 lds\n"
"s_addk_i32 m0, 0x808\n"
"s_nop 0\n"
"buffer_load_dwordx4 v228, s[40:43], s80 offen offset:0 lds\n"
"buffer_load_dwordx4 v229, s[40:43], s80 offen offset:1024 lds\n"
"s_cmp_lg_u32 s11, 0\n"
"s_cbranch_scc1 REDO_%=\n"
"s_lshl_b32 s4, s23, 4\n"
"s_or_b32 s4, s4, s22\n"
"s_lshl_b32 s4, s4, 18\n"
"s_mul_i32 s5, s22, 229376\n"
"s_mul_i32 s6, s23, 401408\n"
"s_mul_i32 s7, s24, 896\n"
"s_add_u32 s6, s6, s7\n"
"s_lshl_b32 s7, s24, 26\n"
"s_mul_i32 s86, s23, 8404992\n"
"s_add_u32 s7, s7, s86\n"
"s_add_u32 s44, s44, s4\n"
"s_addc_u32 s45, s45, 0\n"
"s_add_u32 s48, s48, s4\n"
"s_addc_u32 s49, s49, 0\n"
"s_add_u32 s56, s56, 0x1fffff\n"
"s_addc_u32 s57, s57, 0\n"
"s_and_b32 s56, s56, 0xffe00000\n"
"s_add_u32 s56, s56, s7\n"
"s_addc_u32 s57, s57, 0\n"
"s_add_u32 s52, s52, s5\n"
"s_addc_u32 s53, s53, 0\n"
"s_add_u32 s64, s64, s6\n"
"s_addc_u32 s65, s65, 0\n"
"s_add_u32 s60, s56, 7340032\n"
"s_addc_u32 s61, s57, 0\n"
"s_mov_b32 s26, s23\n"
"s_mul_i32 s27, s26, 37\n"
"s_lshr_b32 s27, s27, 8\n"
"s_mul_i32 s27, s27, 7\n"
"s_sub_u32 s26, s26, s27\n"
"s_lshl_b32 s26, s26, 7\n"
"s_mov_b32 s46, 262144\n"
"s_mov_b32 s47, 0x20000\n"
"s_mov_b32 s50, 262144\n"
"s_mov_b32 s51, 0x20000\n"
"s_mov_b32 s54, 229376\n"
"s_mov_b32 s55, 0x20000\n"
"s_mov_b32 s58, 7340032\n"
"s_mov_b32 s59, 0x20000\n"
"s_mov_b32 s66, 400512\n"
"s_mov_b32 s67, 0x20000\n"
"s_mov_b32 s62, 147456\n"
"s_mov_b32 s63, 0x20000\n"
"s_mov_b32 s85, 0x5f800000\n"
"s_mov_b32 s16, 0x40000\n"
"v_mov_b32 v249, 0xff800000\n"
"v_mov_b32 v250, 0xff800000\n"
"v_mov_b32 v251, 0xff800000\n"
"v_mov_b32 v252, 0xff800000\n"
"REDO_%=:\n"
"s_cmp_eq_u32 s81, 0\n"
"s_cselect_b64 s[88:89], s[60:61], s[88:89]\n"
"s_cmp_eq_u32 s81, 1\n"
"s_cselect_b64 s[88:89], s[52:53], s[88:89]\n"
"s_cmp_eq_u32 s81, 2\n"
"s_cselect_b64 s[88:89], s[44:45], s[88:89]\n"
"s_cmp_eq_u32 s81, 3\n"
"s_cselect_b64 s[88:89], s[48:49], s[88:89]\n"
"global_load_dword v237, v12, s[88:89]\n"
"s_and_b32 s84, %2, 1\n"
"v_bfe_u32 v4, v15, 2, 2\n"
"v_mul_u32_u24 v4, 4160, v4\n"
"v_bfe_u32 v5, v15, 4, 1\n"
"v_mul_u32_u24 v5, 2056, v5\n"
"v_bfe_u32 v6, v15, 5, 1\n"
"v_lshl_add_u32 v4, v6, 10, v4\n"
"v_and_b32 v7, 3, v15\n"
"v_lshl_add_u32 v5, v7, 4, v5\n"
"s_lshl_b32 s86, s84, 6\n"
"v_add3_u32 v230, v4, v5, s86\n"
"s_xor_b32 s86, s86, 64\n"
"v_add3_u32 v231, v4, v5, s86\n"
"v_and_b32 v4, 15, v15\n"
"v_lshrrev_b32 v5, 1, v4\n"
"v_lshrrev_b32 v6, 4, v15\n"
"v_xor_b32 v5, v5, v6\n"
"v_lshlrev_b32 v5, 4, v5\n"
"v_lshl_add_u32 v4, v4, 7, v5\n"
"v_add_u32 v232, s79, v4\n"
"v_xor_b32 v4, 64, v4\n"
"v_add_u32 v233, s79, v4\n"
"s_lshl_b32 s86, %2, 12\n"
"v_lshl_add_u32 v248, v15, 4, s86\n"
"s_mov_b32 s97, s26\n"
"s_mov_b32 s98, s26\n"
"v_and_b32 v8, 7, v15\n"
"v_mul_u32_u24 v8, 2080, v8\n"
"v_bfe_u32 v9, v15, 3, 1\n"
"v_lshl_add_u32 v8, v9, 10, v8\n"
"v_and_b32 v9, 48, v15\n"
"s_mul_i32 s86, s84, 576\n"
"s_xor_b32 s86, s86, 0\n"
"v_add3_u32 v240, v8, v9, s86\n"
"s_mul_i32 s86, s84, 576\n"
"s_xor_b32 s86, s86, 64\n"
"v_add3_u32 v241, v8, v9, s86\n"
"s_mul_i32 s86, s84, 576\n"
"s_xor_b32 s86, s86, 512\n"
"v_add3_u32 v242, v8, v9, s86\n"
"s_mul_i32 s86, s84, 576\n"
"s_xor_b32 s86, s86, 576\n"
"v_add3_u32 v243, v8, v9, s86\n"
"v_bfe_u32 v8, v15, 2, 3\n"
"v_mul_u32_u24 v8, 2080, v8\n"
"v_bfe_u32 v9, v15, 5, 1\n"
"v_lshl_add_u32 v8, v9, 10, v8\n"
"v_and_b32 v9, 3, v15\n"
"v_lshlrev_b32 v9, 3, v9\n"
"s_mul_i32 s86, s84, 576\n"
"s_xor_b32 s86, s86, 0\n"
"v_add3_u32 v244, v8, v9, s86\n"
"s_mul_i32 s86, s84, 576\n"
"s_xor_b32 s86, s86, 64\n"
"v_add3_u32 v245, v8, v9, s86\n"
"s_mul_i32 s86, s84, 576\n"
"s_xor_b32 s86, s86, 512\n"
"v_add3_u32 v246, v8, v9, s86\n"
"s_mul_i32 s86, s84, 576\n"
"s_xor_b32 s86, s86, 576\n"
"v_add3_u32 v247, v8, v9, s86\n"
"s_mul_i32 s86, %2, 2560\n"
"s_add_u32 s86, s86, 149472\n"
"v_lshl_add_u32 v253, v15, 3, s86\n"
"s_xor_b32 s86, %2, 1\n"
"s_mul_i32 s86, s86, 2560\n"
"s_add_u32 s86, s86, 149472\n"
"v_lshl_add_u32 v254, v15, 3, s86\n"
"s_add_u32 s24, s29, 0x20000\n"
"s_mov_b32 s92, 0x3db8aa3b\n"
"s_mul_i32 s93, s84, -256\n"
"s_mul_i32 s94, s84, -768\n"
"s_add_u32 s94, s94, 256\n"
"s_lshl_b32 s86, s22, 9\n"
"s_lshl_b32 s87, s84, 15\n"
"s_add_u32 s87, s87, s86\n"
"s_lshl_b32 s72, s72, 16\n"
"s_add_u32 s72, s72, s87\n"
"s_xor_b32 s73, s72, 0x8000\n"
"s_lshl_b32 s74, s74, 16\n"
"s_add_u32 s74, s74, s87\n"
"s_xor_b32 s75, s74, 0x8000\n"
"s_lshl_b32 s76, s76, 16\n"
"s_add_u32 s76, s76, s87\n"
"s_xor_b32 s77, s76, 0x8000\n"
"s_lshl_b32 s78, %2, 15\n"
"s_add_u32 s78, s78, 786432\n"
"s_add_u32 s78, s78, s86\n"
"s_lshl_b32 s34, s30, 2\n"
"s_add_u32 s34, s34, %2\n"
"s_lshl_b32 s34, s34, 14\n"
"s_cmp_ge_u32 s30, 14\n"
"s_cselect_b32 s34, 0x40000000, s34\n"
"s_add_u32 s35, s34, 4096\n"
"s_mul_i32 s18, s30, 28672\n"
"s_lshr_b32 s86, %2, 1\n"
"s_lshl_b32 s86, s86, 6\n"
"s_add_u32 s18, s18, s86\n"
"s_lshl_b32 s86, s84, 4\n"
"s_add_u32 s18, s18, s86\n"
"s_mul_i32 s86, s30, 43\n"
"s_lshr_b32 s86, s86, 7\n"
"s_lshr_b32 s87, %2, 1\n"
"s_cmp_eq_u32 s30, 12\n"
"s_cselect_b32 s86, s87, s86\n"
"s_add_u32 s87, s87, 2\n"
"s_cmp_ge_u32 s30, 13\n"
"s_cselect_b32 s86, s87, s86\n"
"s_lshl_b32 s27, s86, 4\n"
"s_lshl_b32 s81, s96, 12\n"
"s_add_u32 s81, s81, 0x100\n"
"s_add_u32 m0, s79, 0xc2a0\n"
"s_nop 0\n"
"buffer_load_dwordx4 v226, s[40:43], s81 offen offset:0 lds\n"
"buffer_load_dwordx4 v227, s[40:43], s81 offen offset:1024 lds\n"
"s_addk_i32 m0, 0x808\n"
"s_nop 0\n"
"buffer_load_dwordx4 v228, s[40:43], s81 offen offset:0 lds\n"
"buffer_load_dwordx4 v229, s[40:43], s81 offen offset:1024 lds\n"
"s_waitcnt vmcnt(5)\n"
"s_barrier\n"
"ds_read_b128 a[0:3], v232 offset:0\n"
"ds_read_b64_tr_b16 a[224:225], v230 offset:33216\n"
"ds_read_b64_tr_b16 a[226:227], v230 offset:33472\n"
"ds_read_b128 a[16:19], v233 offset:0\n"
"ds_read_b64_tr_b16 a[228:229], v230 offset:33728\n"
"ds_read_b64_tr_b16 a[230:231], v230 offset:33984\n"
"ds_read_b128 a[20:23], v233 offset:2048\n"
"ds_read_b128 a[4:7], v232 offset:2048\n"
"ds_read_b128 a[8:11], v232 offset:16608\n"
"ds_read_b128 a[24:27], v233 offset:16608\n"
"ds_read_b128 a[28:31], v233 offset:18656\n"
"ds_read_b128 a[12:15], v232 offset:18656\n"
"ds_read_b64_tr_b16 a[232:233], v230 offset:33224\n"
"s_waitcnt lgkmcnt(10)\n"
"v_mfma_f32_16x16x32_bf16 v[0:3], a[224:227], a[0:3], 0\n"
"s_waitcnt lgkmcnt(7)\n"
"s_add_u32 s95, s95, 128\n"
"s_cmp_eq_u32 s95, 896\n"
"s_cselect_b32 s95, 0, s95\n"
"s_add_u32 m0, s79, 0x10380\n"
"v_mfma_f32_16x16x32_bf16 v[0:3], a[228:231], a[16:19], v[0:3]\n"
"s_waitcnt lgkmcnt(6)\n"
"s_add_u32 s82, s95, s68\n"
"ds_read_b64_tr_b16 a[234:235], v230 offset:33480\n"
"v_mfma_f32_16x16x32_bf16 v[8:11], a[228:231], a[20:23], 0\n"
"s_waitcnt lgkmcnt(6)\n"
"buffer_load_dwordx4 v224, s[36:39], s82 offen offset:0 lds\n"
"v_mfma_f32_16x16x32_bf16 v[8:11], a[224:227], a[4:7], v[8:11]\n"
"s_waitcnt lgkmcnt(5)\n"
"s_add_u32 s83, s95, s69\n"
"ds_read_b64_tr_b16 a[236:237], v230 offset:33736\n"
"v_mfma_f32_16x16x32_bf16 v[16:19], a[224:227], a[8:11], 0\n"
"s_waitcnt lgkmcnt(5)\n"
"s_add_u32 s96, s96, 128\n"
"s_cmp_eq_u32 s96, 896\n"
"s_cselect_b32 s96, 0, s96\n"
"ds_read_b64_tr_b16 a[238:239], v230 offset:33992\n"
"v_mfma_f32_16x16x32_bf16 v[16:19], a[228:231], a[24:27], v[16:19]\n"
"s_waitcnt lgkmcnt(5)\n"
"s_lshl_b32 s80, s96, 12\n"
"ds_read_b64_tr_b16 a[240:241], v231 offset:33216\n"
"v_mfma_f32_16x16x32_bf16 v[24:27], a[228:231], a[28:31], 0\n"
"s_waitcnt lgkmcnt(5)\n"
"s_mov_b64 exec, 0\n"
"s_cmp_eq_u32 %2, 1\n"
"s_cbranch_scc1 PG2_%=\n"
"s_cmp_eq_u32 %2, 2\n"
"s_cbranch_scc1 PG3_%=\n"
"s_cmp_eq_u32 %2, 3\n"
"s_cbranch_scc1 PG4_%=\n"
"s_branch PG1_%=\n"
"PGB0_%=:\n"
"s_mov_b64 exec, -1\n"
"s_nop 3\n"
"v_mfma_f32_16x16x32_bf16 v[24:27], a[224:227], a[12:15], v[24:27]\n"
"s_waitcnt lgkmcnt(3)\n"
"s_mov_b32 s86, 4096\n"
"ds_read_b64_tr_b16 a[242:243], v231 offset:33472\n"
"v_mfma_f32_16x16x32_bf16 v[28:31], a[232:235], a[12:15], 0\n"
"s_waitcnt lgkmcnt(2)\n"
"ds_read_b64_tr_b16 a[244:245], v231 offset:33728\n"
"v_bfe_u32 v238, v248, 4, 6\n"
"v_mfma_f32_16x16x32_bf16 v[28:31], a[236:239], a[28:31], v[28:31]\n"
"s_lshl_b32 s81, s96, 12\n"
"ds_read_b64_tr_b16 a[246:247], v231 offset:33984\n"
"v_lshlrev_b32 v238, 13, v238\n"
"v_mfma_f32_16x16x32_bf16 v[20:23], a[236:239], a[24:27], 0\n"
"s_add_u32 s81, s81, 0x100\n"
"ds_read_b64_tr_b16 a[252:253], v231 offset:33736\n"
"v_mov_b32 v239, 1\n"
"v_mfma_f32_16x16x32_bf16 v[20:23], a[232:235], a[8:11], v[20:23]\n"
"s_add_u32 s96, s96, 128\n"
"s_cmp_eq_u32 s96, 896\n"
"s_cselect_b32 s96, 0, s96\n"
"ds_read_b64_tr_b16 a[254:255], v231 offset:33992\n"
"v_add_u32 v232, 0x10380, v232\n"
"v_mfma_f32_16x16x32_bf16 v[12:15], a[232:235], a[4:7], 0\n"
"s_mov_b32 s9, s99\n"
"ds_read_b64_tr_b16 a[248:249], v231 offset:33224\n"
"v_add_u32 v233, 0x10380, v233\n"
"v_mfma_f32_16x16x32_bf16 v[12:15], a[236:239], a[20:23], v[12:15]\n"
"s_mov_b32 s10, s99\n"
"ds_read_b64_tr_b16 a[250:251], v231 offset:33480\n"
"v_add_u32 v240, 0x14460, v240\n"
"v_mfma_f32_16x16x32_bf16 v[4:7], a[236:239], a[16:19], 0\n"
"s_and_b32 s19, s22, 3\n"
"s_lshl_b32 s19, s19, 21\n"
"s_add_u32 s19, s19, 0xe0000\n"
"buffer_load_dwordx4 v225, s[36:39], s82 offen offset:1024 lds\n"
"v_mfma_f32_16x16x32_bf16 v[4:7], a[232:235], a[0:3], v[4:7]\n"
"s_waitcnt lgkmcnt(6)\n"
"s_addk_i32 m0, 0x800\n"
"s_add_u32 s82, s95, s70\n"
"v_mfma_f32_16x16x32_bf16 v[32:35], a[240:243], a[0:3], 0\n"
"s_waitcnt lgkmcnt(4)\n"
"ds_read_b64_tr_b16 a[224:225], v230 offset:33344\n"
"v_add_u32 v242, 0x14460, v242\n"
"v_mfma_f32_16x16x32_bf16 v[32:35], a[244:247], a[16:19], v[32:35]\n"
"s_mov_b32 s5, 0\n"
"ds_read_b64_tr_b16 a[226:227], v230 offset:33600\n"
"v_add_u32 v241, 0x14460, v241\n"
"v_mfma_f32_16x16x32_bf16 v[40:43], a[244:247], a[20:23], 0\n"
"s_mov_b32 s6, 0\n"
"ds_read_b64_tr_b16 a[228:229], v230 offset:33856\n"
"v_add_u32 v243, 0x14460, v243\n"
"v_mfma_f32_16x16x32_bf16 v[40:43], a[240:243], a[4:7], v[40:43]\n"
"s_mov_b32 s7, 0\n"
"s_mov_b32 s8, 0\n"
"ds_read_b64_tr_b16 a[230:231], v230 offset:34112\n"
"v_mfma_f32_16x16x32_bf16 v[48:51], a[240:243], a[8:11], 0\n"
"s_mov_b32 s4, 0\n"
"ds_read_b64_tr_b16 a[232:233], v230 offset:33352\n"
"v_mfma_f32_16x16x32_bf16 v[48:51], a[244:247], a[24:27], v[48:51]\n"
"s_waitcnt lgkmcnt(7)\n"
"ds_read_b64_tr_b16 a[234:235], v230 offset:33608\n"
"v_mfma_f32_16x16x32_bf16 v[52:55], a[252:255], a[24:27], 0\n"
"s_waitcnt lgkmcnt(6)\n"
"ds_read_b64_tr_b16 a[236:237], v230 offset:33864\n"
"v_mfma_f32_16x16x32_bf16 v[52:55], a[248:251], a[8:11], v[52:55]\n"
"ds_read_b64_tr_b16 a[238:239], v230 offset:34120\n"
"v_mfma_f32_16x16x32_bf16 v[44:47], a[248:251], a[4:7], 0\n"
"s_mov_b64 exec, 0\n"
"s_cmp_eq_u32 %2, 1\n"
"s_cbranch_scc1 PG6_%=\n"
"s_cmp_eq_u32 %2, 2\n"
"s_cbranch_scc1 PG7_%=\n"
"s_cmp_eq_u32 %2, 3\n"
"s_cbranch_scc1 PG8_%=\n"
"s_branch PG5_%=\n"
"PGB1_%=:\n"
"s_mov_b64 exec, -1\n"
"s_nop 3\n"
"v_mfma_f32_16x16x32_bf16 v[44:47], a[252:255], a[20:23], v[44:47]\n"
"ds_read_b64_tr_b16 a[240:241], v231 offset:33344\n"
"v_mfma_f32_16x16x32_bf16 v[36:39], a[252:255], a[16:19], 0\n"
"ds_read_b64_tr_b16 a[242:243], v231 offset:33600\n"
"v_mfma_f32_16x16x32_bf16 v[36:39], a[248:251], a[0:3], v[36:39]\n"
"s_waitcnt lgkmcnt(8)\n"
"ds_read_b64_tr_b16 a[244:245], v231 offset:33856\n"
"v_mfma_f32_16x16x32_bf16 v[56:59], a[224:227], a[0:3], 0\n"
"s_waitcnt lgkmcnt(7)\n"
"buffer_load_dwordx4 v224, s[36:39], s83 offen offset:0 lds\n"
"v_mfma_f32_16x16x32_bf16 v[56:59], a[228:231], a[16:19], v[56:59]\n"
"ds_read_b64_tr_b16 a[246:247], v231 offset:34112\n"
"v_mfma_f32_16x16x32_bf16 v[64:67], a[228:231], a[20:23], 0\n"
"ds_read_b64_tr_b16 a[248:249], v231 offset:33352\n"
"v_mfma_f32_16x16x32_bf16 v[64:67], a[224:227], a[4:7], v[64:67]\n"
"s_waitcnt vmcnt(7)\n"
"buffer_load_dwordx2 v[236:237], off, s[60:63], s86 sc1\n"
"v_mfma_f32_16x16x32_bf16 v[72:75], a[224:227], a[8:11], 0\n"
"s_lshl_b32 s86, s22, 2\n"
"s_add_u32 s86, s86, %2\n"
"s_lshl_b32 s86, s86, 7\n"
"s_add_u32 s86, s86, 65536\n"
"s_add_u32 s12, s60, s86\n"
"s_addc_u32 s13, s61, 0\n"
"ds_read_b64_tr_b16 a[250:251], v231 offset:33608\n"
"v_mfma_f32_16x16x32_bf16 v[72:75], a[228:231], a[24:27], v[72:75]\n"
"s_mov_b32 s14, 0\n"
"s_mov_b32 s15, 0\n"
"ds_read_b64_tr_b16 a[252:253], v231 offset:33864\n"
"v_mfma_f32_16x16x32_bf16 v[80:83], a[228:231], a[28:31], 0\n"
"ds_read_b64_tr_b16 a[254:255], v231 offset:34120\n"
"v_mfma_f32_16x16x32_bf16 v[80:83], a[224:227], a[12:15], v[80:83]\n"
"s_waitcnt lgkmcnt(8)\n"
"v_mfma_f32_16x16x32_bf16 v[84:87], a[232:235], a[12:15], 0\n"
"v_mfma_f32_16x16x32_bf16 v[84:87], a[236:239], a[28:31], v[84:87]\n"
"v_mfma_f32_16x16x32_bf16 v[76:79], a[236:239], a[24:27], 0\n"
"buffer_store_dword v239, v238, s[60:63], s86 offen sc1\n"
"v_mfma_f32_16x16x32_bf16 v[76:79], a[232:235], a[8:11], v[76:79]\n"
"s_waitcnt vmcnt(5) lgkmcnt(0)\n"
"s_barrier\n"
"v_mfma_f32_16x16x32_bf16 v[68:71], a[232:235], a[4:7], 0\n"
"s_mul_i32 s86, %2, 1792\n"
"ds_read_b64_tr_b16 a[224:225], v230 offset:49824\n"
"v_add_u32 v238, 0x40e0, v248\n"
"v_mfma_f32_16x16x32_bf16 v[68:71], a[236:239], a[20:23], v[68:71]\n"
"ds_read_b64_tr_b16 a[226:227], v230 offset:50080\n"
"v_xor_b32 v239, 0x1000, v248\n"
"v_mfma_f32_16x16x32_bf16 v[60:63], a[236:239], a[16:19], 0\n"
"buffer_load_dwordx4 v225, s[36:39], s83 offen offset:1024 lds\n"
"v_mfma_f32_16x16x32_bf16 v[60:63], a[232:235], a[0:3], v[60:63]\n"
"s_add_u32 m0, s79, 0x14460\n"
"s_add_u32 s83, s95, s71\n"
"ds_read_b64_tr_b16 a[228:229], v230 offset:50336\n"
"v_mfma_f32_16x16x32_bf16 v[88:91], a[240:243], a[0:3], 0\n"
"s_add_u32 s95, s95, 128\n"
"s_cmp_eq_u32 s95, 896\n"
"s_cselect_b32 s95, 0, s95\n"
"ds_read_b64_tr_b16 a[230:231], v230 offset:50592\n"
"v_add_u32 v239, 0x40e0, v239\n"
"v_mfma_f32_16x16x32_bf16 v[88:91], a[244:247], a[16:19], v[88:91]\n"
"buffer_load_dwordx4 v224, s[36:39], s82 offen offset:0 lds\n"
"v_mfma_f32_16x16x32_bf16 v[96:99], a[244:247], a[20:23], 0\n"
"ds_read_b64_tr_b16 a[232:233], v230 offset:49832\n"
"v_mfma_f32_16x16x32_bf16 v[96:99], a[240:243], a[4:7], v[96:99]\n"
"ds_read_b64_tr_b16 a[234:235], v230 offset:50088\n"
"v_mfma_f32_16x16x32_bf16 v[104:107], a[240:243], a[8:11], 0\n"
"buffer_load_dwordx4 v225, s[36:39], s82 offen offset:1024 lds\n"
"v_mfma_f32_16x16x32_bf16 v[104:107], a[244:247], a[24:27], v[104:107]\n"
"s_addk_i32 m0, 0x800\n"
"s_add_u32 s82, s95, s68\n"
"ds_read_b64_tr_b16 a[236:237], v230 offset:50344\n"
"v_mfma_f32_16x16x32_bf16 v[108:111], a[252:255], a[24:27], 0\n"
"s_mov_b64 exec, 0\n"
"s_cmp_eq_u32 %2, 1\n"
"s_cbranch_scc1 PG10_%=\n"
"s_cmp_eq_u32 %2, 2\n"
"s_cbranch_scc1 PG11_%=\n"
"s_cmp_eq_u32 %2, 3\n"
"s_cbranch_scc1 PG12_%=\n"
"s_branch PG9_%=\n"
"PGB2_%=:\n"
"s_mov_b64 exec, -1\n"
"s_nop 3\n"
"v_mfma_f32_16x16x32_bf16 v[108:111], a[248:251], a[8:11], v[108:111]\n"
"buffer_load_dwordx4 v224, s[36:39], s83 offen offset:0 lds\n"
"v_mfma_f32_16x16x32_bf16 v[100:103], a[248:251], a[4:7], 0\n"
"ds_read_b64_tr_b16 a[238:239], v230 offset:50600\n"
"v_mfma_f32_16x16x32_bf16 v[100:103], a[252:255], a[20:23], v[100:103]\n"
"ds_read_b64_tr_b16 a[240:241], v231 offset:49824\n"
"v_mfma_f32_16x16x32_bf16 v[92:95], a[252:255], a[16:19], 0\n"
"ds_read_b64_tr_b16 a[242:243], v231 offset:50080\n"
"v_mfma_f32_16x16x32_bf16 v[92:95], a[248:251], a[0:3], v[92:95]\n"
"s_waitcnt lgkmcnt(7)\n"
"ds_read_b64_tr_b16 a[244:245], v231 offset:50336\n"
"v_mfma_f32_16x16x32_bf16 v[112:115], a[224:227], a[0:3], 0\n"
"s_waitcnt lgkmcnt(7)\n"
"ds_read_b64_tr_b16 a[246:247], v231 offset:50592\n"
"v_mfma_f32_16x16x32_bf16 v[112:115], a[228:231], a[16:19], v[112:115]\n"
"ds_read_b64_tr_b16 a[248:249], v231 offset:49832\n"
"v_mfma_f32_16x16x32_bf16 v[120:123], a[228:231], a[20:23], 0\n"
"ds_read_b64_tr_b16 a[252:253], v231 offset:50344\n"
"v_mfma_f32_16x16x32_bf16 v[120:123], a[224:227], a[4:7], v[120:123]\n"
"ds_read_b64_tr_b16 a[254:255], v231 offset:50600\n"
"v_mfma_f32_16x16x32_bf16 v[128:131], a[224:227], a[8:11], 0\n"
"ds_read_b64_tr_b16 a[250:251], v231 offset:50088\n"
"v_mfma_f32_16x16x32_bf16 v[128:131], a[228:231], a[24:27], v[128:131]\n"
"v_mfma_f32_16x16x32_bf16 v[136:139], a[228:231], a[28:31], 0\n"
"ds_read_b64_tr_b16 a[228:229], v230 offset:50464\n"
"v_mfma_f32_16x16x32_bf16 v[136:139], a[224:227], a[12:15], v[136:139]\n"
"s_waitcnt lgkmcnt(10)\n"
"ds_read_b64_tr_b16 a[230:231], v230 offset:50720\n"
"v_mfma_f32_16x16x32_bf16 v[140:143], a[232:235], a[12:15], 0\n"
"s_waitcnt lgkmcnt(10)\n"
"ds_read_b64_tr_b16 a[224:225], v230 offset:49952\n"
"v_mfma_f32_16x16x32_bf16 v[140:143], a[236:239], a[28:31], v[140:143]\n"
"ds_read_b64_tr_b16 a[226:227], v230 offset:50208\n"
"v_mfma_f32_16x16x32_bf16 v[132:135], a[236:239], a[24:27], 0\n"
"v_mfma_f32_16x16x32_bf16 v[132:135], a[232:235], a[8:11], v[132:135]\n"
"v_mfma_f32_16x16x32_bf16 v[124:127], a[232:235], a[4:7], 0\n"
"buffer_load_dwordx4 v225, s[36:39], s83 offen offset:1024 lds\n"
"v_mfma_f32_16x16x32_bf16 v[124:127], a[236:239], a[20:23], v[124:127]\n"
"s_add_u32 m0, s79, 0x18540\n"
"s_add_u32 s83, s95, s69\n"
"v_mfma_f32_16x16x32_bf16 v[116:119], a[236:239], a[16:19], 0\n"
"ds_read_b64_tr_b16 a[236:237], v230 offset:50472\n"
"v_mfma_f32_16x16x32_bf16 v[116:119], a[232:235], a[0:3], v[116:119]\n"
"s_waitcnt lgkmcnt(6)\n"
"buffer_load_dwordx4 v226, s[40:43], s80 offen offset:0 lds\n"
"v_mfma_f32_16x16x32_bf16 v[144:147], a[240:243], a[0:3], 0\n"
"ds_read_b64_tr_b16 a[238:239], v230 offset:50728\n"
"v_mfma_f32_16x16x32_bf16 v[144:147], a[244:247], a[16:19], v[144:147]\n"
"ds_read_b64_tr_b16 a[232:233], v230 offset:49960\n"
"v_mfma_f32_16x16x32_bf16 v[152:155], a[244:247], a[20:23], 0\n"
"buffer_load_dwordx4 v227, s[40:43], s80 offen offset:1024 lds\n"
"v_mfma_f32_16x16x32_bf16 v[152:155], a[240:243], a[4:7], v[152:155]\n"
"s_addk_i32 m0, 0x808\n"
"ds_read_b64_tr_b16 a[234:235], v230 offset:50216\n"
"v_add_u32 v230, 0x18540, v230\n"
"v_mfma_f32_16x16x32_bf16 v[160:163], a[240:243], a[8:11], 0\n"
"ds_read_b64_tr_b16 a[240:241], v231 offset:49952\n"
"v_mfma_f32_16x16x32_bf16 v[160:163], a[244:247], a[24:27], v[160:163]\n"
"buffer_load_dwordx4 v228, s[40:43], s80 offen offset:0 lds\n"
"v_mfma_f32_16x16x32_bf16 v[164:167], a[252:255], a[24:27], 0\n"
"s_waitcnt lgkmcnt(5)\n"
"ds_read_b64_tr_b16 a[242:243], v231 offset:50208\n"
"v_mfma_f32_16x16x32_bf16 v[164:167], a[248:251], a[8:11], v[164:167]\n"
"ds_read_b64_tr_b16 a[244:245], v231 offset:50464\n"
"v_mfma_f32_16x16x32_bf16 v[156:159], a[248:251], a[4:7], 0\n"
"buffer_load_dwordx4 v229, s[40:43], s80 offen offset:1024 lds\n"
"v_mfma_f32_16x16x32_bf16 v[156:159], a[252:255], a[20:23], v[156:159]\n"
"s_add_u32 m0, s79, 0x1c620\n"
"s_lshl_b32 s80, s96, 12\n"
"ds_read_b64_tr_b16 a[246:247], v231 offset:50720\n"
"v_mfma_f32_16x16x32_bf16 v[148:151], a[252:255], a[16:19], 0\n"
"ds_read_b64_tr_b16 a[252:253], v231 offset:50472\n"
"v_mfma_f32_16x16x32_bf16 v[148:151], a[248:251], a[0:3], v[148:151]\n"
"buffer_load_dwordx4 v224, s[36:39], s82 offen offset:0 lds\n"
"v_mfma_f32_16x16x32_bf16 v[168:171], a[224:227], a[0:3], 0\n"
"ds_read_b64_tr_b16 a[254:255], v231 offset:50728\n"
"v_mfma_f32_16x16x32_bf16 v[168:171], a[228:231], a[16:19], v[168:171]\n"
"ds_read_b64_tr_b16 a[248:249], v231 offset:49960\n"
"v_mfma_f32_16x16x32_bf16 v[176:179], a[228:231], a[20:23], 0\n"
"buffer_load_dwordx4 v225, s[36:39], s82 offen offset:1024 lds\n"
"v_mfma_f32_16x16x32_bf16 v[176:179], a[224:227], a[4:7], v[176:179]\n"
"s_addk_i32 m0, 0x800\n"
"s_add_u32 s82, s95, s70\n"
"ds_read_b64_tr_b16 a[250:251], v231 offset:50216\n"
"v_mfma_f32_16x16x32_bf16 v[184:187], a[224:227], a[8:11], 0\n"
"v_add_u32 v231, 0x18540, v231\n"
"v_mfma_f32_16x16x32_bf16 v[184:187], a[228:231], a[24:27], v[184:187]\n"
"buffer_load_dwordx4 v224, s[36:39], s83 offen offset:0 lds\n"
"v_mfma_f32_16x16x32_bf16 v[192:195], a[228:231], a[28:31], 0\n"
"s_mov_b64 exec, 0\n"
"s_cmp_eq_u32 %2, 1\n"
"s_cbranch_scc1 PG14_%=\n"
"s_cmp_eq_u32 %2, 2\n"
"s_cbranch_scc1 PG15_%=\n"
"s_cmp_eq_u32 %2, 3\n"
"s_cbranch_scc1 PGB3_%=\n"
"s_branch PG13_%=\n"
"PGB3_%=:\n"
"s_mov_b64 exec, -1\n"
"s_nop 3\n"
"v_mfma_f32_16x16x32_bf16 v[192:195], a[224:227], a[12:15], v[192:195]\n"
"s_waitcnt lgkmcnt(5)\n"
"v_mfma_f32_16x16x32_bf16 v[196:199], a[232:235], a[12:15], 0\n"
"buffer_load_dwordx4 v225, s[36:39], s83 offen offset:1024 lds\n"
"v_mfma_f32_16x16x32_bf16 v[196:199], a[236:239], a[28:31], v[196:199]\n"
"s_add_u32 m0, s79, 0x20700\n"
"s_add_u32 s83, s95, s71\n"
"v_mfma_f32_16x16x32_bf16 v[188:191], a[236:239], a[24:27], 0\n"
"s_waitcnt vmcnt(4) lgkmcnt(0)\n"
"s_barrier\n"
"v_mfma_f32_16x16x32_bf16 v[188:191], a[232:235], a[8:11], v[188:191]\n"
"s_load_dword s4, s[12:13], 0x0\n"
"buffer_load_dwordx4 v224, s[36:39], s82 offen offset:0 lds\n"
"v_mfma_f32_16x16x32_bf16 v[180:183], a[232:235], a[4:7], 0\n"
"s_add_u32 s95, s95, 128\n"
"s_cmp_eq_u32 s95, 896\n"
"s_cselect_b32 s95, 0, s95\n"
"ds_read_b128 a[32:35], v232 offset:0\n"
"v_readfirstlane_b32 s90, v236\n"
"v_mfma_f32_16x16x32_bf16 v[180:183], a[236:239], a[20:23], v[180:183]\n"
"ds_read_b128 a[36:39], v232 offset:2048\n"
"v_readfirstlane_b32 s91, v237\n"
"v_mfma_f32_16x16x32_bf16 v[172:175], a[236:239], a[16:19], 0\n"
"buffer_load_dwordx4 v225, s[36:39], s82 offen offset:1024 lds\n"
"v_mfma_f32_16x16x32_bf16 v[172:175], a[232:235], a[0:3], v[172:175]\n"
"s_addk_i32 m0, 0x800\n"
"s_add_u32 s82, s95, s68\n"
"ds_read_b128 a[48:51], v233 offset:0\n"
"v_mfma_f32_16x16x32_bf16 v[200:203], a[240:243], a[0:3], 0\n"
"s_mul_i32 s17, s90, 0x9e3779b1\n"
"ds_read_b128 a[52:55], v233 offset:2048\n"
"v_mfma_f32_16x16x32_bf16 v[200:203], a[244:247], a[16:19], v[200:203]\n"
"PG1_%=:\n"
"s_cbranch_execz PGB0_%=\n"
"s_or_b32 s17, s17, 1\n"
"buffer_load_dwordx4 v224, s[36:39], s83 offen offset:0 lds\n"
"v_mfma_f32_16x16x32_bf16 v[208:211], a[244:247], a[20:23], 0\n"
"s_add_u32 s91, s91, s17\n"
"ds_read_b128 a[40:43], v232 offset:16608\n"
"v_mfma_f32_16x16x32_bf16 v[208:211], a[240:243], a[4:7], v[208:211]\n"
"s_mul_i32 s90, s90, 0x85ebca6b\n"
"ds_read_b64_tr_b16 a[224:225], v230 offset:0\n"
"v_mfma_f32_16x16x32_bf16 v[216:219], a[240:243], a[8:11], 0\n"
"s_add_u32 s90, s90, s91\n"
"buffer_load_dwordx4 v225, s[36:39], s83 offen offset:1024 lds\n"
"v_mfma_f32_16x16x32_bf16 v[216:219], a[244:247], a[24:27], v[216:219]\n"
"s_add_u32 m0, s79, 0x0\n"
"s_add_u32 s83, s95, s69\n"
"ds_read_b64_tr_b16 a[226:227], v230 offset:256\n"
"v_mfma_f32_16x16x32_bf16 v[220:223], a[252:255], a[24:27], 0\n"
"s_and_b32 s90, s90, -16\n"
"ds_read_b64_tr_b16 a[228:229], v230 offset:512\n"
"v_mfma_f32_16x16x32_bf16 v[220:223], a[248:251], a[8:11], v[220:223]\n"
"s_add_u32 s17, s90, 1\n"
"buffer_load_dwordx4 v226, s[40:43], s81 offen offset:0 lds\n"
"v_mfma_f32_16x16x32_bf16 v[212:215], a[248:251], a[4:7], 0\n"
"ds_read_b64_tr_b16 a[230:231], v230 offset:768\n"
"v_mfma_f32_16x16x32_bf16 v[212:215], a[252:255], a[20:23], v[212:215]\n"
"ds_read_b128 a[44:47], v232 offset:18656\n"
"v_mfma_f32_16x16x32_bf16 v[204:207], a[252:255], a[16:19], 0\n"
"buffer_load_dwordx4 v227, s[40:43], s81 offen offset:1024 lds\n"
"v_mfma_f32_16x16x32_bf16 v[204:207], a[248:251], a[0:3], v[204:207]\n"
"s_waitcnt lgkmcnt(3)\n"
"s_addk_i32 m0, 0x808\n"
"ds_read_b128 a[56:59], v233 offset:16608\n"
"v_mfma_f32_16x16x32_bf16 v[0:3], a[224:227], a[32:35], v[0:3]\n"
"s_waitcnt lgkmcnt(2)\n"
"ds_read_b128 a[60:63], v233 offset:18656\n"
"v_mfma_f32_16x16x32_bf16 v[0:3], a[228:231], a[48:51], v[0:3]\n"
"buffer_load_dwordx4 v228, s[40:43], s81 offen offset:0 lds\n"
"v_mfma_f32_16x16x32_bf16 v[8:11], a[228:231], a[52:55], v[8:11]\n"
"ds_read_b64_tr_b16 a[236:237], v230 offset:520\n"
"v_mfma_f32_16x16x32_bf16 v[8:11], a[224:227], a[36:39], v[8:11]\n"
"ds_read_b64_tr_b16 a[232:233], v230 offset:8\n"
"v_mfma_f32_16x16x32_bf16 v[16:19], a[224:227], a[40:43], v[16:19]\n"
"s_waitcnt lgkmcnt(3)\n"
"buffer_load_dwordx4 v229, s[40:43], s81 offen offset:1024 lds\n"
"v_mfma_f32_16x16x32_bf16 v[16:19], a[228:231], a[56:59], v[16:19]\n"
"s_waitcnt lgkmcnt(2)\n"
"s_add_u32 m0, s79, 0x40e0\n"
"ds_read_b64_tr_b16 a[234:235], v230 offset:264\n"
"v_mfma_f32_16x16x32_bf16 v[24:27], a[228:231], a[60:63], v[24:27]\n"
"s_lshl_b32 s81, s96, 12\n"
"ds_read_b64_tr_b16 a[238:239], v230 offset:776\n"
"v_mfma_f32_16x16x32_bf16 v[24:27], a[224:227], a[44:47], v[24:27]\n"
"s_waitcnt lgkmcnt(1)\n"
"buffer_load_dwordx4 v226, s[40:43], s80 offen offset:0 lds\n"
"v_mfma_f32_16x16x32_bf16 v[28:31], a[232:235], a[44:47], v[28:31]\n"
"s_waitcnt lgkmcnt(0)\n"
"s_add_u32 s81, s81, 0x100\n"
"ds_read_b64_tr_b16 a[240:241], v231 offset:0\n"
"v_mfma_f32_16x16x32_bf16 v[28:31], a[236:239], a[60:63], v[28:31]\n"
"s_add_u32 s96, s96, 128\n"
"s_cmp_eq_u32 s96, 896\n"
"s_cselect_b32 s96, 0, s96\n"
"ds_read_b64_tr_b16 a[242:243], v231 offset:256\n"
"v_mfma_f32_16x16x32_bf16 v[20:23], a[236:239], a[56:59], v[20:23]\n"
"buffer_load_dwordx4 v227, s[40:43], s80 offen offset:1024 lds\n"
"v_mfma_f32_16x16x32_bf16 v[20:23], a[232:235], a[40:43], v[20:23]\n"
"s_addk_i32 m0, 0x808\n"
"ds_read_b64_tr_b16 a[244:245], v231 offset:512\n"
"v_mfma_f32_16x16x32_bf16 v[12:15], a[232:235], a[36:39], v[12:15]\n"
"ds_read_b64_tr_b16 a[246:247], v231 offset:768\n"
"v_mfma_f32_16x16x32_bf16 v[12:15], a[236:239], a[52:55], v[12:15]\n"
"buffer_load_dwordx4 v228, s[40:43], s80 offen offset:0 lds\n"
"v_mfma_f32_16x16x32_bf16 v[4:7], a[236:239], a[48:51], v[4:7]\n"
"ds_read_b64_tr_b16 a[252:253], v231 offset:520\n"
"v_mfma_f32_16x16x32_bf16 v[4:7], a[232:235], a[32:35], v[4:7]\n"
"s_waitcnt lgkmcnt(3)\n"
"ds_read_b64_tr_b16 a[254:255], v231 offset:776\n"
"v_mfma_f32_16x16x32_bf16 v[32:35], a[240:243], a[32:35], v[32:35]\n"
"s_waitcnt lgkmcnt(2)\n"
"ds_read_b64_tr_b16 a[248:249], v231 offset:8\n"
"v_mfma_f32_16x16x32_bf16 v[32:35], a[244:247], a[48:51], v[32:35]\n"
"ds_read_b64_tr_b16 a[250:251], v231 offset:264\n"
"v_mfma_f32_16x16x32_bf16 v[40:43], a[244:247], a[52:55], v[40:43]\n"
"ds_read_b64_tr_b16 a[228:229], v230 offset:640\n"
"v_mfma_f32_16x16x32_bf16 v[40:43], a[240:243], a[36:39], v[40:43]\n"
"buffer_load_dwordx4 v229, s[40:43], s80 offen offset:1024 lds\n"
"v_mfma_f32_16x16x32_bf16 v[48:51], a[240:243], a[40:43], v[48:51]\n"
"s_add_u32 m0, s79, 0x81c0\n"
"s_lshl_b32 s80, s96, 12\n"
"ds_read_b64_tr_b16 a[224:225], v230 offset:128\n"
"v_mfma_f32_16x16x32_bf16 v[48:51], a[244:247], a[56:59], v[48:51]\n"
"s_waitcnt lgkmcnt(4)\n"
"ds_read_b64_tr_b16 a[226:227], v230 offset:384\n"
"v_mfma_f32_16x16x32_bf16 v[52:55], a[252:255], a[56:59], v[52:55]\n"
"s_waitcnt lgkmcnt(3)\n"
"ds_read_b64_tr_b16 a[230:231], v230 offset:896\n"
"v_mfma_f32_16x16x32_bf16 v[52:55], a[248:251], a[40:43], v[52:55]\n"
"ds_read_b64_tr_b16 a[236:237], v230 offset:648\n"
"v_mfma_f32_16x16x32_bf16 v[44:47], a[248:251], a[36:39], v[44:47]\n"
"ds_read_b64_tr_b16 a[238:239], v230 offset:904\n"
"v_mfma_f32_16x16x32_bf16 v[44:47], a[252:255], a[52:55], v[44:47]\n"
"ds_read_b64_tr_b16 a[232:233], v230 offset:136\n"
"v_mfma_f32_16x16x32_bf16 v[36:39], a[252:255], a[48:51], v[36:39]\n"
"ds_read_b64_tr_b16 a[234:235], v230 offset:392\n"
"v_add_u32 v230, 0xfffe7ac0, v230\n"
"v_mfma_f32_16x16x32_bf16 v[36:39], a[248:251], a[32:35], v[36:39]\n"
"s_waitcnt lgkmcnt(5)\n"
"ds_read_b64_tr_b16 a[240:241], v231 offset:128\n"
"v_mfma_f32_16x16x32_bf16 v[56:59], a[224:227], a[32:35], v[56:59]\n"
"s_waitcnt lgkmcnt(5)\n"
"buffer_load_dwordx4 v224, s[36:39], s82 offen offset:0 lds\n"
"v_mfma_f32_16x16x32_bf16 v[56:59], a[228:231], a[48:51], v[56:59]\n"
"ds_read_b64_tr_b16 a[242:243], v231 offset:384\n"
"v_mfma_f32_16x16x32_bf16 v[64:67], a[228:231], a[52:55], v[64:67]\n"
"ds_read_b64_tr_b16 a[244:245], v231 offset:640\n"
"v_mfma_f32_16x16x32_bf16 v[64:67], a[224:227], a[36:39], v[64:67]\n"
"ds_read_b64_tr_b16 a[246:247], v231 offset:896\n"
"v_mfma_f32_16x16x32_bf16 v[72:75], a[224:227], a[40:43], v[72:75]\n"
"buffer_load_dwordx4 v225, s[36:39], s82 offen offset:1024 lds\n"
"v_mfma_f32_16x16x32_bf16 v[72:75], a[228:231], a[56:59], v[72:75]\n"
"s_addk_i32 m0, 0x800\n"
"s_add_u32 s82, s95, s70\n"
"ds_read_b64_tr_b16 a[252:253], v231 offset:648\n"
"v_mfma_f32_16x16x32_bf16 v[80:83], a[228:231], a[60:63], v[80:83]\n"
"ds_read_b64_tr_b16 a[254:255], v231 offset:904\n"
"v_mfma_f32_16x16x32_bf16 v[80:83], a[224:227], a[44:47], v[80:83]\n"
"s_waitcnt lgkmcnt(6)\n"
"buffer_load_dwordx4 v224, s[36:39], s83 offen offset:0 lds\n"
"v_mfma_f32_16x16x32_bf16 v[84:87], a[232:235], a[44:47], v[84:87]\n"
"ds_read_b64_tr_b16 a[248:249], v231 offset:136\n"
"v_mfma_f32_16x16x32_bf16 v[84:87], a[236:239], a[60:63], v[84:87]\n"
"ds_read_b64_tr_b16 a[250:251], v231 offset:392\n"
"v_add_u32 v231, 0xfffe7ac0, v231\n"
"v_mfma_f32_16x16x32_bf16 v[76:79], a[236:239], a[56:59], v[76:79]\n"
"v_mfma_f32_16x16x32_bf16 v[76:79], a[232:235], a[40:43], v[76:79]\n"
"buffer_load_dwordx4 v225, s[36:39], s83 offen offset:1024 lds\n"
"v_mfma_f32_16x16x32_bf16 v[68:71], a[232:235], a[36:39], v[68:71]\n"
"s_add_u32 m0, s79, 0xc2a0\n"
"s_add_u32 s83, s95, s71\n"
"v_mfma_f32_16x16x32_bf16 v[68:71], a[236:239], a[52:55], v[68:71]\n"
"s_add_u32 s95, s95, 128\n"
"s_cmp_eq_u32 s95, 896\n"
"s_cselect_b32 s95, 0, s95\n"
"v_mfma_f32_16x16x32_bf16 v[60:63], a[236:239], a[48:51], v[60:63]\n"
"buffer_load_dwordx4 v224, s[36:39], s82 offen offset:0 lds\n"
"v_mfma_f32_16x16x32_bf16 v[60:63], a[232:235], a[32:35], v[60:63]\n"
"s_waitcnt vmcnt(9) lgkmcnt(0)\n"
"s_barrier\n"
"v_mfma_f32_16x16x32_bf16 v[88:91], a[240:243], a[32:35], v[88:91]\n"
"s_load_dword s5, s[12:13], 0x4000\n"
"ds_read_b64_tr_b16 a[228:229], v230 offset:512\n"
"v_mfma_f32_16x16x32_bf16 v[88:91], a[244:247], a[48:51], v[88:91]\n"
"ds_read_b64_tr_b16 a[224:225], v230 offset:0\n"
"v_mfma_f32_16x16x32_bf16 v[96:99], a[244:247], a[52:55], v[96:99]\n"
"buffer_load_dwordx4 v225, s[36:39], s82 offen offset:1024 lds\n"
"v_mfma_f32_16x16x32_bf16 v[96:99], a[240:243], a[36:39], v[96:99]\n"
"s_addk_i32 m0, 0x800\n"
"s_add_u32 s82, s95, s68\n"
"ds_read_b64_tr_b16 a[226:227], v230 offset:256\n"
"v_mfma_f32_16x16x32_bf16 v[104:107], a[240:243], a[40:43], v[104:107]\n"
"ds_read_b64_tr_b16 a[230:231], v230 offset:768\n"
"v_mfma_f32_16x16x32_bf16 v[104:107], a[244:247], a[56:59], v[104:107]\n"
"buffer_load_dwordx4 v224, s[36:39], s83 offen offset:0 lds\n"
"v_mfma_f32_16x16x32_bf16 v[108:111], a[252:255], a[56:59], v[108:111]\n"
"ds_read_b128 a[64:67], v232 offset:49824\n"
"v_mfma_f32_16x16x32_bf16 v[108:111], a[248:251], a[40:43], v[108:111]\n"
"ds_read_b128 a[68:71], v232 offset:51872\n"
"v_add_u32 v232, 0x10380, v232\n"
"v_mfma_f32_16x16x32_bf16 v[100:103], a[248:251], a[36:39], v[100:103]\n"
"ds_read_b64_tr_b16 a[236:237], v230 offset:520\n"
"v_mfma_f32_16x16x32_bf16 v[100:103], a[252:255], a[52:55], v[100:103]\n"
"buffer_load_dwordx4 v225, s[36:39], s83 offen offset:1024 lds\n"
"v_mfma_f32_16x16x32_bf16 v[92:95], a[252:255], a[48:51], v[92:95]\n"
"s_add_u32 m0, s79, 0x10380\n"
"s_add_u32 s83, s95, s69\n"
"ds_read_b64_tr_b16 a[232:233], v230 offset:8\n"
"v_mfma_f32_16x16x32_bf16 v[92:95], a[248:251], a[32:35], v[92:95]\n"
"s_waitcnt lgkmcnt(5)\n"
"ds_read_b64_tr_b16 a[234:235], v230 offset:264\n"
"v_mfma_f32_16x16x32_bf16 v[112:115], a[224:227], a[32:35], v[112:115]\n"
"s_waitcnt lgkmcnt(5)\n"
"buffer_load_dwordx4 v226, s[40:43], s81 offen offset:0 lds\n"
"v_mfma_f32_16x16x32_bf16 v[112:115], a[228:231], a[48:51], v[112:115]\n"
"s_add_u32 s14, s14, s5\n"
"ds_read_b64_tr_b16 a[238:239], v230 offset:776\n"
"v_mfma_f32_16x16x32_bf16 v[120:123], a[228:231], a[52:55], v[120:123]\n"
"s_add_u32 s14, s14, s5\n"
"ds_read_b128 a[80:83], v233 offset:49824\n"
"v_mfma_f32_16x16x32_bf16 v[120:123], a[224:227], a[36:39], v[120:123]\n"
"s_add_u32 s14, s14, s5\n"
"ds_read_b128 a[84:87], v233 offset:51872\n"
"v_add_u32 v233, 0x10380, v233\n"
"v_mfma_f32_16x16x32_bf16 v[128:131], a[224:227], a[40:43], v[128:131]\n"
"s_add_u32 s14, s14, s5\n"
"ds_read_b128 a[72:75], v232 offset:0\n"
"v_mfma_f32_16x16x32_bf16 v[128:131], a[228:231], a[56:59], v[128:131]\n"
"s_add_u32 s14, s14, s5\n"
"ds_read_b64_tr_b16 a[240:241], v231 offset:0\n"
"v_mfma_f32_16x16x32_bf16 v[136:139], a[228:231], a[60:63], v[136:139]\n"
"s_add_u32 s14, s14, s5\n"
"buffer_load_dwordx4 v227, s[40:43], s81 offen offset:1024 lds\n"
"v_mfma_f32_16x16x32_bf16 v[136:139], a[224:227], a[44:47], v[136:139]\n"
"s_waitcnt lgkmcnt(5)\n"
"s_add_u32 s14, s14, s5\n"
"ds_read_b64_tr_b16 a[242:243], v231 offset:256\n"
"v_mfma_f32_16x16x32_bf16 v[140:143], a[232:235], a[44:47], v[140:143]\n"
"s_waitcnt lgkmcnt(5)\n"
"s_add_u32 s14, s14, s5\n"
"ds_read_b64_tr_b16 a[244:245], v231 offset:512\n"
"v_mfma_f32_16x16x32_bf16 v[140:143], a[236:239], a[60:63], v[140:143]\n"
"s_add_u32 s14, s14, s5\n"
"s_addk_i32 m0, 0x808\n"
"ds_read_b64_tr_b16 a[246:247], v231 offset:768\n"
"v_mfma_f32_16x16x32_bf16 v[132:135], a[236:239], a[56:59], v[132:135]\n"
"s_add_u32 s14, s14, s5\n"
"ds_read_b128 a[76:79], v232 offset:2048\n"
"v_add_u32 v232, 0xfffe7ac0, v232\n"
"v_mfma_f32_16x16x32_bf16 v[132:135], a[232:235], a[40:43], v[132:135]\n"
"s_add_u32 s14, s14, s5\n"
"ds_read_b64_tr_b16 a[252:253], v231 offset:520\n"
"v_mfma_f32_16x16x32_bf16 v[124:127], a[232:235], a[36:39], v[124:127]\n"
"s_add_u32 s14, s14, s5\n"
"ds_read_b64_tr_b16 a[254:255], v231 offset:776\n"
"v_mfma_f32_16x16x32_bf16 v[124:127], a[236:239], a[52:55], v[124:127]\n"
"s_add_u32 s14, s14, s5\n"
"ds_read_b64_tr_b16 a[248:249], v231 offset:8\n"
"v_mfma_f32_16x16x32_bf16 v[116:119], a[236:239], a[48:51], v[116:119]\n"
"s_add_u32 s14, s14, s5\n"
"buffer_load_dwordx4 v228, s[40:43], s81 offen offset:0 lds\n"
"v_mfma_f32_16x16x32_bf16 v[116:119], a[232:235], a[32:35], v[116:119]\n"
"s_waitcnt lgkmcnt(6)\n"
"s_add_u32 s14, s14, s5\n"
"ds_read_b64_tr_b16 a[250:251], v231 offset:264\n"
"v_mfma_f32_16x16x32_bf16 v[144:147], a[240:243], a[32:35], v[144:147]\n"
"s_waitcnt lgkmcnt(5)\n"
"s_add_u32 s14, s14, s5\n"
"ds_read_b128 a[88:91], v233 offset:0\n"
"v_mfma_f32_16x16x32_bf16 v[144:147], a[244:247], a[48:51], v[144:147]\n"
"s_add_u32 s14, s14, s5\n"
"ds_read_b64_tr_b16 a[228:229], v230 offset:640\n"
"v_mfma_f32_16x16x32_bf16 v[152:155], a[244:247], a[52:55], v[152:155]\n"
"s_add_u32 s14, s14, s5\n"
"ds_read_b64_tr_b16 a[224:225], v230 offset:128\n"
"v_mfma_f32_16x16x32_bf16 v[152:155], a[240:243], a[36:39], v[152:155]\n"
"s_add_u32 s14, s14, s5\n"
"ds_read_b64_tr_b16 a[226:227], v230 offset:384\n"
"v_mfma_f32_16x16x32_bf16 v[160:163], a[240:243], a[40:43], v[160:163]\n"
"s_add_u32 s14, s14, s5\n"
"ds_read_b64_tr_b16 a[230:231], v230 offset:896\n"
"v_mfma_f32_16x16x32_bf16 v[160:163], a[244:247], a[56:59], v[160:163]\n"
"s_waitcnt lgkmcnt(7)\n"
"buffer_load_dwordx4 v229, s[40:43], s81 offen offset:1024 lds\n"
"v_mfma_f32_16x16x32_bf16 v[164:167], a[252:255], a[56:59], v[164:167]\n"
"s_waitcnt lgkmcnt(5)\n"
"s_add_u32 s14, s14, s5\n"
"ds_read_b128 a[92:95], v233 offset:2048\n"
"v_mfma_f32_16x16x32_bf16 v[164:167], a[248:251], a[40:43], v[164:167]\n"
"s_add_u32 s14, s14, s5\n"
"s_add_u32 m0, s79, 0x14460\n"
"ds_read_b64_tr_b16 a[236:237], v230 offset:648\n"
"v_mfma_f32_16x16x32_bf16 v[156:159], a[248:251], a[36:39], v[156:159]\n"
"s_add_u32 s14, s14, s5\n"
"s_lshl_b32 s81, s96, 12\n"
"ds_read_b64_tr_b16 a[238:239], v230 offset:904\n"
"v_mfma_f32_16x16x32_bf16 v[156:159], a[252:255], a[52:55], v[156:159]\n"
"s_add_u32 s14, s14, s5\n"
"s_add_u32 s81, s81, 0x100\n"
"ds_read_b64_tr_b16 a[232:233], v230 offset:136\n"
"v_mfma_f32_16x16x32_bf16 v[148:151], a[252:255], a[48:51], v[148:151]\n"
"s_add_u32 s96, s96, 128\n"
"s_cmp_eq_u32 s96, 896\n"
"s_cselect_b32 s96, 0, s96\n"
"ds_read_b64_tr_b16 a[234:235], v230 offset:392\n"
"v_add_u32 v233, 0xfffe7ac0, v233\n"
"v_mfma_f32_16x16x32_bf16 v[148:151], a[248:251], a[32:35], v[148:151]\n"
"s_waitcnt lgkmcnt(6)\n"
"ds_read_b64_tr_b16 a[240:241], v231 offset:128\n"
"v_mfma_f32_16x16x32_bf16 v[168:171], a[224:227], a[32:35], v[168:171]\n"
"s_waitcnt lgkmcnt(6)\n"
"buffer_load_dwordx4 v224, s[36:39], s82 offen offset:0 lds\n"
"v_mfma_f32_16x16x32_bf16 v[168:171], a[228:231], a[48:51], v[168:171]\n"
"ds_read_b64_tr_b16 a[242:243], v231 offset:384\n"
"v_mfma_f32_16x16x32_bf16 v[176:179], a[228:231], a[52:55], v[176:179]\n"
"ds_read_b64_tr_b16 a[244:245], v231 offset:640\n"
"v_mfma_f32_16x16x32_bf16 v[176:179], a[224:227], a[36:39], v[176:179]\n"
"ds_read_b64_tr_b16 a[246:247], v231 offset:896\n"
"v_mfma_f32_16x16x32_bf16 v[184:187], a[224:227], a[40:43], v[184:187]\n"
"buffer_load_dwordx4 v225, s[36:39], s82 offen offset:1024 lds\n"
"v_mfma_f32_16x16x32_bf16 v[184:187], a[228:231], a[56:59], v[184:187]\n"
"s_addk_i32 m0, 0x800\n"
"s_add_u32 s82, s95, s70\n"
"ds_read_b64_tr_b16 a[252:253], v231 offset:648\n"
"v_mfma_f32_16x16x32_bf16 v[192:195], a[228:231], a[60:63], v[192:195]\n"
"ds_read_b64_tr_b16 a[254:255], v231 offset:904\n"
"v_mfma_f32_16x16x32_bf16 v[192:195], a[224:227], a[44:47], v[192:195]\n"
"s_waitcnt lgkmcnt(6)\n"
"buffer_load_dwordx4 v224, s[36:39], s83 offen offset:0 lds\n"
"v_mfma_f32_16x16x32_bf16 v[196:199], a[232:235], a[44:47], v[196:199]\n"
"ds_read_b64_tr_b16 a[248:249], v231 offset:136\n"
"v_mfma_f32_16x16x32_bf16 v[196:199], a[236:239], a[60:63], v[196:199]\n"
"ds_read_b64_tr_b16 a[250:251], v231 offset:392\n"
"v_mfma_f32_16x16x32_bf16 v[188:191], a[236:239], a[56:59], v[188:191]\n"
"v_mfma_f32_16x16x32_bf16 v[188:191], a[232:235], a[40:43], v[188:191]\n"
"buffer_load_dwordx4 v225, s[36:39], s83 offen offset:1024 lds\n"
"v_mfma_f32_16x16x32_bf16 v[180:183], a[232:235], a[36:39], v[180:183]\n"
"s_add_u32 m0, s79, 0x18540\n"
"s_add_u32 s83, s95, s71\n"
"v_mfma_f32_16x16x32_bf16 v[180:183], a[236:239], a[52:55], v[180:183]\n"
"s_add_u32 s95, s95, 128\n"
"s_cmp_eq_u32 s95, 896\n"
"s_cselect_b32 s95, 0, s95\n"
"v_mfma_f32_16x16x32_bf16 v[172:175], a[236:239], a[48:51], v[172:175]\n"
"buffer_load_dwordx4 v224, s[36:39], s82 offen offset:0 lds\n"
"v_mfma_f32_16x16x32_bf16 v[172:175], a[232:235], a[32:35], v[172:175]\n"
"s_waitcnt vmcnt(17) lgkmcnt(0)\n"
"s_barrier\n"
"v_mfma_f32_16x16x32_bf16 v[200:203], a[240:243], a[32:35], v[200:203]\n"
"s_load_dword s6, s[12:13], 0x6000\n"
"ds_read_b64_tr_b16 a[228:229], v230 offset:17120\n"
"v_mfma_f32_16x16x32_bf16 v[200:203], a[244:247], a[48:51], v[200:203]\n"
"ds_read_b64_tr_b16 a[224:225], v230 offset:16608\n"
"v_mfma_f32_16x16x32_bf16 v[208:211], a[244:247], a[52:55], v[208:211]\n"
"buffer_load_dwordx4 v225, s[36:39], s82 offen offset:1024 lds\n"
"v_mfma_f32_16x16x32_bf16 v[208:211], a[240:243], a[36:39], v[208:211]\n"
"s_addk_i32 m0, 0x800\n"
"s_add_u32 s82, s95, s68\n"
"ds_read_b64_tr_b16 a[226:227], v230 offset:16864\n"
"v_mfma_f32_16x16x32_bf16 v[216:219], a[240:243], a[40:43], v[216:219]\n"
"ds_read_b64_tr_b16 a[230:231], v230 offset:17376\n"
"v_mfma_f32_16x16x32_bf16 v[216:219], a[244:247], a[56:59], v[216:219]\n"
"buffer_load_dwordx4 v224, s[36:39], s83 offen offset:0 lds\n"
"v_mfma_f32_16x16x32_bf16 v[220:223], a[252:255], a[56:59], v[220:223]\n"
"ds_read_b64_tr_b16 a[236:237], v230 offset:17128\n"
"v_mfma_f32_16x16x32_bf16 v[220:223], a[248:251], a[40:43], v[220:223]\n"
"ds_read_b64_tr_b16 a[232:233], v230 offset:16616\n"
"v_mfma_f32_16x16x32_bf16 v[212:215], a[248:251], a[36:39], v[212:215]\n"
"buffer_load_dwordx4 v225, s[36:39], s83 offen offset:1024 lds\n"
"v_mfma_f32_16x16x32_bf16 v[212:215], a[252:255], a[52:55], v[212:215]\n"
"s_add_u32 m0, s79, 0x0\n"
"s_add_u32 s83, s95, s69\n"
"ds_read_b64_tr_b16 a[234:235], v230 offset:16872\n"
"v_mfma_f32_16x16x32_bf16 v[204:207], a[252:255], a[48:51], v[204:207]\n"
"ds_read_b64_tr_b16 a[238:239], v230 offset:17384\n"
"v_mfma_f32_16x16x32_bf16 v[204:207], a[248:251], a[32:35], v[204:207]\n"
"s_waitcnt lgkmcnt(5)\n"
"buffer_load_dwordx4 v226, s[40:43], s80 offen offset:0 lds\n"
"v_mfma_f32_16x16x32_bf16 v[0:3], a[224:227], a[64:67], v[0:3]\n"
"s_waitcnt lgkmcnt(4)\n"
"s_add_u32 s15, s15, s6\n"
"ds_read_b64_tr_b16 a[240:241], v231 offset:16608\n"
"v_mfma_f32_16x16x32_bf16 v[0:3], a[228:231], a[80:83], v[0:3]\n"
"s_add_u32 s15, s15, s6\n"
"ds_read_b64_tr_b16 a[242:243], v231 offset:16864\n"
"v_mfma_f32_16x16x32_bf16 v[8:11], a[228:231], a[84:87], v[8:11]\n"
"s_add_u32 s15, s15, s6\n"
"ds_read_b64_tr_b16 a[244:245], v231 offset:17120\n"
"v_mfma_f32_16x16x32_bf16 v[8:11], a[224:227], a[68:71], v[8:11]\n"
"s_add_u32 s15, s15, s6\n"
"ds_read_b64_tr_b16 a[246:247], v231 offset:17376\n"
"v_mfma_f32_16x16x32_bf16 v[16:19], a[224:227], a[72:75], v[16:19]\n"
"s_add_u32 s15, s15, s6\n"
"ds_read_b64_tr_b16 a[252:253], v231 offset:17128\n"
"v_mfma_f32_16x16x32_bf16 v[16:19], a[228:231], a[88:91], v[16:19]\n"
"s_add_u32 s15, s15, s6\n"
"ds_read_b64_tr_b16 a[254:255], v231 offset:17384\n"
"v_mfma_f32_16x16x32_bf16 v[24:27], a[228:231], a[92:95], v[24:27]\n"
"s_add_u32 s15, s15, s6\n"
"ds_read_b64_tr_b16 a[248:249], v231 offset:16616\n"
"v_mfma_f32_16x16x32_bf16 v[24:27], a[224:227], a[76:79], v[24:27]\n"
"s_waitcnt lgkmcnt(8)\n"
"s_add_u32 s15, s15, s6\n"
"ds_read_b64_tr_b16 a[250:251], v231 offset:16872\n"
"v_mfma_f32_16x16x32_bf16 v[28:31], a[232:235], a[76:79], v[28:31]\n"
"s_waitcnt lgkmcnt(8)\n"
"s_add_u32 s15, s15, s6\n"
"ds_read_b64_tr_b16 a[228:229], v230 offset:17248\n"
"v_mfma_f32_16x16x32_bf16 v[28:31], a[236:239], a[92:95], v[28:31]\n"
"s_add_u32 s15, s15, s6\n"
"ds_read_b64_tr_b16 a[230:231], v230 offset:17504\n"
"v_mfma_f32_16x16x32_bf16 v[20:23], a[236:239], a[88:91], v[20:23]\n"
"s_add_u32 s15, s15, s6\n"
"buffer_load_dwordx4 v227, s[40:43], s80 offen offset:1024 lds\n"
"v_mfma_f32_16x16x32_bf16 v[20:23], a[232:235], a[72:75], v[20:23]\n"
"s_add_u32 s15, s15, s6\n"
"s_addk_i32 m0, 0x808\n"
"ds_read_b64_tr_b16 a[224:225], v230 offset:16736\n"
"v_mfma_f32_16x16x32_bf16 v[12:15], a[232:235], a[68:71], v[12:15]\n"
"s_add_u32 s15, s15, s6\n"
"ds_read_b64_tr_b16 a[226:227], v230 offset:16992\n"
"v_mfma_f32_16x16x32_bf16 v[12:15], a[236:239], a[84:87], v[12:15]\n"
"s_add_u32 s15, s15, s6\n"
"v_mfma_f32_16x16x32_bf16 v[4:7], a[236:239], a[80:83], v[4:7]\n"
"s_add_u32 s15, s15, s6\n"
"ds_read_b64_tr_b16 a[236:237], v230 offset:17256\n"
"v_mfma_f32_16x16x32_bf16 v[4:7], a[232:235], a[64:67], v[4:7]\n"
"s_waitcnt lgkmcnt(9)\n"
"s_add_u32 s15, s15, s6\n"
"ds_read_b64_tr_b16 a[232:233], v230 offset:16744\n"
"v_mfma_f32_16x16x32_bf16 v[32:35], a[240:243], a[64:67], v[32:35]\n"
"s_add_u32 s15, s15, s6\n"
"ds_read_b64_tr_b16 a[234:235], v230 offset:17000\n"
"v_mfma_f32_16x16x32_bf16 v[32:35], a[244:247], a[80:83], v[32:35]\n"
"s_add_u32 s15, s15, s6\n"
"ds_read_b64_tr_b16 a[238:239], v230 offset:17512\n"
"v_add_u32 v230, 0x10380, v230\n"
"v_mfma_f32_16x16x32_bf16 v[40:43], a[244:247], a[84:87], v[40:43]\n"
"s_add_u32 s15, s15, s6\n"
"v_mfma_f32_16x16x32_bf16 v[40:43], a[240:243], a[68:71], v[40:43]\n"
"s_add_u32 s15, s15, s6\n"
"v_mfma_f32_16x16x32_bf16 v[48:51], a[240:243], a[72:75], v[48:51]\n"
"s_add_u32 s15, s15, s6\n"
"ds_read_b64_tr_b16 a[240:241], v231 offset:16736\n"
"v_mfma_f32_16x16x32_bf16 v[48:51], a[244:247], a[88:91], v[48:51]\n"
"s_waitcnt lgkmcnt(7)\n"
"s_add_u32 s15, s15, s6\n"
"ds_read_b64_tr_b16 a[242:243], v231 offset:16992\n"
"v_mfma_f32_16x16x32_bf16 v[52:55], a[252:255], a[88:91], v[52:55]\n"
"s_add_u32 s15, s15, s6\n"
"buffer_load_dwordx4 v228, s[40:43], s80 offen offset:0 lds\n"
"v_mfma_f32_16x16x32_bf16 v[52:55], a[248:251], a[72:75], v[52:55]\n"
"s_add_u32 s15, s15, s6\n"
"ds_read_b64_tr_b16 a[244:245], v231 offset:17248\n"
"v_mfma_f32_16x16x32_bf16 v[44:47], a[248:251], a[68:71], v[44:47]\n"
"ds_read_b64_tr_b16 a[246:247], v231 offset:17504\n"
"v_mfma_f32_16x16x32_bf16 v[44:47], a[252:255], a[84:87], v[44:47]\n"
"v_mfma_f32_16x16x32_bf16 v[36:39], a[252:255], a[80:83], v[36:39]\n"
"ds_read_b64_tr_b16 a[252:253], v231 offset:17256\n"
"v_mfma_f32_16x16x32_bf16 v[36:39], a[248:251], a[64:67], v[36:39]\n"
"s_waitcnt lgkmcnt(7)\n"
"ds_read_b64_tr_b16 a[254:255], v231 offset:17512\n"
"v_mfma_f32_16x16x32_bf16 v[56:59], a[224:227], a[64:67], v[56:59]\n"
"ds_read_b64_tr_b16 a[248:249], v231 offset:16744\n"
"v_mfma_f32_16x16x32_bf16 v[56:59], a[228:231], a[80:83], v[56:59]\n"
"ds_read_b64_tr_b16 a[250:251], v231 offset:17000\n"
"v_add_u32 v231, 0x10380, v231\n"
"v_mfma_f32_16x16x32_bf16 v[64:67], a[228:231], a[84:87], v[64:67]\n"
"v_mfma_f32_16x16x32_bf16 v[64:67], a[224:227], a[68:71], v[64:67]\n"
"v_mfma_f32_16x16x32_bf16 v[72:75], a[224:227], a[72:75], v[72:75]\n"
"v_mfma_f32_16x16x32_bf16 v[72:75], a[228:231], a[88:91], v[72:75]\n"
"v_mfma_f32_16x16x32_bf16 v[80:83], a[228:231], a[92:95], v[80:83]\n"
"buffer_load_dwordx4 v229, s[40:43], s80 offen offset:1024 lds\n"
"v_mfma_f32_16x16x32_bf16 v[80:83], a[224:227], a[76:79], v[80:83]\n"
"s_waitcnt lgkmcnt(5)\n"
"s_add_u32 m0, s79, 0x1c620\n"
"s_lshl_b32 s80, s96, 12\n"
"v_mfma_f32_16x16x32_bf16 v[84:87], a[232:235], a[76:79], v[84:87]\n"
"s_waitcnt vmcnt(12) lgkmcnt(0)\n"
"s_barrier\n"
"v_mfma_f32_16x16x32_bf16 v[84:87], a[236:239], a[92:95], v[84:87]\n"
"s_load_dword s7, s[12:13], 0xa000\n"
"ds_read_b64_tr_b16 a[228:229], v230 offset:512\n"
"v_mfma_f32_16x16x32_bf16 v[76:79], a[236:239], a[88:91], v[76:79]\n"
"ds_read_b64_tr_b16 a[224:225], v230 offset:0\n"
"v_mfma_f32_16x16x32_bf16 v[76:79], a[232:235], a[72:75], v[76:79]\n"
"ds_read_b64_tr_b16 a[226:227], v230 offset:256\n"
"v_mfma_f32_16x16x32_bf16 v[68:71], a[232:235], a[68:71], v[68:71]\n"
"ds_read_b64_tr_b16 a[230:231], v230 offset:768\n"
"v_mfma_f32_16x16x32_bf16 v[68:71], a[236:239], a[84:87], v[68:71]\n"
"ds_read_b128 a[96:99], v232 offset:0\n"
"v_mfma_f32_16x16x32_bf16 v[60:63], a[236:239], a[80:83], v[60:63]\n"
"ds_read_b64_tr_b16 a[236:237], v230 offset:520\n"
"v_mfma_f32_16x16x32_bf16 v[60:63], a[232:235], a[64:67], v[60:63]\n"
"ds_read_b64_tr_b16 a[232:233], v230 offset:8\n"
"v_mfma_f32_16x16x32_bf16 v[88:91], a[240:243], a[64:67], v[88:91]\n"
"ds_read_b64_tr_b16 a[234:235], v230 offset:264\n"
"v_mfma_f32_16x16x32_bf16 v[88:91], a[244:247], a[80:83], v[88:91]\n"
"buffer_load_dwordx4 v226, s[40:43], s81 offen offset:0 lds\n"
"v_mfma_f32_16x16x32_bf16 v[96:99], a[244:247], a[84:87], v[96:99]\n"
"ds_read_b64_tr_b16 a[238:239], v230 offset:776\n"
"v_mfma_f32_16x16x32_bf16 v[96:99], a[240:243], a[68:71], v[96:99]\n"
"ds_read_b128 a[100:103], v232 offset:2048\n"
"v_mfma_f32_16x16x32_bf16 v[104:107], a[240:243], a[72:75], v[104:107]\n"
"ds_read_b128 a[112:115], v233 offset:0\n"
"v_mfma_f32_16x16x32_bf16 v[104:107], a[244:247], a[88:91], v[104:107]\n"
"s_add_u32 s15, s15, s7\n"
"ds_read_b128 a[116:119], v233 offset:2048\n"
"v_mfma_f32_16x16x32_bf16 v[108:111], a[252:255], a[88:91], v[108:111]\n"
"s_add_u32 s15, s15, s7\n"
"ds_read_b64_tr_b16 a[240:241], v231 offset:0\n"
"v_mfma_f32_16x16x32_bf16 v[108:111], a[248:251], a[72:75], v[108:111]\n"
"s_add_u32 s15, s15, s7\n"
"ds_read_b64_tr_b16 a[242:243], v231 offset:256\n"
"v_mfma_f32_16x16x32_bf16 v[100:103], a[248:251], a[68:71], v[100:103]\n"
"s_add_u32 s15, s15, s7\n"
"ds_read_b64_tr_b16 a[244:245], v231 offset:512\n"
"v_mfma_f32_16x16x32_bf16 v[100:103], a[252:255], a[84:87], v[100:103]\n"
"s_add_u32 s15, s15, s7\n"
"ds_read_b64_tr_b16 a[246:247], v231 offset:768\n"
"v_mfma_f32_16x16x32_bf16 v[92:95], a[252:255], a[80:83], v[92:95]\n"
"s_add_u32 s15, s15, s7\n"
"ds_read_b128 a[104:107], v232 offset:16608\n"
"v_mfma_f32_16x16x32_bf16 v[92:95], a[248:251], a[64:67], v[92:95]\n"
"s_waitcnt lgkmcnt(10)\n"
"buffer_load_dwordx4 v227, s[40:43], s81 offen offset:1024 lds\n"
"v_mfma_f32_16x16x32_bf16 v[112:115], a[224:227], a[64:67], v[112:115]\n"
"s_add_u32 s15, s15, s7\n"
"s_addk_i32 m0, 0x808\n"
"ds_read_b128 a[108:111], v232 offset:18656\n"
"v_mfma_f32_16x16x32_bf16 v[112:115], a[228:231], a[80:83], v[112:115]\n"
"s_add_u32 s15, s15, s7\n"
"ds_read_b64_tr_b16 a[252:253], v231 offset:520\n"
"v_mfma_f32_16x16x32_bf16 v[120:123], a[228:231], a[84:87], v[120:123]\n"
"s_add_u32 s15, s15, s7\n"
"ds_read_b64_tr_b16 a[254:255], v231 offset:776\n"
"v_mfma_f32_16x16x32_bf16 v[120:123], a[224:227], a[68:71], v[120:123]\n"
"s_add_u32 s15, s15, s7\n"
"ds_read_b64_tr_b16 a[248:249], v231 offset:8\n"
"v_mfma_f32_16x16x32_bf16 v[128:131], a[224:227], a[72:75], v[128:131]\n"
"s_add_u32 s15, s15, s7\n"
"ds_read_b64_tr_b16 a[250:251], v231 offset:264\n"
"v_mfma_f32_16x16x32_bf16 v[128:131], a[228:231], a[88:91], v[128:131]\n"
"s_add_u32 s15, s15, s7\n"
"ds_read_b128 a[120:123], v233 offset:16608\n"
"v_mfma_f32_16x16x32_bf16 v[136:139], a[228:231], a[92:95], v[136:139]\n"
"s_add_u32 s15, s15, s7\n"
"ds_read_b64_tr_b16 a[228:229], v230 offset:640\n"
"v_mfma_f32_16x16x32_bf16 v[136:139], a[224:227], a[76:79], v[136:139]\n"
"s_waitcnt lgkmcnt(10)\n"
"s_add_u32 s15, s15, s7\n"
"ds_read_b64_tr_b16 a[224:225], v230 offset:128\n"
"v_mfma_f32_16x16x32_bf16 v[140:143], a[232:235], a[76:79], v[140:143]\n"
"s_add_u32 s15, s15, s7\n"
"ds_read_b64_tr_b16 a[226:227], v230 offset:384\n"
"v_mfma_f32_16x16x32_bf16 v[140:143], a[236:239], a[92:95], v[140:143]\n"
"s_add_u32 s15, s15, s7\n"
"buffer_load_dwordx4 v228, s[40:43], s81 offen offset:0 lds\n"
"v_mfma_f32_16x16x32_bf16 v[132:135], a[236:239], a[88:91], v[132:135]\n"
"s_add_u32 s15, s15, s7\n"
"ds_read_b64_tr_b16 a[230:231], v230 offset:896\n"
"v_mfma_f32_16x16x32_bf16 v[132:135], a[232:235], a[72:75], v[132:135]\n"
"s_add_u32 s15, s15, s7\n"
"ds_read_b128 a[124:127], v233 offset:18656\n"
"v_mfma_f32_16x16x32_bf16 v[124:127], a[232:235], a[68:71], v[124:127]\n"
"s_add_u32 s15, s15, s7\n"
"v_mfma_f32_16x16x32_bf16 v[124:127], a[236:239], a[84:87], v[124:127]\n"
"s_add_u32 s15, s15, s7\n"
"v_mfma_f32_16x16x32_bf16 v[116:119], a[236:239], a[80:83], v[116:119]\n"
"s_add_u32 s15, s15, s7\n"
"ds_read_b64_tr_b16 a[236:237], v230 offset:648\n"
"v_mfma_f32_16x16x32_bf16 v[116:119], a[232:235], a[64:67], v[116:119]\n"
"s_add_u32 s15, s15, s7\n"
"ds_read_b64_tr_b16 a[238:239], v230 offset:904\n"
"v_mfma_f32_16x16x32_bf16 v[144:147], a[240:243], a[64:67], v[144:147]\n"
"s_waitcnt lgkmcnt(8)\n"
"s_add_u32 s15, s15, s7\n"
"ds_read_b64_tr_b16 a[232:233], v230 offset:136\n"
"v_mfma_f32_16x16x32_bf16 v[144:147], a[244:247], a[80:83], v[144:147]\n"
"s_add_u32 s15, s15, s7\n"
"ds_read_b64_tr_b16 a[234:235], v230 offset:392\n"
"v_add_u32 v230, 0xfffefc80, v230\n"
"v_mfma_f32_16x16x32_bf16 v[152:155], a[244:247], a[84:87], v[152:155]\n"
"v_mfma_f32_16x16x32_bf16 v[152:155], a[240:243], a[68:71], v[152:155]\n"
"buffer_load_dwordx4 v229, s[40:43], s81 offen offset:1024 lds\n"
"v_mfma_f32_16x16x32_bf16 v[160:163], a[240:243], a[72:75], v[160:163]\n"
"s_add_u32 m0, s79, 0x20700\n"
"s_lshl_b32 s81, s96, 12\n"
"ds_read_b64_tr_b16 a[240:241], v231 offset:128\n"
"v_mfma_f32_16x16x32_bf16 v[160:163], a[244:247], a[88:91], v[160:163]\n"
"s_add_u32 s81, s81, 0x100\n"
"s_add_u32 s96, s96, 128\n"
"s_cmp_eq_u32 s96, 896\n"
"s_cselect_b32 s96, 0, s96\n"
"ds_read_b64_tr_b16 a[242:243], v231 offset:384\n"
"v_mfma_f32_16x16x32_bf16 v[164:167], a[252:255], a[88:91], v[164:167]\n"
"ds_read_b64_tr_b16 a[244:245], v231 offset:640\n"
"v_mfma_f32_16x16x32_bf16 v[164:167], a[248:251], a[72:75], v[164:167]\n"
"ds_read_b64_tr_b16 a[246:247], v231 offset:896\n"
"v_mfma_f32_16x16x32_bf16 v[156:159], a[248:251], a[68:71], v[156:159]\n"
"v_mfma_f32_16x16x32_bf16 v[156:159], a[252:255], a[84:87], v[156:159]\n"
"v_mfma_f32_16x16x32_bf16 v[148:151], a[252:255], a[80:83], v[148:151]\n"
"ds_read_b64_tr_b16 a[252:253], v231 offset:648\n"
"v_mfma_f32_16x16x32_bf16 v[148:151], a[248:251], a[64:67], v[148:151]\n"
"s_waitcnt lgkmcnt(7)\n"
"ds_read_b64_tr_b16 a[254:255], v231 offset:904\n"
"v_mfma_f32_16x16x32_bf16 v[168:171], a[224:227], a[64:67], v[168:171]\n"
"buffer_load_dwordx4 v224, s[36:39], s82 offen offset:0 lds\n"
"v_mfma_f32_16x16x32_bf16 v[168:171], a[228:231], a[80:83], v[168:171]\n"
"ds_read_b64_tr_b16 a[248:249], v231 offset:136\n"
"v_mfma_f32_16x16x32_bf16 v[176:179], a[228:231], a[84:87], v[176:179]\n"
"ds_read_b64_tr_b16 a[250:251], v231 offset:392\n"
"v_add_u32 v231, 0xfffefc80, v231\n"
"v_mfma_f32_16x16x32_bf16 v[176:179], a[224:227], a[68:71], v[176:179]\n"
"v_mfma_f32_16x16x32_bf16 v[184:187], a[224:227], a[72:75], v[184:187]\n"
"buffer_load_dwordx4 v225, s[36:39], s82 offen offset:1024 lds\n"
"v_mfma_f32_16x16x32_bf16 v[184:187], a[228:231], a[88:91], v[184:187]\n"
"s_addk_i32 m0, 0x800\n"
"s_add_u32 s82, s95, s70\n"
"v_mfma_f32_16x16x32_bf16 v[192:195], a[228:231], a[92:95], v[192:195]\n"
"PG2_%=:\n"
"s_cbranch_execz PGB0_%=\n"
"v_mfma_f32_16x16x32_bf16 v[192:195], a[224:227], a[76:79], v[192:195]\n"
"s_waitcnt lgkmcnt(4)\n"
"buffer_load_dwordx4 v224, s[36:39], s83 offen offset:0 lds\n"
"v_mfma_f32_16x16x32_bf16 v[196:199], a[232:235], a[76:79], v[196:199]\n"
"s_waitcnt vmcnt(7) lgkmcnt(0)\n"
"s_barrier\n"
"v_mfma_f32_16x16x32_bf16 v[196:199], a[236:239], a[92:95], v[196:199]\n"
"s_load_dword s8, s[12:13], 0x8000\n"
"ds_read_b64_tr_b16 a[228:229], v230 offset:512\n"
"v_mfma_f32_16x16x32_bf16 v[188:191], a[236:239], a[88:91], v[188:191]\n"
"ds_read_b64_tr_b16 a[224:225], v230 offset:0\n"
"v_mfma_f32_16x16x32_bf16 v[188:191], a[232:235], a[72:75], v[188:191]\n"
"buffer_load_dwordx4 v225, s[36:39], s83 offen offset:1024 lds\n"
"v_mfma_f32_16x16x32_bf16 v[180:183], a[232:235], a[68:71], v[180:183]\n"
"s_add_u32 m0, s79, 0x40e0\n"
"s_add_u32 s83, s95, s71\n"
"ds_read_b64_tr_b16 a[226:227], v230 offset:256\n"
"v_mfma_f32_16x16x32_bf16 v[180:183], a[236:239], a[84:87], v[180:183]\n"
"s_add_u32 s95, s95, 128\n"
"s_cmp_eq_u32 s95, 896\n"
"s_cselect_b32 s95, 0, s95\n"
"ds_read_b64_tr_b16 a[230:231], v230 offset:768\n"
"v_mfma_f32_16x16x32_bf16 v[172:175], a[236:239], a[80:83], v[172:175]\n"
"buffer_load_dwordx4 v224, s[36:39], s82 offen offset:0 lds\n"
"v_mfma_f32_16x16x32_bf16 v[172:175], a[232:235], a[64:67], v[172:175]\n"
"ds_read_b128 a[128:131], v232 offset:49824\n"
"v_mfma_f32_16x16x32_bf16 v[200:203], a[240:243], a[64:67], v[200:203]\n"
"ds_read_b128 a[132:135], v232 offset:51872\n"
"v_add_u32 v232, 0x10380, v232\n"
"v_mfma_f32_16x16x32_bf16 v[200:203], a[244:247], a[80:83], v[200:203]\n"
"ds_read_b64_tr_b16 a[236:237], v230 offset:520\n"
"v_mfma_f32_16x16x32_bf16 v[208:211], a[244:247], a[84:87], v[208:211]\n"
"buffer_load_dwordx4 v225, s[36:39], s82 offen offset:1024 lds\n"
"v_mfma_f32_16x16x32_bf16 v[208:211], a[240:243], a[68:71], v[208:211]\n"
"s_addk_i32 m0, 0x800\n"
"s_add_u32 s82, s95, s68\n"
"ds_read_b64_tr_b16 a[232:233], v230 offset:8\n"
"v_mfma_f32_16x16x32_bf16 v[216:219], a[240:243], a[72:75], v[216:219]\n"
"s_bfe_u32 s68, s72, 0x1000f\n"
"ds_read_b64_tr_b16 a[234:235], v230 offset:264\n"
"v_mfma_f32_16x16x32_bf16 v[216:219], a[244:247], a[88:91], v[216:219]\n"
"s_add_u32 s14, s14, s8\n"
"buffer_load_dwordx4 v224, s[36:39], s83 offen offset:0 lds\n"
"v_mfma_f32_16x16x32_bf16 v[220:223], a[252:255], a[88:91], v[220:223]\n"
"s_add_u32 s14, s14, s8\n"
"s_lshl_b32 s68, s68, 10\n"
"ds_read_b64_tr_b16 a[238:239], v230 offset:776\n"
"v_mfma_f32_16x16x32_bf16 v[220:223], a[248:251], a[72:75], v[220:223]\n"
"s_add_u32 s14, s14, s8\n"
"s_add_u32 s68, s68, 149472\n"
"ds_read_b128 a[144:147], v233 offset:49824\n"
"v_mfma_f32_16x16x32_bf16 v[212:215], a[248:251], a[68:71], v[212:215]\n"
"s_add_u32 s14, s14, s8\n"
"buffer_load_dwordx4 v225, s[36:39], s83 offen offset:1024 lds\n"
"v_mfma_f32_16x16x32_bf16 v[212:215], a[252:255], a[84:87], v[212:215]\n"
"s_add_u32 s14, s14, s8\n"
"s_add_u32 m0, s79, 0x81c0\n"
"ds_read_b128 a[148:151], v233 offset:51872\n"
"v_mfma_f32_16x16x32_bf16 v[204:207], a[252:255], a[80:83], v[204:207]\n"
"s_add_u32 s14, s14, s8\n"
"ds_read_b128 a[136:139], v232 offset:0\n"
"v_add_u32 v233, 0x10380, v233\n"
"v_mfma_f32_16x16x32_bf16 v[204:207], a[248:251], a[64:67], v[204:207]\n"
"s_waitcnt lgkmcnt(8)\n"
"s_add_u32 s14, s14, s8\n"
"ds_read_b64_tr_b16 a[240:241], v231 offset:0\n"
"v_mfma_f32_16x16x32_bf16 v[0:3], a[224:227], a[96:99], v[0:3]\n"
"s_add_u32 s14, s14, s8\n"
"buffer_load_dwordx4 v226, s[40:43], s80 offen offset:0 lds\n"
"v_mfma_f32_16x16x32_bf16 v[0:3], a[228:231], a[112:115], v[0:3]\n"
"s_add_u32 s14, s14, s8\n"
"s_add_u32 s83, s95, s69\n"
"ds_read_b64_tr_b16 a[242:243], v231 offset:256\n"
"v_mfma_f32_16x16x32_bf16 v[8:11], a[228:231], a[116:119], v[8:11]\n"
"s_add_u32 s14, s14, s8\n"
"s_bfe_u32 s69, s73, 0x1000f\n"
"ds_read_b64_tr_b16 a[244:245], v231 offset:512\n"
"v_mfma_f32_16x16x32_bf16 v[8:11], a[224:227], a[100:103], v[8:11]\n"
"s_add_u32 s14, s14, s8\n"
"s_lshl_b32 s69, s69, 10\n"
"ds_read_b64_tr_b16 a[246:247], v231 offset:768\n"
"v_mfma_f32_16x16x32_bf16 v[16:19], a[224:227], a[104:107], v[16:19]\n"
"s_add_u32 s14, s14, s8\n"
"s_add_u32 s69, s69, 149472\n"
"ds_read_b128 a[140:143], v232 offset:2048\n"
"v_mfma_f32_16x16x32_bf16 v[16:19], a[228:231], a[120:123], v[16:19]\n"
"s_add_u32 s14, s14, s8\n"
"ds_read_b64_tr_b16 a[252:253], v231 offset:520\n"
"v_mfma_f32_16x16x32_bf16 v[24:27], a[228:231], a[124:127], v[24:27]\n"
"s_add_u32 s14, s14, s8\n"
"buffer_load_dwordx4 v227, s[40:43], s80 offen offset:1024 lds\n"
"v_mfma_f32_16x16x32_bf16 v[24:27], a[224:227], a[108:111], v[24:27]\n"
"s_waitcnt lgkmcnt(8)\n"
"s_add_u32 s14, s14, s8\n"
"s_addk_i32 m0, 0x808\n"
"v_mfma_f32_16x16x32_bf16 v[28:31], a[232:235], a[108:111], v[28:31]\n"
"s_add_u32 s14, s14, s8\n"
"ds_read_b64_tr_b16 a[254:255], v231 offset:776\n"
"v_mfma_f32_16x16x32_bf16 v[28:31], a[236:239], a[124:127], v[28:31]\n"
"s_add_u32 s14, s14, s8\n"
"ds_read_b64_tr_b16 a[248:249], v231 offset:8\n"
"v_mfma_f32_16x16x32_bf16 v[20:23], a[236:239], a[120:123], v[20:23]\n"
"s_add_u32 s14, s14, s8\n"
"ds_read_b64_tr_b16 a[250:251], v231 offset:264\n"
"v_mfma_f32_16x16x32_bf16 v[20:23], a[232:235], a[104:107], v[20:23]\n"
"s_add_u32 s14, s14, s8\n"
"ds_read_b128 a[152:155], v233 offset:0\n"
"v_mfma_f32_16x16x32_bf16 v[12:15], a[232:235], a[100:103], v[12:15]\n"
"s_add_u32 s14, s14, s8\n"
"ds_read_b64_tr_b16 a[228:229], v230 offset:640\n"
"v_mfma_f32_16x16x32_bf16 v[12:15], a[236:239], a[116:119], v[12:15]\n"
"s_add_u32 s14, s14, s8\n"
"ds_read_b64_tr_b16 a[224:225], v230 offset:128\n"
"v_mfma_f32_16x16x32_bf16 v[4:7], a[236:239], a[112:115], v[4:7]\n"
"s_add_u32 s14, s14, s8\n"
"buffer_load_dwordx4 v228, s[40:43], s80 offen offset:0 lds\n"
"v_mfma_f32_16x16x32_bf16 v[4:7], a[232:235], a[96:99], v[4:7]\n"
"s_waitcnt lgkmcnt(8)\n"
"s_add_u32 s14, s14, s8\n"
"ds_read_b64_tr_b16 a[226:227], v230 offset:384\n"
"v_mfma_f32_16x16x32_bf16 v[32:35], a[240:243], a[96:99], v[32:35]\n"
"s_add_u32 s14, s14, s8\n"
"ds_read_b64_tr_b16 a[230:231], v230 offset:896\n"
"v_mfma_f32_16x16x32_bf16 v[32:35], a[244:247], a[112:115], v[32:35]\n"
"s_cmp_gt_u32 s14, s15\n"
"s_cselect_b32 s31, 1, 0\n"
"ds_read_b128 a[156:159], v233 offset:2048\n"
"v_mfma_f32_16x16x32_bf16 v[40:43], a[244:247], a[116:119], v[40:43]\n"
"ds_read_b64_tr_b16 a[236:237], v230 offset:648\n"
"v_mfma_f32_16x16x32_bf16 v[40:43], a[240:243], a[100:103], v[40:43]\n"
"ds_read_b64_tr_b16 a[238:239], v230 offset:904\n"
"v_mfma_f32_16x16x32_bf16 v[48:51], a[240:243], a[104:107], v[48:51]\n"
"ds_read_b64_tr_b16 a[232:233], v230 offset:136\n"
"v_mfma_f32_16x16x32_bf16 v[48:51], a[244:247], a[120:123], v[48:51]\n"
"s_waitcnt lgkmcnt(10)\n"
"buffer_load_dwordx4 v229, s[40:43], s80 offen offset:1024 lds\n"
"v_mfma_f32_16x16x32_bf16 v[52:55], a[252:255], a[120:123], v[52:55]\n"
"s_waitcnt lgkmcnt(9)\n"
"s_add_u32 m0, s79, 0xc2a0\n"
"ds_read_b64_tr_b16 a[234:235], v230 offset:392\n"
"v_mfma_f32_16x16x32_bf16 v[52:55], a[248:251], a[104:107], v[52:55]\n"
"s_add_u32 s80, s9, s29\n"
"ds_read_b64_tr_b16 a[240:241], v231 offset:128\n"
"v_add_u32 v230, 0x1c620, v230\n"
"v_mfma_f32_16x16x32_bf16 v[44:47], a[248:251], a[100:103], v[44:47]\n"
"s_addk_i32 s9, 0x1000\n"
"ds_read_b64_tr_b16 a[242:243], v231 offset:384\n"
"v_mfma_f32_16x16x32_bf16 v[44:47], a[252:255], a[116:119], v[44:47]\n"
"s_bitset0_b32 s9, 15\n"
"ds_read_b64_tr_b16 a[244:245], v231 offset:640\n"
"v_mfma_f32_16x16x32_bf16 v[36:39], a[252:255], a[112:115], v[36:39]\n"
"ds_read_b64_tr_b16 a[246:247], v231 offset:896\n"
"v_mfma_f32_16x16x32_bf16 v[36:39], a[248:251], a[96:99], v[36:39]\n"
"s_waitcnt lgkmcnt(10)\n"
"ds_read_b64_tr_b16 a[252:253], v231 offset:648\n"
"v_mfma_f32_16x16x32_bf16 v[56:59], a[224:227], a[96:99], v[56:59]\n"
"s_waitcnt lgkmcnt(10)\n"
"buffer_load_dwordx4 v224, s[36:39], s82 offen offset:0 lds\n"
"v_mfma_f32_16x16x32_bf16 v[56:59], a[228:231], a[112:115], v[56:59]\n"
"ds_read_b64_tr_b16 a[254:255], v231 offset:904\n"
"v_mfma_f32_16x16x32_bf16 v[64:67], a[228:231], a[116:119], v[64:67]\n"
"ds_read_b64_tr_b16 a[248:249], v231 offset:136\n"
"v_mfma_f32_16x16x32_bf16 v[64:67], a[224:227], a[100:103], v[64:67]\n"
"ds_read_b64_tr_b16 a[250:251], v231 offset:392\n"
"v_add_u32 v231, 0x1c620, v231\n"
"v_mfma_f32_16x16x32_bf16 v[72:75], a[224:227], a[104:107], v[72:75]\n"
"v_mfma_f32_16x16x32_bf16 v[72:75], a[228:231], a[120:123], v[72:75]\n"
"buffer_load_dwordx4 v225, s[36:39], s82 offen offset:1024 lds\n"
"v_mfma_f32_16x16x32_bf16 v[80:83], a[228:231], a[124:127], v[80:83]\n"
"s_addk_i32 m0, 0x800\n"
"s_add_u32 s82, s95, s70\n"
"v_mfma_f32_16x16x32_bf16 v[80:83], a[224:227], a[108:111], v[80:83]\n"
"s_waitcnt lgkmcnt(7)\n"
"v_mfma_f32_16x16x32_bf16 v[84:87], a[232:235], a[108:111], v[84:87]\n"
"buffer_load_dwordx4 v224, s[36:39], s83 offen offset:0 lds\n"
"v_mfma_f32_16x16x32_bf16 v[84:87], a[236:239], a[124:127], v[84:87]\n"
"s_waitcnt vmcnt(15) lgkmcnt(0)\n"
"s_barrier\n"
"v_mfma_f32_16x16x32_bf16 v[76:79], a[236:239], a[120:123], v[76:79]\n"
"ds_read_b64_tr_b16 a[228:229], v230 offset:512\n"
"v_mfma_f32_16x16x32_bf16 v[76:79], a[232:235], a[104:107], v[76:79]\n"
"buffer_load_dwordx4 v225, s[36:39], s83 offen offset:1024 lds\n"
"v_mfma_f32_16x16x32_bf16 v[68:71], a[232:235], a[100:103], v[68:71]\n"
"s_add_u32 m0, s79, 0x10380\n"
"s_add_u32 s83, s95, s71\n"
"ds_read_b64_tr_b16 a[224:225], v230 offset:0\n"
"v_mfma_f32_16x16x32_bf16 v[68:71], a[236:239], a[116:119], v[68:71]\n"
"s_lshl_b32 s95, %2, 12\n"
"ds_read_b64_tr_b16 a[226:227], v230 offset:256\n"
"v_mfma_f32_16x16x32_bf16 v[60:63], a[236:239], a[112:115], v[60:63]\n"
"buffer_load_dwordx4 v224, s[36:39], s82 offen offset:0 lds\n"
"v_mfma_f32_16x16x32_bf16 v[60:63], a[232:235], a[96:99], v[60:63]\n"
"ds_read_b64_tr_b16 a[230:231], v230 offset:768\n"
"v_mfma_f32_16x16x32_bf16 v[88:91], a[240:243], a[96:99], v[88:91]\n"
"ds_read_b64_tr_b16 a[236:237], v230 offset:520\n"
"v_mfma_f32_16x16x32_bf16 v[88:91], a[244:247], a[112:115], v[88:91]\n"
"ds_read_b64_tr_b16 a[232:233], v230 offset:8\n"
"v_mfma_f32_16x16x32_bf16 v[96:99], a[244:247], a[116:119], v[96:99]\n"
"buffer_load_dwordx4 v225, s[36:39], s82 offen offset:1024 lds\n"
"v_mfma_f32_16x16x32_bf16 v[96:99], a[240:243], a[100:103], v[96:99]\n"
"s_addk_i32 m0, 0x800\n"
"ds_read_b64_tr_b16 a[234:235], v230 offset:264\n"
"v_mfma_f32_16x16x32_bf16 v[104:107], a[240:243], a[104:107], v[104:107]\n"
"ds_read_b64_tr_b16 a[238:239], v230 offset:776\n"
"v_mfma_f32_16x16x32_bf16 v[104:107], a[244:247], a[120:123], v[104:107]\n"
"buffer_load_dwordx4 v224, s[36:39], s83 offen offset:0 lds\n"
"v_mfma_f32_16x16x32_bf16 v[108:111], a[252:255], a[120:123], v[108:111]\n"
"ds_read_b64_tr_b16 a[240:241], v231 offset:0\n"
"v_mov_b32 v224, v244\n"
"v_mfma_f32_16x16x32_bf16 v[108:111], a[248:251], a[104:107], v[108:111]\n"
"ds_read_b64_tr_b16 a[242:243], v231 offset:256\n"
"v_add_u32 v244, 0x10380, v244\n"
"v_mfma_f32_16x16x32_bf16 v[100:103], a[248:251], a[100:103], v[100:103]\n"
"ds_read_b64_tr_b16 a[244:245], v231 offset:512\n"
"v_add_u32 v224, 0x18540, v224\n"
"v_mfma_f32_16x16x32_bf16 v[100:103], a[252:255], a[116:119], v[100:103]\n"
"ds_read_b64_tr_b16 a[246:247], v231 offset:768\n"
"v_mfma_f32_16x16x32_bf16 v[92:95], a[252:255], a[112:115], v[92:95]\n"
"buffer_load_dwordx4 v225, s[36:39], s83 offen offset:1024 lds\n"
"v_mfma_f32_16x16x32_bf16 v[92:95], a[248:251], a[96:99], v[92:95]\n"
"s_waitcnt lgkmcnt(8)\n"
"s_add_u32 m0, s79, 0x0\n"
"ds_read_b64_tr_b16 a[252:253], v231 offset:520\n"
"v_mfma_f32_16x16x32_bf16 v[112:115], a[224:227], a[96:99], v[112:115]\n"
"ds_read_b64_tr_b16 a[254:255], v231 offset:776\n"
"v_mov_b32 v225, v245\n"
"v_mfma_f32_16x16x32_bf16 v[112:115], a[228:231], a[112:115], v[112:115]\n"
"ds_read_b64_tr_b16 a[248:249], v231 offset:8\n"
"v_add_u32 v245, 0x10380, v245\n"
"v_mfma_f32_16x16x32_bf16 v[120:123], a[228:231], a[116:119], v[120:123]\n"
"ds_read_b64_tr_b16 a[250:251], v231 offset:264\n"
"v_add_u32 v225, 0x18540, v225\n"
"v_mfma_f32_16x16x32_bf16 v[120:123], a[224:227], a[100:103], v[120:123]\n"
"v_mfma_f32_16x16x32_bf16 v[128:131], a[224:227], a[104:107], v[128:131]\n"
"buffer_load_dwordx4 v226, s[40:43], s81 offen offset:0 lds\n"
"v_mfma_f32_16x16x32_bf16 v[128:131], a[228:231], a[120:123], v[128:131]\n"
"v_mfma_f32_16x16x32_bf16 v[136:139], a[228:231], a[124:127], v[136:139]\n"
"ds_read_b64_tr_b16 a[228:229], v230 offset:640\n"
"v_mfma_f32_16x16x32_bf16 v[136:139], a[224:227], a[108:111], v[136:139]\n"
"s_waitcnt lgkmcnt(7)\n"
"buffer_load_dwordx4 v227, s[40:43], s81 offen offset:1024 lds\n"
"v_mfma_f32_16x16x32_bf16 v[140:143], a[232:235], a[108:111], v[140:143]\n"
"s_addk_i32 m0, 0x808\n"
"ds_read_b64_tr_b16 a[224:225], v230 offset:128\n"
"v_mfma_f32_16x16x32_bf16 v[140:143], a[236:239], a[124:127], v[140:143]\n"
"ds_read_b64_tr_b16 a[226:227], v230 offset:384\n"
"v_mfma_f32_16x16x32_bf16 v[132:135], a[236:239], a[120:123], v[132:135]\n"
"buffer_load_dwordx4 v228, s[40:43], s81 offen offset:0 lds\n"
"v_mfma_f32_16x16x32_bf16 v[132:135], a[232:235], a[104:107], v[132:135]\n"
"ds_read_b64_tr_b16 a[230:231], v230 offset:896\n"
"v_mfma_f32_16x16x32_bf16 v[124:127], a[232:235], a[100:103], v[124:127]\n"
"v_mfma_f32_16x16x32_bf16 v[124:127], a[236:239], a[116:119], v[124:127]\n"
"buffer_load_dwordx4 v229, s[40:43], s81 offen offset:1024 lds\n"
"v_mfma_f32_16x16x32_bf16 v[116:119], a[236:239], a[112:115], v[116:119]\n"
"s_add_u32 m0, s79, 0x14460\n"
"s_lshl_b32 s81, s96, 12\n"
"ds_read_b64_tr_b16 a[236:237], v230 offset:648\n"
"v_mfma_f32_16x16x32_bf16 v[116:119], a[232:235], a[96:99], v[116:119]\n"
"ds_read_b64_tr_b16 a[232:233], v230 offset:136\n"
"v_mfma_f32_16x16x32_bf16 v[144:147], a[240:243], a[96:99], v[144:147]\n"
"s_waitcnt lgkmcnt(6)\n"
"buffer_load_dwordx4 v248, s[44:47], s80 offen offset:0 nt lds\n"
"v_mfma_f32_16x16x32_bf16 v[144:147], a[244:247], a[112:115], v[144:147]\n"
"ds_read_b64_tr_b16 a[234:235], v230 offset:392\n"
"v_mfma_f32_16x16x32_bf16 v[152:155], a[244:247], a[116:119], v[152:155]\n"
"ds_read_b64_tr_b16 a[238:239], v230 offset:904\n"
"v_add_u32 v230, 0xfffebba0, v230\n"
"v_mfma_f32_16x16x32_bf16 v[152:155], a[240:243], a[100:103], v[152:155]\n"
"v_mfma_f32_16x16x32_bf16 v[160:163], a[240:243], a[104:107], v[160:163]\n"
"buffer_load_dwordx4 v248, s[44:47], s80 offen offset:1024 nt lds\n"
"v_mfma_f32_16x16x32_bf16 v[160:163], a[244:247], a[120:123], v[160:163]\n"
"s_addk_i32 m0, 0x20\n"
"ds_read_b64_tr_b16 a[240:241], v231 offset:128\n"
"v_mfma_f32_16x16x32_bf16 v[164:167], a[252:255], a[120:123], v[164:167]\n"
"ds_read_b64_tr_b16 a[242:243], v231 offset:384\n"
"v_mfma_f32_16x16x32_bf16 v[164:167], a[248:251], a[104:107], v[164:167]\n"
"ds_read_b64_tr_b16 a[244:245], v231 offset:640\n"
"v_mfma_f32_16x16x32_bf16 v[156:159], a[248:251], a[100:103], v[156:159]\n"
"ds_read_b64_tr_b16 a[246:247], v231 offset:896\n"
"v_mfma_f32_16x16x32_bf16 v[156:159], a[252:255], a[116:119], v[156:159]\n"
"v_mfma_f32_16x16x32_bf16 v[148:151], a[252:255], a[112:115], v[148:151]\n"
"ds_read_b64_tr_b16 a[252:253], v231 offset:648\n"
"v_mfma_f32_16x16x32_bf16 v[148:151], a[248:251], a[96:99], v[148:151]\n"
"s_waitcnt lgkmcnt(7)\n"
"ds_read_b64_tr_b16 a[254:255], v231 offset:904\n"
"v_mfma_f32_16x16x32_bf16 v[168:171], a[224:227], a[96:99], v[168:171]\n"
"buffer_load_dwordx4 v248, s[44:47], s80 offen offset:2048 nt lds\n"
"v_mfma_f32_16x16x32_bf16 v[168:171], a[228:231], a[112:115], v[168:171]\n"
"ds_read_b64_tr_b16 a[248:249], v231 offset:136\n"
"v_mfma_f32_16x16x32_bf16 v[176:179], a[228:231], a[116:119], v[176:179]\n"
"ds_read_b64_tr_b16 a[250:251], v231 offset:392\n"
"v_add_u32 v231, 0xfffebba0, v231\n"
"v_mfma_f32_16x16x32_bf16 v[176:179], a[224:227], a[100:103], v[176:179]\n"
"v_mfma_f32_16x16x32_bf16 v[184:187], a[224:227], a[104:107], v[184:187]\n"
"v_mfma_f32_16x16x32_bf16 v[184:187], a[228:231], a[120:123], v[184:187]\n"
"v_mfma_f32_16x16x32_bf16 v[192:195], a[228:231], a[124:127], v[192:195]\n"
"v_mfma_f32_16x16x32_bf16 v[192:195], a[224:227], a[108:111], v[192:195]\n"
"s_waitcnt vmcnt(15) lgkmcnt(0)\n"
"s_barrier\n"
"v_mfma_f32_16x16x32_bf16 v[196:199], a[232:235], a[108:111], v[196:199]\n"
"ds_read_b64_tr_b16 a[228:229], v230 offset:512\n"
"v_mfma_f32_16x16x32_bf16 v[196:199], a[236:239], a[124:127], v[196:199]\n"
"buffer_load_dwordx4 v248, s[44:47], s80 offen offset:3072 nt lds\n"
"v_mfma_f32_16x16x32_bf16 v[188:191], a[236:239], a[120:123], v[188:191]\n"
"s_add_u32 m0, s79, 0x18540\n"
"s_add_u32 s80, s9, s29\n"
"ds_read_b64_tr_b16 a[224:225], v230 offset:0\n"
"v_mfma_f32_16x16x32_bf16 v[188:191], a[232:235], a[104:107], v[188:191]\n"
"s_addk_i32 s9, 0x1000\n"
"ds_read_b64_tr_b16 a[226:227], v230 offset:256\n"
"v_mfma_f32_16x16x32_bf16 v[180:183], a[232:235], a[100:103], v[180:183]\n"
"s_bitset0_b32 s9, 15\n"
"ds_read_b64_tr_b16 a[230:231], v230 offset:768\n"
"v_mfma_f32_16x16x32_bf16 v[180:183], a[236:239], a[116:119], v[180:183]\n"
"ds_read_b128 a[160:163], v232 offset:33216\n"
"v_mfma_f32_16x16x32_bf16 v[172:175], a[236:239], a[112:115], v[172:175]\n"
"ds_read_b64_tr_b16 a[236:237], v230 offset:520\n"
"v_mfma_f32_16x16x32_bf16 v[172:175], a[232:235], a[96:99], v[172:175]\n"
"ds_read_b64_tr_b16 a[238:239], v230 offset:776\n"
"v_mfma_f32_16x16x32_bf16 v[200:203], a[240:243], a[96:99], v[200:203]\n"
"ds_read_b64_tr_b16 a[232:233], v230 offset:8\n"
"v_mfma_f32_16x16x32_bf16 v[200:203], a[244:247], a[112:115], v[200:203]\n"
"buffer_load_dwordx4 v226, s[40:43], s81 offen offset:0 lds\n"
"v_mfma_f32_16x16x32_bf16 v[208:211], a[244:247], a[116:119], v[208:211]\n"
"ds_read_b64_tr_b16 a[234:235], v230 offset:264\n"
"v_mfma_f32_16x16x32_bf16 v[208:211], a[240:243], a[100:103], v[208:211]\n"
"ds_read_b128 a[164:167], v232 offset:35264\n"
"v_add_u32 v232, 0xfffebba0, v232\n"
"v_mfma_f32_16x16x32_bf16 v[216:219], a[240:243], a[104:107], v[216:219]\n"
"ds_read_b64_tr_b16 a[240:241], v231 offset:0\n"
"v_mfma_f32_16x16x32_bf16 v[216:219], a[244:247], a[120:123], v[216:219]\n"
"buffer_load_dwordx4 v227, s[40:43], s81 offen offset:1024 lds\n"
"v_mfma_f32_16x16x32_bf16 v[220:223], a[252:255], a[120:123], v[220:223]\n"
"s_addk_i32 m0, 0x808\n"
"ds_read_b64_tr_b16 a[242:243], v231 offset:256\n"
"v_mfma_f32_16x16x32_bf16 v[220:223], a[248:251], a[104:107], v[220:223]\n"
"ds_read_b64_tr_b16 a[244:245], v231 offset:512\n"
"v_mfma_f32_16x16x32_bf16 v[212:215], a[248:251], a[100:103], v[212:215]\n"
"buffer_load_dwordx4 v228, s[40:43], s81 offen offset:0 lds\n"
"v_mfma_f32_16x16x32_bf16 v[212:215], a[252:255], a[116:119], v[212:215]\n"
"ds_read_b64_tr_b16 a[246:247], v231 offset:768\n"
"v_mfma_f32_16x16x32_bf16 v[204:207], a[252:255], a[112:115], v[204:207]\n"
"ds_read_b128 a[176:179], v233 offset:33216\n"
"v_mfma_f32_16x16x32_bf16 v[204:207], a[248:251], a[96:99], v[204:207]\n"
"s_waitcnt lgkmcnt(8)\n"
"buffer_load_dwordx4 v229, s[40:43], s81 offen offset:1024 lds\n"
"v_mfma_f32_16x16x32_bf16 v[0:3], a[224:227], a[128:131], v[0:3]\n"
"s_add_u32 m0, s79, 0x1c620\n"
"s_lshl_b32 s81, s96, 12\n"
"ds_read_b64_tr_b16 a[252:253], v231 offset:520\n"
"v_mfma_f32_16x16x32_bf16 v[0:3], a[228:231], a[144:147], v[0:3]\n"
"s_add_u32 s81, s81, 0x100\n"
"s_add_u32 s96, s96, 128\n"
"s_cmp_eq_u32 s96, 896\n"
"s_cselect_b32 s96, 0, s96\n"
"ds_read_b64_tr_b16 a[254:255], v231 offset:776\n"
"v_mfma_f32_16x16x32_bf16 v[8:11], a[228:231], a[148:151], v[8:11]\n"
"ds_read_b64_tr_b16 a[248:249], v231 offset:8\n"
"v_mfma_f32_16x16x32_bf16 v[8:11], a[224:227], a[132:135], v[8:11]\n"
"buffer_load_dwordx4 v248, s[44:47], s80 offen offset:0 nt lds\n"
"v_mfma_f32_16x16x32_bf16 v[16:19], a[224:227], a[136:139], v[16:19]\n"
"ds_read_b64_tr_b16 a[250:251], v231 offset:264\n"
"v_mfma_f32_16x16x32_bf16 v[16:19], a[228:231], a[152:155], v[16:19]\n"
"ds_read_b128 a[180:183], v233 offset:35264\n"
"v_add_u32 v233, 0xfffebba0, v233\n"
"v_mfma_f32_16x16x32_bf16 v[24:27], a[228:231], a[156:159], v[24:27]\n"
"ds_read_b128 a[168:171], v232 offset:0\n"
"v_mfma_f32_16x16x32_bf16 v[24:27], a[224:227], a[140:143], v[24:27]\n"
"s_waitcnt lgkmcnt(8)\n"
"ds_read_b64_tr_b16 a[228:229], v230 offset:640\n"
"v_mfma_f32_16x16x32_bf16 v[28:31], a[232:235], a[140:143], v[28:31]\n"
"ds_read_b64_tr_b16 a[224:225], v230 offset:128\n"
"v_mfma_f32_16x16x32_bf16 v[28:31], a[236:239], a[156:159], v[28:31]\n"
"ds_read_b64_tr_b16 a[226:227], v230 offset:384\n"
"v_mfma_f32_16x16x32_bf16 v[20:23], a[236:239], a[152:155], v[20:23]\n"
"ds_read_b64_tr_b16 a[230:231], v230 offset:896\n"
"v_mfma_f32_16x16x32_bf16 v[20:23], a[232:235], a[136:139], v[20:23]\n"
"ds_read_b128 a[172:175], v232 offset:2048\n"
"v_mfma_f32_16x16x32_bf16 v[12:15], a[232:235], a[132:135], v[12:15]\n"
"ds_read_b128 a[184:187], v233 offset:0\n"
"v_mfma_f32_16x16x32_bf16 v[12:15], a[236:239], a[148:151], v[12:15]\n"
"buffer_load_dwordx4 v248, s[44:47], s80 offen offset:1024 nt lds\n"
"v_mfma_f32_16x16x32_bf16 v[4:7], a[236:239], a[144:147], v[4:7]\n"
"s_addk_i32 m0, 0x20\n"
"ds_read_b128 a[188:191], v233 offset:2048\n"
"v_mfma_f32_16x16x32_bf16 v[4:7], a[232:235], a[128:131], v[4:7]\n"
"ds_read_b64_tr_b16 a[236:237], v230 offset:648\n"
"v_mfma_f32_16x16x32_bf16 v[32:35], a[240:243], a[128:131], v[32:35]\n"
"s_waitcnt lgkmcnt(10)\n"
"ds_read_b64_tr_b16 a[232:233], v230 offset:136\n"
"v_mfma_f32_16x16x32_bf16 v[32:35], a[244:247], a[144:147], v[32:35]\n"
"ds_read_b64_tr_b16 a[234:235], v230 offset:392\n"
"v_mfma_f32_16x16x32_bf16 v[40:43], a[244:247], a[148:151], v[40:43]\n"
"ds_read_b64_tr_b16 a[238:239], v230 offset:904\n"
"v_add_u32 v230, 0xffff7e40, v230\n"
"v_mfma_f32_16x16x32_bf16 v[40:43], a[240:243], a[132:135], v[40:43]\n"
"v_mfma_f32_16x16x32_bf16 v[48:51], a[240:243], a[136:139], v[48:51]\n"
"ds_read_b64_tr_b16 a[240:241], v231 offset:128\n"
"v_mfma_f32_16x16x32_bf16 v[48:51], a[244:247], a[152:155], v[48:51]\n"
"ds_read_b64_tr_b16 a[242:243], v231 offset:384\n"
"v_mfma_f32_16x16x32_bf16 v[52:55], a[252:255], a[152:155], v[52:55]\n"
"ds_read_b64_tr_b16 a[244:245], v231 offset:640\n"
"v_mfma_f32_16x16x32_bf16 v[52:55], a[248:251], a[136:139], v[52:55]\n"
"ds_read_b64_tr_b16 a[246:247], v231 offset:896\n"
"v_mfma_f32_16x16x32_bf16 v[44:47], a[248:251], a[132:135], v[44:47]\n"
"buffer_load_dwordx4 v248, s[44:47], s80 offen offset:2048 nt lds\n"
"v_mfma_f32_16x16x32_bf16 v[44:47], a[252:255], a[148:151], v[44:47]\n"
"v_mfma_f32_16x16x32_bf16 v[36:39], a[252:255], a[144:147], v[36:39]\n"
"ds_read_b64_tr_b16 a[252:253], v231 offset:648\n"
"v_mfma_f32_16x16x32_bf16 v[36:39], a[248:251], a[128:131], v[36:39]\n"
"s_waitcnt lgkmcnt(8)\n"
"ds_read_b64_tr_b16 a[254:255], v231 offset:904\n"
"v_mfma_f32_16x16x32_bf16 v[56:59], a[224:227], a[128:131], v[56:59]\n"
"ds_read_b64_tr_b16 a[248:249], v231 offset:136\n"
"v_mfma_f32_16x16x32_bf16 v[56:59], a[228:231], a[144:147], v[56:59]\n"
"ds_read_b64_tr_b16 a[250:251], v231 offset:392\n"
"v_add_u32 v231, 0xffff7e40, v231\n"
"v_mfma_f32_16x16x32_bf16 v[64:67], a[228:231], a[148:151], v[64:67]\n"
"v_mfma_f32_16x16x32_bf16 v[64:67], a[224:227], a[132:135], v[64:67]\n"
"v_mfma_f32_16x16x32_bf16 v[72:75], a[224:227], a[136:139], v[72:75]\n"
"v_mfma_f32_16x16x32_bf16 v[72:75], a[228:231], a[152:155], v[72:75]\n"
"v_mfma_f32_16x16x32_bf16 v[80:83], a[228:231], a[156:159], v[80:83]\n"
"s_waitcnt vmcnt(11) lgkmcnt(0)\n"
"s_barrier\n"
"v_mfma_f32_16x16x32_bf16 v[80:83], a[224:227], a[140:143], v[80:83]\n"
"buffer_load_dwordx4 v248, s[44:47], s80 offen offset:3072 nt lds\n"
"v_mfma_f32_16x16x32_bf16 v[84:87], a[232:235], a[140:143], v[84:87]\n"
"s_add_u32 m0, s79, 0x40e0\n"
"s_add_u32 s80, s9, s29\n"
"ds_read_b64_tr_b16 a[228:229], v230 offset:512\n"
"v_mfma_f32_16x16x32_bf16 v[84:87], a[236:239], a[156:159], v[84:87]\n"
"s_addk_i32 s9, 0x1000\n"
"ds_read_b64_tr_b16 a[224:225], v230 offset:0\n"
"v_mfma_f32_16x16x32_bf16 v[76:79], a[236:239], a[152:155], v[76:79]\n"
"s_bitset0_b32 s9, 15\n"
"ds_read_b64_tr_b16 a[226:227], v230 offset:256\n"
"v_mfma_f32_16x16x32_bf16 v[76:79], a[232:235], a[136:139], v[76:79]\n"
"ds_read_b64_tr_b16 a[230:231], v230 offset:768\n"
"v_mfma_f32_16x16x32_bf16 v[68:71], a[232:235], a[132:135], v[68:71]\n"
"ds_read_b128 a[192:195], v232 offset:33216\n"
"v_mfma_f32_16x16x32_bf16 v[68:71], a[236:239], a[148:151], v[68:71]\n"
"ds_read_b128 a[196:199], v232 offset:35264\n"
"v_mfma_f32_16x16x32_bf16 v[60:63], a[236:239], a[144:147], v[60:63]\n"
"ds_read_b64_tr_b16 a[236:237], v230 offset:520\n"
"v_mfma_f32_16x16x32_bf16 v[60:63], a[232:235], a[128:131], v[60:63]\n"
"ds_read_b64_tr_b16 a[232:233], v230 offset:8\n"
"v_mfma_f32_16x16x32_bf16 v[88:91], a[240:243], a[128:131], v[88:91]\n"
"ds_read_b64_tr_b16 a[234:235], v230 offset:264\n"
"v_mfma_f32_16x16x32_bf16 v[88:91], a[244:247], a[144:147], v[88:91]\n"
"buffer_load_dwordx4 v226, s[40:43], s81 offen offset:0 lds\n"
"v_mfma_f32_16x16x32_bf16 v[96:99], a[244:247], a[148:151], v[96:99]\n"
"ds_read_b64_tr_b16 a[238:239], v230 offset:776\n"
"v_mfma_f32_16x16x32_bf16 v[96:99], a[240:243], a[132:135], v[96:99]\n"
"ds_read_b128 a[208:211], v233 offset:33216\n"
"v_mfma_f32_16x16x32_bf16 v[104:107], a[240:243], a[136:139], v[104:107]\n"
"ds_read_b64_tr_b16 a[240:241], v231 offset:0\n"
"v_mfma_f32_16x16x32_bf16 v[104:107], a[244:247], a[152:155], v[104:107]\n"
"buffer_load_dwordx4 v227, s[40:43], s81 offen offset:1024 lds\n"
"v_mfma_f32_16x16x32_bf16 v[108:111], a[252:255], a[152:155], v[108:111]\n"
"s_addk_i32 m0, 0x808\n"
"ds_read_b64_tr_b16 a[242:243], v231 offset:256\n"
"v_mfma_f32_16x16x32_bf16 v[108:111], a[248:251], a[136:139], v[108:111]\n"
"ds_read_b64_tr_b16 a[244:245], v231 offset:512\n"
"v_mfma_f32_16x16x32_bf16 v[100:103], a[248:251], a[132:135], v[100:103]\n"
"ds_read_b64_tr_b16 a[246:247], v231 offset:768\n"
"v_mfma_f32_16x16x32_bf16 v[100:103], a[252:255], a[148:151], v[100:103]\n"
"buffer_load_dwordx4 v228, s[40:43], s81 offen offset:0 lds\n"
"v_mfma_f32_16x16x32_bf16 v[92:95], a[252:255], a[144:147], v[92:95]\n"
"ds_read_b64_tr_b16 a[252:253], v231 offset:520\n"
"v_mfma_f32_16x16x32_bf16 v[92:95], a[248:251], a[128:131], v[92:95]\n"
"s_waitcnt lgkmcnt(8)\n"
"ds_read_b64_tr_b16 a[254:255], v231 offset:776\n"
"v_mfma_f32_16x16x32_bf16 v[112:115], a[224:227], a[128:131], v[112:115]\n"
"ds_read_b64_tr_b16 a[248:249], v231 offset:8\n"
"v_mfma_f32_16x16x32_bf16 v[112:115], a[228:231], a[144:147], v[112:115]\n"
"buffer_load_dwordx4 v229, s[40:43], s81 offen offset:1024 lds\n"
"v_mfma_f32_16x16x32_bf16 v[120:123], a[228:231], a[148:151], v[120:123]\n"
"s_add_u32 m0, s79, 0x81c0\n"
"s_lshl_b32 s81, s96, 12\n"
"ds_read_b64_tr_b16 a[250:251], v231 offset:264\n"
"v_mfma_f32_16x16x32_bf16 v[120:123], a[224:227], a[132:135], v[120:123]\n"
"ds_read_b128 a[212:215], v233 offset:35264\n"
"v_mfma_f32_16x16x32_bf16 v[128:131], a[224:227], a[136:139], v[128:131]\n"
"ds_read_b128 a[200:203], v232 offset:49824\n"
"v_mfma_f32_16x16x32_bf16 v[128:131], a[228:231], a[152:155], v[128:131]\n"
"buffer_load_dwordx4 v248, s[44:47], s80 offen offset:0 nt lds\n"
"v_mfma_f32_16x16x32_bf16 v[136:139], a[228:231], a[156:159], v[136:139]\n"
"ds_read_b64_tr_b16 a[228:229], v230 offset:640\n"
"v_mfma_f32_16x16x32_bf16 v[136:139], a[224:227], a[140:143], v[136:139]\n"
"s_waitcnt lgkmcnt(8)\n"
"ds_read_b64_tr_b16 a[224:225], v230 offset:128\n"
"v_mfma_f32_16x16x32_bf16 v[140:143], a[232:235], a[140:143], v[140:143]\n"
"ds_read_b64_tr_b16 a[226:227], v230 offset:384\n"
"v_mfma_f32_16x16x32_bf16 v[140:143], a[236:239], a[156:159], v[140:143]\n"
"ds_read_b64_tr_b16 a[230:231], v230 offset:896\n"
"v_mfma_f32_16x16x32_bf16 v[132:135], a[236:239], a[152:155], v[132:135]\n"
"ds_read_b128 a[204:207], v232 offset:51872\n"
"v_mfma_f32_16x16x32_bf16 v[132:135], a[232:235], a[136:139], v[132:135]\n"
"ds_read_b128 a[216:219], v233 offset:49824\n"
"v_mfma_f32_16x16x32_bf16 v[124:127], a[232:235], a[132:135], v[124:127]\n"
"ds_read_b128 a[220:223], v233 offset:51872\n"
"v_mfma_f32_16x16x32_bf16 v[124:127], a[236:239], a[148:151], v[124:127]\n"
"v_mfma_f32_16x16x32_bf16 v[116:119], a[236:239], a[144:147], v[116:119]\n"
"ds_read_b64_tr_b16 a[236:237], v230 offset:648\n"
"v_mfma_f32_16x16x32_bf16 v[116:119], a[232:235], a[128:131], v[116:119]\n"
"buffer_load_dwordx4 v248, s[44:47], s80 offen offset:1024 nt lds\n"
"v_mfma_f32_16x16x32_bf16 v[144:147], a[240:243], a[128:131], v[144:147]\n"
"s_waitcnt lgkmcnt(8)\n"
"s_addk_i32 m0, 0x20\n"
"ds_read_b64_tr_b16 a[232:233], v230 offset:136\n"
"v_mfma_f32_16x16x32_bf16 v[144:147], a[244:247], a[144:147], v[144:147]\n"
"ds_read_b64_tr_b16 a[234:235], v230 offset:392\n"
"v_mfma_f32_16x16x32_bf16 v[152:155], a[244:247], a[148:151], v[152:155]\n"
"ds_read_b64_tr_b16 a[238:239], v230 offset:904\n"
"v_add_u32 v230, 0x18540, v230\n"
"v_mfma_f32_16x16x32_bf16 v[152:155], a[240:243], a[132:135], v[152:155]\n"
"v_mfma_f32_16x16x32_bf16 v[160:163], a[240:243], a[136:139], v[160:163]\n"
"ds_read_b64_tr_b16 a[240:241], v231 offset:128\n"
"v_mfma_f32_16x16x32_bf16 v[160:163], a[244:247], a[152:155], v[160:163]\n"
"ds_read_b64_tr_b16 a[242:243], v231 offset:384\n"
"v_mfma_f32_16x16x32_bf16 v[164:167], a[252:255], a[152:155], v[164:167]\n"
"ds_read_b64_tr_b16 a[244:245], v231 offset:640\n"
"v_mfma_f32_16x16x32_bf16 v[164:167], a[248:251], a[136:139], v[164:167]\n"
"ds_read_b64_tr_b16 a[246:247], v231 offset:896\n"
"v_mfma_f32_16x16x32_bf16 v[156:159], a[248:251], a[132:135], v[156:159]\n"
"v_mfma_f32_16x16x32_bf16 v[156:159], a[252:255], a[148:151], v[156:159]\n"
"buffer_load_dwordx4 v248, s[44:47], s80 offen offset:2048 nt lds\n"
"v_mfma_f32_16x16x32_bf16 v[148:151], a[252:255], a[144:147], v[148:151]\n"
"ds_read_b64_tr_b16 a[252:253], v231 offset:648\n"
"v_mfma_f32_16x16x32_bf16 v[148:151], a[248:251], a[128:131], v[148:151]\n"
"s_waitcnt lgkmcnt(8)\n"
"ds_read_b64_tr_b16 a[254:255], v231 offset:904\n"
"v_mfma_f32_16x16x32_bf16 v[168:171], a[224:227], a[128:131], v[168:171]\n"
"ds_read_b64_tr_b16 a[248:249], v231 offset:136\n"
"v_mfma_f32_16x16x32_bf16 v[168:171], a[228:231], a[144:147], v[168:171]\n"
"ds_read_b64_tr_b16 a[250:251], v231 offset:392\n"
"v_add_u32 v231, 0x18540, v231\n"
"v_mfma_f32_16x16x32_bf16 v[176:179], a[228:231], a[148:151], v[176:179]\n"
"v_mfma_f32_16x16x32_bf16 v[176:179], a[224:227], a[132:135], v[176:179]\n"
"v_mfma_f32_16x16x32_bf16 v[184:187], a[224:227], a[136:139], v[184:187]\n"
"v_mfma_f32_16x16x32_bf16 v[184:187], a[228:231], a[152:155], v[184:187]\n"
"v_mfma_f32_16x16x32_bf16 v[192:195], a[228:231], a[156:159], v[192:195]\n"
"s_waitcnt vmcnt(11) lgkmcnt(0)\n"
"s_barrier\n"
"v_mfma_f32_16x16x32_bf16 v[192:195], a[224:227], a[140:143], v[192:195]\n"
"buffer_load_dwordx4 v248, s[44:47], s80 offen offset:3072 nt lds\n"
"v_mfma_f32_16x16x32_bf16 v[196:199], a[232:235], a[140:143], v[196:199]\n"
"s_add_u32 m0, s79, 0x20700\n"
"s_add_u32 s80, s9, s29\n"
"ds_read_b64_tr_b16 a[228:229], v230 offset:512\n"
"v_mfma_f32_16x16x32_bf16 v[196:199], a[236:239], a[156:159], v[196:199]\n"
"s_addk_i32 s9, 0x1000\n"
"ds_read_b64_tr_b16 a[224:225], v230 offset:0\n"
"v_mfma_f32_16x16x32_bf16 v[188:191], a[236:239], a[152:155], v[188:191]\n"
"s_bitset0_b32 s9, 15\n"
"ds_read_b64_tr_b16 a[226:227], v230 offset:256\n"
"v_mfma_f32_16x16x32_bf16 v[188:191], a[232:235], a[136:139], v[188:191]\n"
"ds_read_b64_tr_b16 a[230:231], v230 offset:768\n"
"v_mfma_f32_16x16x32_bf16 v[180:183], a[232:235], a[132:135], v[180:183]\n"
"PG3_%=:\n"
"s_cbranch_execz PGB0_%=\n"
"v_mfma_f32_16x16x32_bf16 v[180:183], a[236:239], a[148:151], v[180:183]\n"
"v_mfma_f32_16x16x32_bf16 v[172:175], a[236:239], a[144:147], v[172:175]\n"
"ds_read_b64_tr_b16 a[236:237], v230 offset:520\n"
"v_mfma_f32_16x16x32_bf16 v[172:175], a[232:235], a[128:131], v[172:175]\n"
"ds_read_b64_tr_b16 a[238:239], v230 offset:776\n"
"v_mfma_f32_16x16x32_bf16 v[200:203], a[240:243], a[128:131], v[200:203]\n"
"ds_read_b64_tr_b16 a[232:233], v230 offset:8\n"
"v_mfma_f32_16x16x32_bf16 v[200:203], a[244:247], a[144:147], v[200:203]\n"
"buffer_load_dwordx4 v226, s[40:43], s81 offen offset:0 lds\n"
"v_mfma_f32_16x16x32_bf16 v[208:211], a[244:247], a[148:151], v[208:211]\n"
"ds_read_b64_tr_b16 a[234:235], v230 offset:264\n"
"v_mfma_f32_16x16x32_bf16 v[208:211], a[240:243], a[132:135], v[208:211]\n"
"v_mfma_f32_16x16x32_bf16 v[216:219], a[240:243], a[136:139], v[216:219]\n"
"ds_read_b64_tr_b16 a[240:241], v231 offset:0\n"
"v_mfma_f32_16x16x32_bf16 v[216:219], a[244:247], a[152:155], v[216:219]\n"
"ds_read_b64_tr_b16 a[242:243], v231 offset:256\n"
"v_mfma_f32_16x16x32_bf16 v[220:223], a[252:255], a[152:155], v[220:223]\n"
"buffer_load_dwordx4 v227, s[40:43], s81 offen offset:1024 lds\n"
"v_mfma_f32_16x16x32_bf16 v[220:223], a[248:251], a[136:139], v[220:223]\n"
"s_addk_i32 m0, 0x808\n"
"ds_read_b64_tr_b16 a[244:245], v231 offset:512\n"
"v_mfma_f32_16x16x32_bf16 v[212:215], a[248:251], a[132:135], v[212:215]\n"
"ds_read_b64_tr_b16 a[246:247], v231 offset:768\n"
"v_mfma_f32_16x16x32_bf16 v[212:215], a[252:255], a[148:151], v[212:215]\n"
"v_mfma_f32_16x16x32_bf16 v[204:207], a[252:255], a[144:147], v[204:207]\n"
"buffer_load_dwordx4 v228, s[40:43], s81 offen offset:0 lds\n"
"v_mfma_f32_16x16x32_bf16 v[204:207], a[248:251], a[128:131], v[204:207]\n"
"s_waitcnt lgkmcnt(6)\n"
"ds_read_b64_tr_b16 a[252:253], v231 offset:520\n"
"v_mfma_f32_16x16x32_bf16 v[0:3], a[224:227], a[160:163], v[0:3]\n"
"ds_read_b64_tr_b16 a[254:255], v231 offset:776\n"
"v_mfma_f32_16x16x32_bf16 v[0:3], a[228:231], a[176:179], v[0:3]\n"
"ds_read_b64_tr_b16 a[248:249], v231 offset:8\n"
"v_mfma_f32_16x16x32_bf16 v[8:11], a[228:231], a[180:183], v[8:11]\n"
"ds_read_b64_tr_b16 a[250:251], v231 offset:264\n"
"v_mfma_f32_16x16x32_bf16 v[8:11], a[224:227], a[164:167], v[8:11]\n"
"buffer_load_dwordx4 v229, s[40:43], s81 offen offset:1024 lds\n"
"v_mfma_f32_16x16x32_bf16 v[16:19], a[224:227], a[168:171], v[16:19]\n"
"s_add_u32 m0, s79, 0x0\n"
"s_lshl_b32 s81, s96, 12\n"
"v_mfma_f32_16x16x32_bf16 v[16:19], a[228:231], a[184:187], v[16:19]\n"
"s_add_u32 s81, s81, 0x100\n"
"s_bfe_u32 s96, s78, 0x1000f\n"
"v_mfma_f32_16x16x32_bf16 v[24:27], a[228:231], a[188:191], v[24:27]\n"
"s_lshl_b32 s96, s96, 10\n"
"ds_read_b64_tr_b16 a[228:229], v230 offset:640\n"
"v_mfma_f32_16x16x32_bf16 v[24:27], a[224:227], a[172:175], v[24:27]\n"
"s_waitcnt lgkmcnt(6)\n"
"buffer_load_dwordx4 v248, s[44:47], s80 offen offset:0 nt lds\n"
"v_mfma_f32_16x16x32_bf16 v[28:31], a[232:235], a[172:175], v[28:31]\n"
"s_add_u32 s96, s96, 149472\n"
"ds_read_b64_tr_b16 a[224:225], v230 offset:128\n"
"v_mfma_f32_16x16x32_bf16 v[28:31], a[236:239], a[188:191], v[28:31]\n"
"ds_read_b64_tr_b16 a[226:227], v230 offset:384\n"
"v_mfma_f32_16x16x32_bf16 v[20:23], a[236:239], a[184:187], v[20:23]\n"
"ds_read_b64_tr_b16 a[230:231], v230 offset:896\n"
"v_mfma_f32_16x16x32_bf16 v[20:23], a[232:235], a[168:171], v[20:23]\n"
"v_mfma_f32_16x16x32_bf16 v[12:15], a[232:235], a[164:167], v[12:15]\n"
"v_mfma_f32_16x16x32_bf16 v[12:15], a[236:239], a[180:183], v[12:15]\n"
"v_mfma_f32_16x16x32_bf16 v[4:7], a[236:239], a[176:179], v[4:7]\n"
"ds_read_b64_tr_b16 a[236:237], v230 offset:648\n"
"v_mfma_f32_16x16x32_bf16 v[4:7], a[232:235], a[160:163], v[4:7]\n"
"ds_read_b64_tr_b16 a[232:233], v230 offset:136\n"
"v_mfma_f32_16x16x32_bf16 v[32:35], a[240:243], a[160:163], v[32:35]\n"
"s_waitcnt lgkmcnt(6)\n"
"ds_read_b64_tr_b16 a[234:235], v230 offset:392\n"
"v_mfma_f32_16x16x32_bf16 v[32:35], a[244:247], a[176:179], v[32:35]\n"
"buffer_load_dwordx4 v248, s[44:47], s80 offen offset:1024 nt lds\n"
"v_mfma_f32_16x16x32_bf16 v[40:43], a[244:247], a[180:183], v[40:43]\n"
"s_addk_i32 m0, 0x20\n"
"ds_read_b64_tr_b16 a[238:239], v230 offset:904\n"
"v_add_u32 v230, 0xfffebba0, v230\n"
"v_mfma_f32_16x16x32_bf16 v[40:43], a[240:243], a[164:167], v[40:43]\n"
"v_mfma_f32_16x16x32_bf16 v[48:51], a[240:243], a[168:171], v[48:51]\n"
"ds_read_b64_tr_b16 a[240:241], v231 offset:128\n"
"v_mfma_f32_16x16x32_bf16 v[48:51], a[244:247], a[184:187], v[48:51]\n"
"ds_read_b64_tr_b16 a[242:243], v231 offset:384\n"
"v_mfma_f32_16x16x32_bf16 v[52:55], a[252:255], a[184:187], v[52:55]\n"
"ds_read_b64_tr_b16 a[244:245], v231 offset:640\n"
"v_mfma_f32_16x16x32_bf16 v[52:55], a[248:251], a[168:171], v[52:55]\n"
"ds_read_b64_tr_b16 a[246:247], v231 offset:896\n"
"v_mfma_f32_16x16x32_bf16 v[44:47], a[248:251], a[164:167], v[44:47]\n"
"v_mfma_f32_16x16x32_bf16 v[44:47], a[252:255], a[180:183], v[44:47]\n"
"v_mfma_f32_16x16x32_bf16 v[36:39], a[252:255], a[176:179], v[36:39]\n"
"buffer_load_dwordx4 v248, s[44:47], s80 offen offset:2048 nt lds\n"
"v_mfma_f32_16x16x32_bf16 v[36:39], a[248:251], a[160:163], v[36:39]\n"
"s_waitcnt lgkmcnt(6)\n"
"ds_read_b64_tr_b16 a[252:253], v231 offset:648\n"
"v_mfma_f32_16x16x32_bf16 v[56:59], a[224:227], a[160:163], v[56:59]\n"
"ds_read_b64_tr_b16 a[254:255], v231 offset:904\n"
"v_mfma_f32_16x16x32_bf16 v[56:59], a[228:231], a[176:179], v[56:59]\n"
"ds_read_b64_tr_b16 a[248:249], v231 offset:136\n"
"v_mfma_f32_16x16x32_bf16 v[64:67], a[228:231], a[180:183], v[64:67]\n"
"ds_read_b64_tr_b16 a[250:251], v231 offset:392\n"
"v_add_u32 v231, 0xfffebba0, v231\n"
"v_mfma_f32_16x16x32_bf16 v[64:67], a[224:227], a[164:167], v[64:67]\n"
"v_mfma_f32_16x16x32_bf16 v[72:75], a[224:227], a[168:171], v[72:75]\n"
"v_mfma_f32_16x16x32_bf16 v[72:75], a[228:231], a[184:187], v[72:75]\n"
"v_mfma_f32_16x16x32_bf16 v[80:83], a[228:231], a[188:191], v[80:83]\n"
"v_mfma_f32_16x16x32_bf16 v[80:83], a[224:227], a[172:175], v[80:83]\n"
"s_waitcnt vmcnt(11) lgkmcnt(0)\n"
"s_barrier\n"
"v_mfma_f32_16x16x32_bf16 v[84:87], a[232:235], a[172:175], v[84:87]\n"
"buffer_load_dwordx4 v248, s[44:47], s80 offen offset:3072 nt lds\n"
"v_mfma_f32_16x16x32_bf16 v[84:87], a[236:239], a[188:191], v[84:87]\n"
"s_add_u32 m0, s79, 0xc2a0\n"
"s_add_u32 s80, s10, s29\n"
"ds_read_b64_tr_b16 a[228:229], v230 offset:512\n"
"v_mfma_f32_16x16x32_bf16 v[76:79], a[236:239], a[184:187], v[76:79]\n"
"s_addk_i32 s10, 0x1000\n"
"ds_read_b64_tr_b16 a[230:231], v230 offset:768\n"
"v_mfma_f32_16x16x32_bf16 v[76:79], a[232:235], a[168:171], v[76:79]\n"
"s_bitset0_b32 s10, 15\n"
"ds_read_b64_tr_b16 a[224:225], v230 offset:0\n"
"v_mfma_f32_16x16x32_bf16 v[68:71], a[232:235], a[164:167], v[68:71]\n"
"ds_read_b64_tr_b16 a[226:227], v230 offset:256\n"
"v_mfma_f32_16x16x32_bf16 v[68:71], a[236:239], a[180:183], v[68:71]\n"
"v_mfma_f32_16x16x32_bf16 v[60:63], a[236:239], a[176:179], v[60:63]\n"
"ds_read_b64_tr_b16 a[236:237], v230 offset:520\n"
"v_mfma_f32_16x16x32_bf16 v[60:63], a[232:235], a[160:163], v[60:63]\n"
"ds_read_b64_tr_b16 a[232:233], v230 offset:8\n"
"v_mfma_f32_16x16x32_bf16 v[88:91], a[240:243], a[160:163], v[88:91]\n"
"ds_read_b64_tr_b16 a[234:235], v230 offset:264\n"
"v_mfma_f32_16x16x32_bf16 v[88:91], a[244:247], a[176:179], v[88:91]\n"
"buffer_load_dwordx4 v226, s[40:43], s81 offen offset:0 lds\n"
"v_mfma_f32_16x16x32_bf16 v[96:99], a[244:247], a[180:183], v[96:99]\n"
"ds_read_b64_tr_b16 a[238:239], v230 offset:776\n"
"v_mov_b32 v226, v246\n"
"v_mfma_f32_16x16x32_bf16 v[96:99], a[240:243], a[164:167], v[96:99]\n"
"v_add_u32 v246, 0x10380, v246\n"
"v_add_u32 v226, 0x18540, v226\n"
"v_mfma_f32_16x16x32_bf16 v[104:107], a[240:243], a[168:171], v[104:107]\n"
"ds_read_b64_tr_b16 a[240:241], v231 offset:0\n"
"v_mfma_f32_16x16x32_bf16 v[104:107], a[244:247], a[184:187], v[104:107]\n"
"ds_read_b64_tr_b16 a[242:243], v231 offset:256\n"
"v_mfma_f32_16x16x32_bf16 v[108:111], a[252:255], a[184:187], v[108:111]\n"
"buffer_load_dwordx4 v227, s[40:43], s81 offen offset:1024 lds\n"
"v_mfma_f32_16x16x32_bf16 v[108:111], a[248:251], a[168:171], v[108:111]\n"
"s_addk_i32 m0, 0x808\n"
"ds_read_b64_tr_b16 a[244:245], v231 offset:512\n"
"v_mov_b32 v227, v247\n"
"v_mfma_f32_16x16x32_bf16 v[100:103], a[248:251], a[164:167], v[100:103]\n"
"ds_read_b64_tr_b16 a[246:247], v231 offset:768\n"
"v_add_u32 v247, 0x10380, v247\n"
"v_mfma_f32_16x16x32_bf16 v[100:103], a[252:255], a[180:183], v[100:103]\n"
"v_add_u32 v227, 0x18540, v227\n"
"v_mfma_f32_16x16x32_bf16 v[92:95], a[252:255], a[176:179], v[92:95]\n"
"ds_read_b64_tr_b16 a[252:253], v231 offset:520\n"
"v_mfma_f32_16x16x32_bf16 v[92:95], a[248:251], a[160:163], v[92:95]\n"
"s_waitcnt lgkmcnt(7)\n"
"buffer_load_dwordx4 v228, s[40:43], s81 offen offset:0 lds\n"
"v_mfma_f32_16x16x32_bf16 v[112:115], a[224:227], a[160:163], v[112:115]\n"
"ds_read_b64_tr_b16 a[254:255], v231 offset:776\n"
"v_mfma_f32_16x16x32_bf16 v[112:115], a[228:231], a[176:179], v[112:115]\n"
"ds_read_b64_tr_b16 a[248:249], v231 offset:8\n"
"v_mfma_f32_16x16x32_bf16 v[120:123], a[228:231], a[180:183], v[120:123]\n"
"ds_read_b64_tr_b16 a[250:251], v231 offset:264\n"
"v_mfma_f32_16x16x32_bf16 v[120:123], a[224:227], a[164:167], v[120:123]\n"
"v_mfma_f32_16x16x32_bf16 v[128:131], a[224:227], a[168:171], v[128:131]\n"
"buffer_load_dwordx4 v229, s[40:43], s81 offen offset:1024 lds\n"
"v_mfma_f32_16x16x32_bf16 v[128:131], a[228:231], a[184:187], v[128:131]\n"
"s_add_u32 m0, s79, 0x10380\n"
"s_add_u32 s81, s10, s29\n"
"v_mfma_f32_16x16x32_bf16 v[136:139], a[228:231], a[188:191], v[136:139]\n"
"s_addk_i32 s10, 0x1000\n"
"ds_read_b64_tr_b16 a[228:229], v230 offset:640\n"
"v_mfma_f32_16x16x32_bf16 v[136:139], a[224:227], a[172:175], v[136:139]\n"
"s_waitcnt lgkmcnt(6)\n"
"s_bitset0_b32 s10, 15\n"
"ds_read_b64_tr_b16 a[224:225], v230 offset:128\n"
"v_mfma_f32_16x16x32_bf16 v[140:143], a[232:235], a[172:175], v[140:143]\n"
"ds_read_b64_tr_b16 a[226:227], v230 offset:384\n"
"v_mfma_f32_16x16x32_bf16 v[140:143], a[236:239], a[188:191], v[140:143]\n"
"buffer_load_dwordx4 v248, s[48:51], s80 offen offset:0 nt lds\n"
"v_mfma_f32_16x16x32_bf16 v[132:135], a[236:239], a[184:187], v[132:135]\n"
"ds_read_b64_tr_b16 a[230:231], v230 offset:896\n"
"v_mfma_f32_16x16x32_bf16 v[132:135], a[232:235], a[168:171], v[132:135]\n"
"v_mfma_f32_16x16x32_bf16 v[124:127], a[232:235], a[164:167], v[124:127]\n"
"v_mfma_f32_16x16x32_bf16 v[124:127], a[236:239], a[180:183], v[124:127]\n"
"v_mfma_f32_16x16x32_bf16 v[116:119], a[236:239], a[176:179], v[116:119]\n"
"ds_read_b64_tr_b16 a[236:237], v230 offset:648\n"
"v_mfma_f32_16x16x32_bf16 v[116:119], a[232:235], a[160:163], v[116:119]\n"
"ds_read_b64_tr_b16 a[232:233], v230 offset:136\n"
"v_mfma_f32_16x16x32_bf16 v[144:147], a[240:243], a[160:163], v[144:147]\n"
"s_waitcnt lgkmcnt(6)\n"
"ds_read_b64_tr_b16 a[234:235], v230 offset:392\n"
"v_mfma_f32_16x16x32_bf16 v[144:147], a[244:247], a[176:179], v[144:147]\n"
"ds_read_b64_tr_b16 a[238:239], v230 offset:904\n"
"v_add_u32 v230, 0x1c620, v230\n"
"v_mfma_f32_16x16x32_bf16 v[152:155], a[244:247], a[180:183], v[152:155]\n"
"v_mfma_f32_16x16x32_bf16 v[152:155], a[240:243], a[164:167], v[152:155]\n"
"v_mfma_f32_16x16x32_bf16 v[160:163], a[240:243], a[168:171], v[160:163]\n"
"ds_read_b64_tr_b16 a[240:241], v231 offset:128\n"
"v_mfma_f32_16x16x32_bf16 v[160:163], a[244:247], a[184:187], v[160:163]\n"
"ds_read_b64_tr_b16 a[242:243], v231 offset:384\n"
"v_mfma_f32_16x16x32_bf16 v[164:167], a[252:255], a[184:187], v[164:167]\n"
"ds_read_b64_tr_b16 a[244:245], v231 offset:640\n"
"v_mfma_f32_16x16x32_bf16 v[164:167], a[248:251], a[168:171], v[164:167]\n"
"ds_read_b64_tr_b16 a[246:247], v231 offset:896\n"
"v_mfma_f32_16x16x32_bf16 v[156:159], a[248:251], a[164:167], v[156:159]\n"
"buffer_load_dwordx4 v248, s[48:51], s80 offen offset:1024 nt lds\n"
"v_mfma_f32_16x16x32_bf16 v[156:159], a[252:255], a[180:183], v[156:159]\n"
"s_addk_i32 m0, 0x20\n"
"v_mfma_f32_16x16x32_bf16 v[148:151], a[252:255], a[176:179], v[148:151]\n"
"ds_read_b64_tr_b16 a[252:253], v231 offset:648\n"
"v_mfma_f32_16x16x32_bf16 v[148:151], a[248:251], a[160:163], v[148:151]\n"
"s_waitcnt lgkmcnt(7)\n"
"ds_read_b64_tr_b16 a[254:255], v231 offset:904\n"
"v_mfma_f32_16x16x32_bf16 v[168:171], a[224:227], a[160:163], v[168:171]\n"
"ds_read_b64_tr_b16 a[248:249], v231 offset:136\n"
"v_mfma_f32_16x16x32_bf16 v[168:171], a[228:231], a[176:179], v[168:171]\n"
"ds_read_b64_tr_b16 a[250:251], v231 offset:392\n"
"v_add_u32 v231, 0x1c620, v231\n"
"v_mfma_f32_16x16x32_bf16 v[176:179], a[228:231], a[180:183], v[176:179]\n"
"v_mfma_f32_16x16x32_bf16 v[176:179], a[224:227], a[164:167], v[176:179]\n"
"v_mfma_f32_16x16x32_bf16 v[184:187], a[224:227], a[168:171], v[184:187]\n"
"v_mfma_f32_16x16x32_bf16 v[184:187], a[228:231], a[184:187], v[184:187]\n"
"v_mfma_f32_16x16x32_bf16 v[192:195], a[228:231], a[188:191], v[192:195]\n"
"s_waitcnt vmcnt(10) lgkmcnt(0)\n"
"s_barrier\n"
"v_mfma_f32_16x16x32_bf16 v[192:195], a[224:227], a[172:175], v[192:195]\n"
"ds_read_b64_tr_b16 a[228:229], v230 offset:512\n"
"v_mfma_f32_16x16x32_bf16 v[196:199], a[232:235], a[172:175], v[196:199]\n"
"ds_read_b64_tr_b16 a[224:225], v230 offset:0\n"
"v_mfma_f32_16x16x32_bf16 v[196:199], a[236:239], a[188:191], v[196:199]\n"
"ds_read_b64_tr_b16 a[226:227], v230 offset:256\n"
"v_mfma_f32_16x16x32_bf16 v[188:191], a[236:239], a[184:187], v[188:191]\n"
"ds_read_b64_tr_b16 a[230:231], v230 offset:768\n"
"v_mfma_f32_16x16x32_bf16 v[188:191], a[232:235], a[168:171], v[188:191]\n"
"buffer_load_dwordx4 v248, s[48:51], s80 offen offset:2048 nt lds\n"
"v_mfma_f32_16x16x32_bf16 v[180:183], a[232:235], a[164:167], v[180:183]\n"
"v_mfma_f32_16x16x32_bf16 v[180:183], a[236:239], a[180:183], v[180:183]\n"
"v_mfma_f32_16x16x32_bf16 v[172:175], a[236:239], a[176:179], v[172:175]\n"
"ds_read_b64_tr_b16 a[236:237], v230 offset:520\n"
"v_mfma_f32_16x16x32_bf16 v[172:175], a[232:235], a[160:163], v[172:175]\n"
"ds_read_b64_tr_b16 a[232:233], v230 offset:8\n"
"v_mfma_f32_16x16x32_bf16 v[200:203], a[240:243], a[160:163], v[200:203]\n"
"ds_read_b64_tr_b16 a[234:235], v230 offset:264\n"
"v_mfma_f32_16x16x32_bf16 v[200:203], a[244:247], a[176:179], v[200:203]\n"
"ds_read_b64_tr_b16 a[238:239], v230 offset:776\n"
"v_mfma_f32_16x16x32_bf16 v[208:211], a[244:247], a[180:183], v[208:211]\n"
"v_mfma_f32_16x16x32_bf16 v[208:211], a[240:243], a[164:167], v[208:211]\n"
"v_mfma_f32_16x16x32_bf16 v[216:219], a[240:243], a[168:171], v[216:219]\n"
"ds_read_b64_tr_b16 a[240:241], v230 offset:128\n"
"v_mfma_f32_16x16x32_bf16 v[216:219], a[244:247], a[184:187], v[216:219]\n"
"ds_read_b64_tr_b16 a[242:243], v230 offset:384\n"
"v_mfma_f32_16x16x32_bf16 v[220:223], a[252:255], a[184:187], v[220:223]\n"
"ds_read_b64_tr_b16 a[244:245], v230 offset:640\n"
"v_mfma_f32_16x16x32_bf16 v[220:223], a[248:251], a[168:171], v[220:223]\n"
"ds_read_b64_tr_b16 a[246:247], v230 offset:896\n"
"v_mfma_f32_16x16x32_bf16 v[212:215], a[248:251], a[164:167], v[212:215]\n"
"v_mfma_f32_16x16x32_bf16 v[212:215], a[252:255], a[180:183], v[212:215]\n"
"buffer_load_dwordx4 v248, s[48:51], s80 offen offset:3072 nt lds\n"
"v_mfma_f32_16x16x32_bf16 v[204:207], a[252:255], a[176:179], v[204:207]\n"
"s_add_u32 m0, s79, 0x18540\n"
"s_add_u32 s80, s9, s29\n"
"ds_read_b64_tr_b16 a[252:253], v230 offset:648\n"
"v_mfma_f32_16x16x32_bf16 v[204:207], a[248:251], a[160:163], v[204:207]\n"
"s_waitcnt lgkmcnt(7)\n"
"s_addk_i32 s9, 0x1000\n"
"ds_read_b64_tr_b16 a[248:249], v230 offset:136\n"
"v_mfma_f32_16x16x32_bf16 v[0:3], a[224:227], a[192:195], v[0:3]\n"
"s_bitset0_b32 s9, 15\n"
"ds_read_b64_tr_b16 a[250:251], v230 offset:392\n"
"v_mfma_f32_16x16x32_bf16 v[0:3], a[228:231], a[208:211], v[0:3]\n"
"ds_read_b64_tr_b16 a[254:255], v230 offset:904\n"
"v_add_u32 v230, 0xfffebba0, v230\n"
"v_mfma_f32_16x16x32_bf16 v[8:11], a[228:231], a[212:215], v[8:11]\n"
"v_mfma_f32_16x16x32_bf16 v[8:11], a[224:227], a[196:199], v[8:11]\n"
"v_mfma_f32_16x16x32_bf16 v[16:19], a[224:227], a[200:203], v[16:19]\n"
"v_mfma_f32_16x16x32_bf16 v[16:19], a[228:231], a[216:219], v[16:19]\n"
"v_mfma_f32_16x16x32_bf16 v[24:27], a[228:231], a[220:223], v[24:27]\n"
"ds_read_b64_tr_b16 a[228:229], v231 offset:512\n"
"v_cvt_pk_bf16_f32 v0, v0, v1\n"
"v_mfma_f32_16x16x32_bf16 v[24:27], a[224:227], a[204:207], v[24:27]\n"
"s_waitcnt lgkmcnt(5)\n"
"ds_read_b64_tr_b16 a[230:231], v231 offset:768\n"
"v_cvt_pk_bf16_f32 v1, v2, v3\n"
"v_mfma_f32_16x16x32_bf16 v[28:31], a[232:235], a[204:207], v[28:31]\n"
"ds_read_b64_tr_b16 a[224:225], v231 offset:0\n"
"v_cvt_pk_bf16_f32 v8, v8, v9\n"
"v_mfma_f32_16x16x32_bf16 v[28:31], a[236:239], a[220:223], v[28:31]\n"
"ds_read_b64_tr_b16 a[226:227], v231 offset:256\n"
"v_cvt_pk_bf16_f32 v9, v10, v11\n"
"v_mfma_f32_16x16x32_bf16 v[20:23], a[236:239], a[216:219], v[20:23]\n"
"v_cvt_pk_bf16_f32 v16, v16, v17\n"
"v_cvt_pk_bf16_f32 v24, v24, v25\n"
"v_mfma_f32_16x16x32_bf16 v[20:23], a[232:235], a[200:203], v[20:23]\n"
"v_cvt_pk_bf16_f32 v17, v18, v19\n"
"v_cvt_pk_bf16_f32 v25, v26, v27\n"
"v_mfma_f32_16x16x32_bf16 v[12:15], a[232:235], a[196:199], v[12:15]\n"
"v_cvt_pk_bf16_f32 v26, v28, v29\n"
"v_cvt_pk_bf16_f32 v27, v30, v31\n"
"v_mfma_f32_16x16x32_bf16 v[12:15], a[236:239], a[212:215], v[12:15]\n"
"v_accvgpr_write_b32 a0, v0\n"
"v_accvgpr_write_b32 a1, v1\n"
"v_mfma_f32_16x16x32_bf16 v[4:7], a[236:239], a[208:211], v[4:7]\n"
"ds_read_b64_tr_b16 a[236:237], v231 offset:520\n"
"v_cvt_pk_bf16_f32 v18, v20, v21\n"
"v_mfma_f32_16x16x32_bf16 v[4:7], a[232:235], a[192:195], v[4:7]\n"
"ds_read_b64_tr_b16 a[238:239], v231 offset:776\n"
"v_cvt_pk_bf16_f32 v19, v22, v23\n"
"v_mfma_f32_16x16x32_bf16 v[56:59], a[240:243], a[192:195], v[56:59]\n"
"ds_read_b64_tr_b16 a[232:233], v231 offset:8\n"
"v_cvt_pk_bf16_f32 v10, v12, v13\n"
"v_mfma_f32_16x16x32_bf16 v[56:59], a[244:247], a[208:211], v[56:59]\n"
"ds_read_b64_tr_b16 a[234:235], v231 offset:264\n"
"v_cvt_pk_bf16_f32 v11, v14, v15\n"
"v_mfma_f32_16x16x32_bf16 v[64:67], a[244:247], a[212:215], v[64:67]\n"
"buffer_load_dwordx4 v248, s[48:51], s81 offen offset:0 nt lds\n"
"v_mfma_f32_16x16x32_bf16 v[64:67], a[240:243], a[196:199], v[64:67]\n"
"v_cvt_pk_bf16_f32 v2, v4, v5\n"
"v_cvt_pk_bf16_f32 v3, v6, v7\n"
"v_mfma_f32_16x16x32_bf16 v[72:75], a[240:243], a[200:203], v[72:75]\n"
"v_cvt_pk_bf16_f32 v56, v56, v57\n"
"v_accvgpr_write_b32 a2, v2\n"
"v_mfma_f32_16x16x32_bf16 v[72:75], a[244:247], a[216:219], v[72:75]\n"
"v_cvt_pk_bf16_f32 v57, v58, v59\n"
"v_accvgpr_write_b32 a3, v3\n"
"v_mfma_f32_16x16x32_bf16 v[80:83], a[244:247], a[220:223], v[80:83]\n"
"ds_read_b64_tr_b16 a[244:245], v231 offset:640\n"
"v_cvt_pk_bf16_f32 v64, v64, v65\n"
"v_mfma_f32_16x16x32_bf16 v[80:83], a[240:243], a[204:207], v[80:83]\n"
"s_waitcnt lgkmcnt(5)\n"
"ds_read_b64_tr_b16 a[246:247], v231 offset:896\n"
"v_cvt_pk_bf16_f32 v65, v66, v67\n"
"v_mfma_f32_16x16x32_bf16 v[84:87], a[248:251], a[204:207], v[84:87]\n"
"ds_read_b64_tr_b16 a[240:241], v231 offset:128\n"
"v_cvt_pk_bf16_f32 v72, v72, v73\n"
"v_mfma_f32_16x16x32_bf16 v[84:87], a[252:255], a[220:223], v[84:87]\n"
"ds_read_b64_tr_b16 a[242:243], v231 offset:384\n"
"v_cvt_pk_bf16_f32 v73, v74, v75\n"
"v_mfma_f32_16x16x32_bf16 v[76:79], a[252:255], a[216:219], v[76:79]\n"
"v_cvt_pk_bf16_f32 v80, v80, v81\n"
"v_accvgpr_write_b32 a8, v8\n"
"v_mfma_f32_16x16x32_bf16 v[76:79], a[248:251], a[200:203], v[76:79]\n"
"v_cvt_pk_bf16_f32 v81, v82, v83\n"
"v_accvgpr_write_b32 a9, v9\n"
"v_mfma_f32_16x16x32_bf16 v[68:71], a[248:251], a[196:199], v[68:71]\n"
"v_cvt_pk_bf16_f32 v82, v84, v85\n"
"v_cvt_pk_bf16_f32 v83, v86, v87\n"
"v_mfma_f32_16x16x32_bf16 v[68:71], a[252:255], a[212:215], v[68:71]\n"
"v_accvgpr_write_b32 a10, v10\n"
"v_accvgpr_write_b32 a11, v11\n"
"v_mfma_f32_16x16x32_bf16 v[60:63], a[252:255], a[208:211], v[60:63]\n"
"ds_read_b64_tr_b16 a[252:253], v231 offset:648\n"
"v_cvt_pk_bf16_f32 v74, v76, v77\n"
"v_mfma_f32_16x16x32_bf16 v[60:63], a[248:251], a[192:195], v[60:63]\n"
"ds_read_b64_tr_b16 a[254:255], v231 offset:904\n"
"v_cvt_pk_bf16_f32 v75, v78, v79\n"
"v_mfma_f32_16x16x32_bf16 v[32:35], a[224:227], a[192:195], v[32:35]\n"
"buffer_load_dwordx4 v248, s[48:51], s81 offen offset:1024 nt lds\n"
"v_mfma_f32_16x16x32_bf16 v[32:35], a[228:231], a[208:211], v[32:35]\n"
"s_addk_i32 m0, 0x20\n"
"ds_read_b64_tr_b16 a[248:249], v231 offset:136\n"
"v_cvt_pk_bf16_f32 v66, v68, v69\n"
"v_mfma_f32_16x16x32_bf16 v[40:43], a[228:231], a[212:215], v[40:43]\n"
"ds_read_b64_tr_b16 a[250:251], v231 offset:392\n"
"v_cvt_pk_bf16_f32 v58, v60, v61\n"
"v_mfma_f32_16x16x32_bf16 v[40:43], a[224:227], a[196:199], v[40:43]\n"
"v_add_u32 v231, 0xfffebba0, v231\n"
"v_cvt_pk_bf16_f32 v59, v62, v63\n"
"v_mfma_f32_16x16x32_bf16 v[48:51], a[224:227], a[200:203], v[48:51]\n"
"v_cvt_pk_bf16_f32 v67, v70, v71\n"
"v_cvt_pk_bf16_f32 v32, v32, v33\n"
"v_mfma_f32_16x16x32_bf16 v[48:51], a[228:231], a[216:219], v[48:51]\n"
"s_waitcnt lgkmcnt(4)\n"
"v_cvt_pk_bf16_f32 v33, v34, v35\n"
"v_accvgpr_write_b32 a32, v32\n"
"v_mfma_f32_16x16x32_bf16 v[52:55], a[236:239], a[216:219], v[52:55]\n"
"v_cvt_pk_bf16_f32 v40, v40, v41\n"
"v_accvgpr_write_b32 a33, v33\n"
"v_mfma_f32_16x16x32_bf16 v[52:55], a[232:235], a[200:203], v[52:55]\n"
"s_waitcnt vmcnt(6) lgkmcnt(0)\n"
"s_barrier\n"
"v_mfma_f32_16x16x32_bf16 v[44:47], a[232:235], a[196:199], v[44:47]\n"
"ds_read_b64_tr_b16 a[224:225], v230 offset:0\n"
"v_cvt_pk_bf16_f32 v41, v42, v43\n"
"v_mfma_f32_16x16x32_bf16 v[44:47], a[236:239], a[212:215], v[44:47]\n"
"ds_read_b64_tr_b16 a[226:227], v230 offset:256\n"
"v_cvt_pk_bf16_f32 v48, v48, v49\n"
"v_mfma_f32_16x16x32_bf16 v[36:39], a[236:239], a[208:211], v[36:39]\n"
"ds_read_b64_tr_b16 a[228:229], v230 offset:512\n"
"v_cvt_pk_bf16_f32 v49, v50, v51\n"
"v_mfma_f32_16x16x32_bf16 v[36:39], a[232:235], a[192:195], v[36:39]\n"
"ds_read_b64_tr_b16 a[230:231], v230 offset:768\n"
"v_cvt_pk_bf16_f32 v50, v52, v53\n"
"v_mfma_f32_16x16x32_bf16 v[88:91], a[240:243], a[192:195], v[88:91]\n"
"ds_read_b64_tr_b16 a[236:237], v230 offset:520\n"
"v_cvt_pk_bf16_f32 v42, v44, v45\n"
"v_mfma_f32_16x16x32_bf16 v[88:91], a[244:247], a[208:211], v[88:91]\n"
"ds_read_b64_tr_b16 a[232:233], v230 offset:8\n"
"v_cvt_pk_bf16_f32 v43, v46, v47\n"
"v_mfma_f32_16x16x32_bf16 v[96:99], a[244:247], a[212:215], v[96:99]\n"
"ds_read_b64_tr_b16 a[234:235], v230 offset:264\n"
"v_cvt_pk_bf16_f32 v34, v36, v37\n"
"v_mfma_f32_16x16x32_bf16 v[96:99], a[240:243], a[196:199], v[96:99]\n"
"buffer_load_dwordx4 v248, s[48:51], s81 offen offset:2048 nt lds\n"
"v_mfma_f32_16x16x32_bf16 v[104:107], a[240:243], a[200:203], v[104:107]\n"
"ds_read_b64_tr_b16 a[238:239], v230 offset:776\n"
"v_cvt_pk_bf16_f32 v35, v38, v39\n"
"v_mfma_f32_16x16x32_bf16 v[104:107], a[244:247], a[216:219], v[104:107]\n"
"ds_read_b64_tr_b16 a[240:241], v230 offset:128\n"
"v_cvt_pk_bf16_f32 v88, v88, v89\n"
"v_mfma_f32_16x16x32_bf16 v[108:111], a[252:255], a[216:219], v[108:111]\n"
"ds_read_b64_tr_b16 a[242:243], v230 offset:384\n"
"v_cvt_pk_bf16_f32 v89, v90, v91\n"
"v_mfma_f32_16x16x32_bf16 v[108:111], a[248:251], a[200:203], v[108:111]\n"
"ds_read_b64_tr_b16 a[244:245], v230 offset:640\n"
"v_cvt_pk_bf16_f32 v96, v96, v97\n"
"v_mfma_f32_16x16x32_bf16 v[100:103], a[248:251], a[196:199], v[100:103]\n"
"ds_read_b64_tr_b16 a[246:247], v230 offset:896\n"
"v_cvt_pk_bf16_f32 v97, v98, v99\n"
"v_mfma_f32_16x16x32_bf16 v[100:103], a[252:255], a[212:215], v[100:103]\n"
"ds_write_b128 v238, v[24:27] offset:0\n"
"v_cvt_pk_bf16_f32 v51, v54, v55\n"
"v_mfma_f32_16x16x32_bf16 v[92:95], a[252:255], a[208:211], v[92:95]\n"
"ds_read_b64_tr_b16 a[252:253], v230 offset:648\n"
"v_cvt_pk_bf16_f32 v104, v104, v105\n"
"v_mfma_f32_16x16x32_bf16 v[92:95], a[248:251], a[192:195], v[92:95]\n"
"s_waitcnt lgkmcnt(10)\n"
"ds_read_b64_tr_b16 a[248:249], v230 offset:136\n"
"v_cvt_pk_bf16_f32 v105, v106, v107\n"
"v_mfma_f32_16x16x32_bf16 v[112:115], a[224:227], a[192:195], v[112:115]\n"
"ds_read_b64_tr_b16 a[250:251], v230 offset:392\n"
"v_cvt_pk_bf16_f32 v98, v100, v101\n"
"v_mfma_f32_16x16x32_bf16 v[112:115], a[228:231], a[208:211], v[112:115]\n"
"ds_read_b64_tr_b16 a[254:255], v230 offset:904\n"
"v_cvt_pk_bf16_f32 v99, v102, v103\n"
"v_mfma_f32_16x16x32_bf16 v[120:123], a[228:231], a[212:215], v[120:123]\n"
"ds_write_b128 v238, v[80:83] offset:1024\n"
"v_cvt_pk_bf16_f32 v90, v92, v93\n"
"v_mfma_f32_16x16x32_bf16 v[120:123], a[224:227], a[196:199], v[120:123]\n"
"v_cvt_pk_bf16_f32 v91, v94, v95\n"
"v_cvt_pk_bf16_f32 v106, v108, v109\n"
"v_mfma_f32_16x16x32_bf16 v[128:131], a[224:227], a[200:203], v[128:131]\n"
"v_cvt_pk_bf16_f32 v112, v112, v113\n"
"v_cvt_pk_bf16_f32 v107, v110, v111\n"
"v_mfma_f32_16x16x32_bf16 v[128:131], a[228:231], a[216:219], v[128:131]\n"
"buffer_load_dwordx4 v248, s[48:51], s81 offen offset:3072 nt lds\n"
"v_mfma_f32_16x16x32_bf16 v[136:139], a[228:231], a[220:223], v[136:139]\n"
"s_add_u32 m0, s79, 0x20700\n"
"ds_read_b64_tr_b16 a[228:229], v231 offset:512\n"
"v_cvt_pk_bf16_f32 v113, v114, v115\n"
"v_mfma_f32_16x16x32_bf16 v[136:139], a[224:227], a[204:207], v[136:139]\n"
"s_waitcnt lgkmcnt(8)\n"
"s_add_u32 s81, s9, s29\n"
"ds_read_b64_tr_b16 a[224:225], v231 offset:0\n"
"v_mfma_f32_16x16x32_bf16 v[140:143], a[232:235], a[204:207], v[140:143]\n"
"s_addk_i32 s9, 0x1000\n"
"ds_read_b64_tr_b16 a[226:227], v231 offset:256\n"
"v_cvt_pk_bf16_f32 v120, v120, v121\n"
"v_mfma_f32_16x16x32_bf16 v[140:143], a[236:239], a[220:223], v[140:143]\n"
"s_bitset0_b32 s9, 15\n"
"ds_read_b64_tr_b16 a[230:231], v231 offset:768\n"
"v_cvt_pk_bf16_f32 v121, v122, v123\n"
"v_mfma_f32_16x16x32_bf16 v[132:135], a[236:239], a[216:219], v[132:135]\n"
"v_cvt_pk_bf16_f32 v128, v128, v129\n"
"v_cvt_pk_bf16_f32 v136, v136, v137\n"
"v_mfma_f32_16x16x32_bf16 v[132:135], a[232:235], a[200:203], v[132:135]\n"
"v_cvt_pk_bf16_f32 v129, v130, v131\n"
"v_cvt_pk_bf16_f32 v137, v138, v139\n"
"v_mfma_f32_16x16x32_bf16 v[124:127], a[232:235], a[196:199], v[124:127]\n"
"v_cvt_pk_bf16_f32 v138, v140, v141\n"
"v_cvt_pk_bf16_f32 v139, v142, v143\n"
"v_mfma_f32_16x16x32_bf16 v[124:127], a[236:239], a[212:215], v[124:127]\n"
"ds_write_b128 v238, v[136:139] offset:2048\n"
"v_accvgpr_write_b32 a34, v34\n"
"v_mfma_f32_16x16x32_bf16 v[116:119], a[236:239], a[208:211], v[116:119]\n"
"ds_read_b64_tr_b16 a[236:237], v231 offset:520\n"
"v_cvt_pk_bf16_f32 v130, v132, v133\n"
"v_mfma_f32_16x16x32_bf16 v[116:119], a[232:235], a[192:195], v[116:119]\n"
"ds_read_b64_tr_b16 a[238:239], v231 offset:776\n"
"v_cvt_pk_bf16_f32 v131, v134, v135\n"
"v_mfma_f32_16x16x32_bf16 v[168:171], a[240:243], a[192:195], v[168:171]\n"
"s_waitcnt lgkmcnt(7)\n"
"ds_read_b64_tr_b16 a[232:233], v231 offset:8\n"
"v_cvt_pk_bf16_f32 v122, v124, v125\n"
"v_mfma_f32_16x16x32_bf16 v[168:171], a[244:247], a[208:211], v[168:171]\n"
"ds_read_b64_tr_b16 a[234:235], v231 offset:264\n"
"v_cvt_pk_bf16_f32 v123, v126, v127\n"
"v_mfma_f32_16x16x32_bf16 v[176:179], a[244:247], a[212:215], v[176:179]\n"
"v_cvt_pk_bf16_f32 v114, v116, v117\n"
"v_cvt_pk_bf16_f32 v115, v118, v119\n"
"v_mfma_f32_16x16x32_bf16 v[176:179], a[240:243], a[196:199], v[176:179]\n"
"v_accvgpr_write_b32 a35, v35\n"
"v_accvgpr_write_b32 a40, v40\n"
"v_mfma_f32_16x16x32_bf16 v[184:187], a[240:243], a[200:203], v[184:187]\n"
"buffer_load_dwordx4 v248, s[44:47], s80 offen offset:0 nt lds\n"
"v_mfma_f32_16x16x32_bf16 v[184:187], a[244:247], a[216:219], v[184:187]\n"
"v_cvt_pk_bf16_f32 v168, v168, v169\n"
"v_accvgpr_write_b32 a41, v41\n"
"v_mfma_f32_16x16x32_bf16 v[192:195], a[244:247], a[220:223], v[192:195]\n"
"ds_read_b64_tr_b16 a[244:245], v231 offset:640\n"
"v_cvt_pk_bf16_f32 v169, v170, v171\n"
"v_mfma_f32_16x16x32_bf16 v[192:195], a[240:243], a[204:207], v[192:195]\n"
"ds_read_b64_tr_b16 a[240:241], v231 offset:128\n"
"v_cvt_pk_bf16_f32 v176, v176, v177\n"
"v_mfma_f32_16x16x32_bf16 v[196:199], a[248:251], a[204:207], v[196:199]\n"
"ds_read_b64_tr_b16 a[242:243], v231 offset:384\n"
"v_cvt_pk_bf16_f32 v177, v178, v179\n"
"v_mfma_f32_16x16x32_bf16 v[196:199], a[252:255], a[220:223], v[196:199]\n"
"ds_read_b64_tr_b16 a[246:247], v231 offset:896\n"
"v_cvt_pk_bf16_f32 v184, v184, v185\n"
"v_mfma_f32_16x16x32_bf16 v[188:191], a[252:255], a[216:219], v[188:191]\n"
"v_cvt_pk_bf16_f32 v185, v186, v187\n"
"v_cvt_pk_bf16_f32 v192, v192, v193\n"
"v_mfma_f32_16x16x32_bf16 v[188:191], a[248:251], a[200:203], v[188:191]\n"
"v_cvt_pk_bf16_f32 v193, v194, v195\n"
"v_accvgpr_write_b32 a42, v42\n"
"v_mfma_f32_16x16x32_bf16 v[180:183], a[248:251], a[196:199], v[180:183]\n"
"v_cvt_pk_bf16_f32 v194, v196, v197\n"
"v_cvt_pk_bf16_f32 v195, v198, v199\n"
"v_mfma_f32_16x16x32_bf16 v[180:183], a[252:255], a[212:215], v[180:183]\n"
"ds_write_b128 v238, v[192:195] offset:3072\n"
"v_accvgpr_write_b32 a43, v43\n"
"v_mfma_f32_16x16x32_bf16 v[172:175], a[252:255], a[208:211], v[172:175]\n"
"ds_read_b64_tr_b16 a[252:253], v231 offset:648\n"
"v_cvt_pk_bf16_f32 v186, v188, v189\n"
"v_mfma_f32_16x16x32_bf16 v[172:175], a[248:251], a[192:195], v[172:175]\n"
"s_waitcnt lgkmcnt(6)\n"
"ds_read_b64_tr_b16 a[254:255], v231 offset:904\n"
"v_cvt_pk_bf16_f32 v187, v190, v191\n"
"v_mfma_f32_16x16x32_bf16 v[144:147], a[224:227], a[192:195], v[144:147]\n"
"ds_read_b64_tr_b16 a[248:249], v231 offset:136\n"
"v_cvt_pk_bf16_f32 v178, v180, v181\n"
"v_mfma_f32_16x16x32_bf16 v[144:147], a[228:231], a[208:211], v[144:147]\n"
"ds_read_b64_tr_b16 a[250:251], v231 offset:392\n"
"v_cvt_pk_bf16_f32 v179, v182, v183\n"
"v_mfma_f32_16x16x32_bf16 v[152:155], a[228:231], a[212:215], v[152:155]\n"
"buffer_load_dwordx4 v248, s[44:47], s80 offen offset:1024 nt lds\n"
"v_mfma_f32_16x16x32_bf16 v[152:155], a[224:227], a[196:199], v[152:155]\n"
"s_addk_i32 m0, 0x20\n"
"v_cvt_pk_bf16_f32 v170, v172, v173\n"
"v_cvt_pk_bf16_f32 v171, v174, v175\n"
"v_mfma_f32_16x16x32_bf16 v[160:163], a[224:227], a[200:203], v[160:163]\n"
"PG4_%=:\n"
"s_cbranch_execz PGB0_%=\n"
"v_cvt_pk_bf16_f32 v144, v144, v145\n"
"v_accvgpr_write_b32 a16, v16\n"
"v_mfma_f32_16x16x32_bf16 v[160:163], a[228:231], a[216:219], v[160:163]\n"
"v_cvt_pk_bf16_f32 v145, v146, v147\n"
"v_accvgpr_write_b32 a17, v17\n"
"v_mfma_f32_16x16x32_bf16 v[164:167], a[236:239], a[216:219], v[164:167]\n"
"s_waitcnt lgkmcnt(0)\n"
"s_barrier\n"
"v_mfma_f32_16x16x32_bf16 v[164:167], a[232:235], a[200:203], v[164:167]\n"
"ds_read_b128 a[224:227], v240 offset:0\n"
"v_cvt_pk_bf16_f32 v152, v152, v153\n"
"v_mfma_f32_16x16x32_bf16 v[156:159], a[232:235], a[196:199], v[156:159]\n"
"ds_read_b128 a[228:231], v240 offset:128\n"
"v_cvt_pk_bf16_f32 v153, v154, v155\n"
"v_mfma_f32_16x16x32_bf16 v[156:159], a[236:239], a[212:215], v[156:159]\n"
"s_barrier\n"
"v_mfma_f32_16x16x32_bf16 v[148:151], a[236:239], a[208:211], v[148:151]\n"
"ds_read_b128 a[236:239], v242 offset:128\n"
"v_cvt_pk_bf16_f32 v160, v160, v161\n"
"v_mfma_f32_16x16x32_bf16 v[148:151], a[232:235], a[192:195], v[148:151]\n"
"ds_read_b128 a[232:235], v242 offset:0\n"
"v_cvt_pk_bf16_f32 v161, v162, v163\n"
"v_mfma_f32_16x16x32_bf16 v[200:203], a[240:243], a[192:195], v[200:203]\n"
"ds_read_b128 a[12:15], v239 offset:1024\n"
"v_cvt_pk_bf16_f32 v154, v156, v157\n"
"v_mfma_f32_16x16x32_bf16 v[200:203], a[244:247], a[208:211], v[200:203]\n"
"ds_read_b128 a[4:7], v239 offset:0\n"
"v_cvt_pk_bf16_f32 v155, v158, v159\n"
"v_mfma_f32_16x16x32_bf16 v[208:211], a[244:247], a[212:215], v[208:211]\n"
"v_cvt_pk_bf16_f32 v146, v148, v149\n"
"v_cvt_pk_bf16_f32 v147, v150, v151\n"
"v_mfma_f32_16x16x32_bf16 v[208:211], a[240:243], a[196:199], v[208:211]\n"
"ds_read_b128 a[28:31], v239 offset:3072\n"
"v_cvt_pk_bf16_f32 v162, v164, v165\n"
"v_mfma_f32_16x16x32_bf16 v[216:219], a[240:243], a[200:203], v[216:219]\n"
"ds_read_b128 a[240:243], v240 offset:256\n"
"v_cvt_pk_bf16_f32 v200, v200, v201\n"
"v_mfma_f32_16x16x32_bf16 v[216:219], a[244:247], a[216:219], v[216:219]\n"
"buffer_load_dwordx4 v248, s[44:47], s80 offen offset:2048 nt lds\n"
"v_mfma_f32_16x16x32_bf16 v[220:223], a[252:255], a[216:219], v[220:223]\n"
"ds_read_b128 a[244:247], v240 offset:384\n"
"v_cvt_pk_bf16_f32 v201, v202, v203\n"
"v_mfma_f32_16x16x32_bf16 v[220:223], a[248:251], a[200:203], v[220:223]\n"
"ds_read_b128 a[20:23], v239 offset:2048\n"
"v_cvt_pk_bf16_f32 v208, v208, v209\n"
"v_mfma_f32_16x16x32_bf16 v[212:215], a[248:251], a[196:199], v[212:215]\n"
"v_cvt_pk_bf16_f32 v209, v210, v211\n"
"v_cvt_pk_bf16_f32 v163, v166, v167\n"
"v_mfma_f32_16x16x32_bf16 v[212:215], a[252:255], a[212:215], v[212:215]\n"
"v_cvt_pk_bf16_f32 v216, v216, v217\n"
"v_accvgpr_write_b32 a18, v18\n"
"v_mfma_f32_16x16x32_bf16 v[204:207], a[252:255], a[208:211], v[204:207]\n"
"ds_read_b128 a[252:255], v242 offset:384\n"
"v_cvt_pk_bf16_f32 v217, v218, v219\n"
"v_mfma_f32_16x16x32_bf16 v[204:207], a[248:251], a[192:195], v[204:207]\n"
"s_waitcnt lgkmcnt(7)\n"
"ds_read_b128 a[248:251], v242 offset:256\n"
"v_cvt_pk_bf16_f32 v218, v220, v221\n"
"v_mfma_f32_16x16x32_bf16 v[4:7], a[224:227], v[0:3], 0\n"
"v_cvt_pk_bf16_f32 v210, v212, v213\n"
"v_cvt_pk_bf16_f32 v211, v214, v215\n"
"v_mfma_f32_16x16x32_bf16 v[4:7], a[228:231], v[56:59], v[4:7]\n"
"v_cvt_pk_bf16_f32 v219, v222, v223\n"
"v_accvgpr_write_b32 a19, v19\n"
"v_mfma_f32_16x16x32_bf16 v[12:15], a[228:231], v[64:67], 0\n"
"v_cvt_pk_bf16_f32 v202, v204, v205\n"
"v_cvt_pk_bf16_f32 v203, v206, v207\n"
"v_mfma_f32_16x16x32_bf16 v[12:15], a[224:227], v[8:11], v[12:15]\n"
"v_accvgpr_write_b32 a48, v48\n"
"v_accvgpr_write_b32 a49, v49\n"
"v_mfma_f32_16x16x32_bf16 v[20:23], a[224:227], v[16:19], 0\n"
"v_accvgpr_write_b32 a50, v50\n"
"v_accvgpr_write_b32 a51, v51\n"
"v_mfma_f32_16x16x32_bf16 v[20:23], a[228:231], v[72:75], v[20:23]\n"
"v_accvgpr_write_b32 a24, v24\n"
"v_accvgpr_write_b32 a25, v25\n"
"v_mfma_f32_16x16x32_bf16 v[28:31], a[228:231], v[80:83], 0\n"
"ds_read_b128 a[228:231], v241 offset:128\n"
"v_accvgpr_write_b32 a26, v26\n"
"v_mfma_f32_16x16x32_bf16 v[28:31], a[224:227], v[24:27], v[28:31]\n"
"ds_read_b128 a[224:227], v241 offset:0\n"
"v_accvgpr_write_b32 a27, v27\n"
"v_mfma_f32_16x16x32_bf16 v[60:63], a[232:235], v[0:3], 0\n"
"buffer_load_dwordx4 v248, s[44:47], s80 offen offset:3072 nt lds\n"
"v_mfma_f32_16x16x32_bf16 v[60:63], a[236:239], v[56:59], v[60:63]\n"
"s_add_u32 m0, s79, 0xc2a0\n"
"s_add_u32 s80, s10, s29\n"
"v_accvgpr_write_b32 a56, v56\n"
"v_mfma_f32_16x16x32_bf16 v[68:71], a[236:239], v[64:67], 0\n"
"s_addk_i32 s10, 0x1000\n"
"v_accvgpr_write_b32 a57, v57\n"
"v_accvgpr_write_b32 a58, v58\n"
"v_mfma_f32_16x16x32_bf16 v[68:71], a[232:235], v[8:11], v[68:71]\n"
"s_bitset0_b32 s10, 15\n"
"v_accvgpr_write_b32 a59, v59\n"
"v_accvgpr_write_b32 a88, v88\n"
"v_mfma_f32_16x16x32_bf16 v[76:79], a[232:235], v[16:19], 0\n"
"v_accvgpr_write_b32 a89, v89\n"
"v_accvgpr_write_b32 a90, v90\n"
"v_mfma_f32_16x16x32_bf16 v[76:79], a[236:239], v[72:75], v[76:79]\n"
"v_accvgpr_write_b32 a91, v91\n"
"v_accvgpr_write_b32 a96, v96\n"
"v_mfma_f32_16x16x32_bf16 v[84:87], a[236:239], v[80:83], 0\n"
"ds_read_b128 a[236:239], v243 offset:128\n"
"v_accvgpr_write_b32 a97, v97\n"
"v_mfma_f32_16x16x32_bf16 v[84:87], a[232:235], v[24:27], v[84:87]\n"
"s_waitcnt lgkmcnt(3)\n"
"ds_read_b128 a[232:235], v243 offset:0\n"
"v_accvgpr_write_b32 a98, v98\n"
"v_mfma_f32_16x16x32_bf16 v[4:7], a[240:243], v[112:115], v[4:7]\n"
"v_accvgpr_write_b32 a99, v99\n"
"v_accvgpr_write_b32 a64, v64\n"
"v_mfma_f32_16x16x32_bf16 v[4:7], a[244:247], v[168:171], v[4:7]\n"
"v_accvgpr_write_b32 a65, v65\n"
"v_accvgpr_write_b32 a66, v66\n"
"v_mfma_f32_16x16x32_bf16 v[12:15], a[244:247], v[176:179], v[12:15]\n"
"v_accvgpr_write_b32 a67, v67\n"
"v_accvgpr_write_b32 a72, v72\n"
"v_mfma_f32_16x16x32_bf16 v[12:15], a[240:243], v[120:123], v[12:15]\n"
"v_accvgpr_write_b32 a73, v73\n"
"v_accvgpr_write_b32 a74, v74\n"
"v_mfma_f32_16x16x32_bf16 v[20:23], a[240:243], v[128:131], v[20:23]\n"
"v_accvgpr_write_b32 a75, v75\n"
"v_accvgpr_write_b32 a104, v104\n"
"v_mfma_f32_16x16x32_bf16 v[20:23], a[244:247], v[184:187], v[20:23]\n"
"v_accvgpr_write_b32 a105, v105\n"
"v_accvgpr_write_b32 a106, v106\n"
"v_mfma_f32_16x16x32_bf16 v[28:31], a[244:247], v[192:195], v[28:31]\n"
"buffer_load_dwordx4 v248, s[44:47], s81 offen offset:0 nt lds\n"
"v_mfma_f32_16x16x32_bf16 v[28:31], a[240:243], v[136:139], v[28:31]\n"
"ds_read_b128 a[240:243], v241 offset:256\n"
"v_accvgpr_write_b32 a107, v107\n"
"v_mfma_f32_16x16x32_bf16 v[60:63], a[248:251], v[112:115], v[60:63]\n"
"ds_read_b128 a[244:247], v241 offset:384\n"
"v_accvgpr_write_b32 a80, v80\n"
"v_mfma_f32_16x16x32_bf16 v[60:63], a[252:255], v[168:171], v[60:63]\n"
"v_accvgpr_write_b32 a81, v81\n"
"v_accvgpr_write_b32 a82, v82\n"
"v_mfma_f32_16x16x32_bf16 v[68:71], a[252:255], v[176:179], v[68:71]\n"
"v_accvgpr_write_b32 a83, v83\n"
"v_accvgpr_write_b32 a112, v112\n"
"v_mfma_f32_16x16x32_bf16 v[68:71], a[248:251], v[120:123], v[68:71]\n"
"v_accvgpr_write_b32 a113, v113\n"
"v_accvgpr_write_b32 a114, v114\n"
"v_mfma_f32_16x16x32_bf16 v[76:79], a[248:251], v[128:131], v[76:79]\n"
"v_accvgpr_write_b32 a115, v115\n"
"v_accvgpr_write_b32 a144, v144\n"
"v_mfma_f32_16x16x32_bf16 v[76:79], a[252:255], v[184:187], v[76:79]\n"
"v_accvgpr_write_b32 a145, v145\n"
"v_accvgpr_write_b32 a146, v146\n"
"v_mfma_f32_16x16x32_bf16 v[84:87], a[252:255], v[192:195], v[84:87]\n"
"ds_read_b128 a[252:255], v243 offset:384\n"
"v_accvgpr_write_b32 a147, v147\n"
"v_mfma_f32_16x16x32_bf16 v[84:87], a[248:251], v[136:139], v[84:87]\n"
"s_waitcnt lgkmcnt(3)\n"
"ds_read_b128 a[248:251], v243 offset:256\n"
"v_accvgpr_write_b32 a152, v152\n"
"v_mfma_f32_16x16x32_bf16 v[4:7], a[224:227], v[32:35], v[4:7]\n"
"v_accvgpr_write_b32 a153, v153\n"
"v_accvgpr_write_b32 a154, v154\n"
"v_mfma_f32_16x16x32_bf16 v[4:7], a[228:231], v[88:91], v[4:7]\n"
"v_accvgpr_write_b32 a155, v155\n"
"v_accvgpr_write_b32 a120, v120\n"
"v_mfma_f32_16x16x32_bf16 v[12:15], a[228:231], v[96:99], v[12:15]\n"
"v_accvgpr_write_b32 a121, v121\n"
"v_accvgpr_write_b32 a122, v122\n"
"v_mfma_f32_16x16x32_bf16 v[12:15], a[224:227], v[40:43], v[12:15]\n"
"v_accvgpr_write_b32 a123, v123\n"
"v_accvgpr_write_b32 a128, v128\n"
"v_mfma_f32_16x16x32_bf16 v[20:23], a[224:227], v[48:51], v[20:23]\n"
"s_waitcnt lgkmcnt(0)\n"
"s_barrier\n"
"v_mfma_f32_16x16x32_bf16 v[20:23], a[228:231], v[104:107], v[20:23]\n"
"buffer_load_dwordx4 v248, s[44:47], s81 offen offset:1024 nt lds\n"
"v_mfma_f32_16x16x32_bf16 v[28:31], a[228:231], a[12:15], v[28:31]\n"
"s_addk_i32 m0, 0x20\n"
"ds_read_b128 a[228:231], v241 offset:33216\n"
"v_accvgpr_write_b32 a129, v129\n"
"v_mfma_f32_16x16x32_bf16 v[28:31], a[224:227], a[4:7], v[28:31]\n"
"ds_read_b128 a[224:227], v240 offset:33216\n"
"v_accvgpr_write_b32 a130, v130\n"
"v_mfma_f32_16x16x32_bf16 v[60:63], a[232:235], v[32:35], v[60:63]\n"
"v_accvgpr_write_b32 a131, v131\n"
"v_accvgpr_write_b32 a160, v160\n"
"v_mfma_f32_16x16x32_bf16 v[60:63], a[236:239], v[88:91], v[60:63]\n"
"v_accvgpr_write_b32 a161, v161\n"
"v_accvgpr_write_b32 a162, v162\n"
"v_mfma_f32_16x16x32_bf16 v[68:71], a[236:239], v[96:99], v[68:71]\n"
"v_accvgpr_write_b32 a163, v163\n"
"v_accvgpr_write_b32 a136, v136\n"
"v_mfma_f32_16x16x32_bf16 v[68:71], a[232:235], v[40:43], v[68:71]\n"
"v_accvgpr_write_b32 a137, v137\n"
"v_accvgpr_write_b32 a138, v138\n"
"v_mfma_f32_16x16x32_bf16 v[76:79], a[232:235], v[48:51], v[76:79]\n"
"v_accvgpr_write_b32 a139, v139\n"
"v_accvgpr_write_b32 a168, v168\n"
"v_mfma_f32_16x16x32_bf16 v[76:79], a[236:239], v[104:107], v[76:79]\n"
"v_accvgpr_write_b32 a169, v169\n"
"v_accvgpr_write_b32 a170, v170\n"
"v_mfma_f32_16x16x32_bf16 v[84:87], a[236:239], a[12:15], v[84:87]\n"
"ds_read_b128 a[236:239], v241 offset:33344\n"
"v_accvgpr_write_b32 a171, v171\n"
"v_mfma_f32_16x16x32_bf16 v[84:87], a[232:235], a[4:7], v[84:87]\n"
"ds_read_b128 a[232:235], v240 offset:33344\n"
"v_accvgpr_write_b32 a200, v200\n"
"v_mfma_f32_16x16x32_bf16 v[4:7], a[240:243], v[144:147], v[4:7]\n"
"v_accvgpr_write_b32 a201, v201\n"
"v_accvgpr_write_b32 a202, v202\n"
"v_mfma_f32_16x16x32_bf16 v[4:7], a[244:247], v[200:203], v[4:7]\n"
"v_accvgpr_write_b32 a203, v203\n"
"v_accvgpr_write_b32 a208, v208\n"
"v_mfma_f32_16x16x32_bf16 v[12:15], a[244:247], v[208:211], v[12:15]\n"
"v_accvgpr_write_b32 a209, v209\n"
"v_accvgpr_write_b32 a210, v210\n"
"v_mfma_f32_16x16x32_bf16 v[12:15], a[240:243], v[152:155], v[12:15]\n"
"buffer_load_dwordx4 v248, s[44:47], s81 offen offset:2048 nt lds\n"
"v_mfma_f32_16x16x32_bf16 v[20:23], a[240:243], v[160:163], v[20:23]\n"
"v_max3_f32 v156, v4, v5, v6\n"
"v_accvgpr_write_b32 a211, v211\n"
"v_mfma_f32_16x16x32_bf16 v[20:23], a[244:247], v[216:219], v[20:23]\n"
"v_accvgpr_write_b32 a176, v176\n"
"v_accvgpr_write_b32 a177, v177\n"
"v_mfma_f32_16x16x32_bf16 v[28:31], a[244:247], a[28:31], v[28:31]\n"
"ds_read_b128 a[244:247], v241 offset:33472\n"
"v_max3_f32 v157, v12, v13, v14\n"
"v_mfma_f32_16x16x32_bf16 v[28:31], a[240:243], a[20:23], v[28:31]\n"
"ds_read_b128 a[240:243], v240 offset:33472\n"
"v_accvgpr_write_b32 a178, v178\n"
"v_mfma_f32_16x16x32_bf16 v[60:63], a[248:251], v[144:147], v[60:63]\n"
"v_max3_f32 v158, v20, v21, v22\n"
"v_accvgpr_write_b32 a179, v179\n"
"v_mfma_f32_16x16x32_bf16 v[60:63], a[252:255], v[200:203], v[60:63]\n"
"v_accvgpr_write_b32 a184, v184\n"
"v_accvgpr_write_b32 a185, v185\n"
"v_mfma_f32_16x16x32_bf16 v[68:71], a[252:255], v[208:211], v[68:71]\n"
"v_max3_f32 v159, v28, v29, v30\n"
"v_accvgpr_write_b32 a186, v186\n"
"v_mfma_f32_16x16x32_bf16 v[68:71], a[248:251], v[152:155], v[68:71]\n"
"v_accvgpr_write_b32 a187, v187\n"
"v_accvgpr_write_b32 a216, v216\n"
"v_mfma_f32_16x16x32_bf16 v[76:79], a[248:251], v[160:163], v[76:79]\n"
"v_max3_f32 v164, v7, v60, v61\n"
"v_max3_f32 v156, v156, v62, v63\n"
"v_mfma_f32_16x16x32_bf16 v[76:79], a[252:255], v[216:219], v[76:79]\n"
"v_max3_f32 v156, v156, v164, v249\n"
"v_accvgpr_write_b32 a217, v217\n"
"v_mfma_f32_16x16x32_bf16 v[84:87], a[252:255], a[28:31], v[84:87]\n"
"ds_read_b128 a[252:255], v241 offset:33600\n"
"v_mov_b32 v164, v156\n"
"v_mfma_f32_16x16x32_bf16 v[84:87], a[248:251], a[20:23], v[84:87]\n"
"s_waitcnt lgkmcnt(3)\n"
"ds_read_b128 a[248:251], v240 offset:33600\n"
"v_add_u32 v241, 0xffff3d60, v241\n"
"v_mfma_f32_16x16x32_bf16 v[36:39], a[224:227], v[0:3], 0\n"
"v_add_u32 v240, 0xffff3d60, v240\n"
"v_max3_f32 v165, v15, v68, v69\n"
"v_mfma_f32_16x16x32_bf16 v[36:39], a[228:231], v[32:35], v[36:39]\n"
"v_max3_f32 v157, v157, v70, v71\n"
"v_max3_f32 v166, v23, v76, v77\n"
"v_mfma_f32_16x16x32_bf16 v[44:47], a[228:231], v[40:43], 0\n"
"buffer_load_dwordx4 v248, s[44:47], s81 offen offset:3072 nt lds\n"
"v_mfma_f32_16x16x32_bf16 v[44:47], a[224:227], v[8:11], v[44:47]\n"
"s_add_u32 m0, s79, 0x40e0\n"
"v_permlane16_swap_b32 v156, v164\n"
"v_mfma_f32_16x16x32_bf16 v[52:55], a[224:227], v[16:19], 0\n"
"s_add_u32 s81, s10, s29\n"
"v_max3_f32 v157, v157, v165, v250\n"
"v_max_f32 v156, v156, v164\n"
"v_mfma_f32_16x16x32_bf16 v[52:55], a[228:231], v[48:51], v[52:55]\n"
"s_addk_i32 s10, 0x1000\n"
"v_mov_b32 v165, v157\n"
"v_mov_b32 v164, v156\n"
"v_mfma_f32_16x16x32_bf16 v[92:95], a[228:231], a[4:7], 0\n"
"s_bitset0_b32 s10, 15\n"
"ds_read_b128 a[228:231], v243 offset:33216\n"
"v_max3_f32 v158, v158, v78, v79\n"
"v_mfma_f32_16x16x32_bf16 v[92:95], a[224:227], v[24:27], v[92:95]\n"
"ds_read_b128 a[224:227], v242 offset:33216\n"
"v_max3_f32 v158, v158, v166, v251\n"
"v_mfma_f32_16x16x32_bf16 v[36:39], a[232:235], v[56:59], v[36:39]\n"
"v_permlane16_swap_b32 v157, v165\n"
"v_mfma_f32_16x16x32_bf16 v[36:39], a[236:239], v[88:91], v[36:39]\n"
"v_mov_b32 v166, v158\n"
"v_max_f32 v157, v157, v165\n"
"v_mfma_f32_16x16x32_bf16 v[44:47], a[236:239], v[96:99], v[44:47]\n"
"v_max3_f32 v167, v31, v84, v85\n"
"v_max3_f32 v159, v159, v86, v87\n"
"v_mfma_f32_16x16x32_bf16 v[44:47], a[232:235], v[64:67], v[44:47]\n"
"v_permlane32_swap_b32 v156, v164\n"
"v_mfma_f32_16x16x32_bf16 v[52:55], a[232:235], v[72:75], v[52:55]\n"
"v_mov_b32 v165, v157\n"
"v_max3_f32 v159, v159, v167, v252\n"
"v_mfma_f32_16x16x32_bf16 v[52:55], a[236:239], v[104:107], v[52:55]\n"
"v_mov_b32 v167, v159\n"
"v_max_f32 v156, v156, v164\n"
"v_mfma_f32_16x16x32_bf16 v[92:95], a[236:239], a[12:15], v[92:95]\n"
"ds_read_b128 a[236:239], v243 offset:33344\n"
"v_mul_f32 v156, -v156, s92\n"
"v_mfma_f32_16x16x32_bf16 v[92:95], a[232:235], v[80:83], v[92:95]\n"
"s_waitcnt lgkmcnt(3)\n"
"ds_read_b128 a[232:235], v242 offset:33344\n"
"v_fma_f32 v4, v4, s92, v156\n"
"v_mfma_f32_16x16x32_bf16 v[36:39], a[240:243], v[112:115], v[36:39]\n"
"buffer_load_dwordx4 v248, s[48:51], s80 offen offset:0 nt lds\n"
"v_mfma_f32_16x16x32_bf16 v[36:39], a[244:247], v[144:147], v[36:39]\n"
"v_permlane16_swap_b32 v158, v166\n"
"v_mfma_f32_16x16x32_bf16 v[44:47], a[244:247], v[152:155], v[44:47]\n"
"v_permlane32_swap_b32 v157, v165\n"
"v_mfma_f32_16x16x32_bf16 v[44:47], a[240:243], v[120:123], v[44:47]\n"
"v_permlane16_swap_b32 v159, v167\n"
"v_mfma_f32_16x16x32_bf16 v[52:55], a[240:243], v[128:131], v[52:55]\n"
"v_exp_f32 v4, v4\n"
"v_mfma_f32_16x16x32_bf16 v[52:55], a[244:247], v[160:163], v[52:55]\n"
"v_fma_f32 v5, v5, s92, v156\n"
"v_fma_f32 v6, v6, s92, v156\n"
"v_mfma_f32_16x16x32_bf16 v[92:95], a[244:247], a[20:23], v[92:95]\n"
"ds_read_b128 a[244:247], v243 offset:33472\n"
"v_fma_f32 v7, v7, s92, v156\n"
"v_mfma_f32_16x16x32_bf16 v[92:95], a[240:243], v[136:139], v[92:95]\n"
"ds_read_b128 a[240:243], v242 offset:33472\n"
"v_fma_f32 v60, v60, s92, v156\n"
"v_mfma_f32_16x16x32_bf16 v[36:39], a[248:251], v[168:171], v[36:39]\n"
"v_exp_f32 v5, v5\n"
"v_mfma_f32_16x16x32_bf16 v[36:39], a[252:255], v[200:203], v[36:39]\n"
"v_exp_f32 v6, v6\n"
"v_mfma_f32_16x16x32_bf16 v[44:47], a[252:255], v[208:211], v[44:47]\n"
"v_exp_f32 v7, v7\n"
"v_mfma_f32_16x16x32_bf16 v[44:47], a[248:251], v[176:179], v[44:47]\n"
"v_exp_f32 v60, v60\n"
"v_mfma_f32_16x16x32_bf16 v[52:55], a[248:251], v[184:187], v[52:55]\n"
"v_fma_f32 v61, v61, s92, v156\n"
"v_fma_f32 v62, v62, s92, v156\n"
"v_mfma_f32_16x16x32_bf16 v[52:55], a[252:255], v[216:219], v[52:55]\n"
"v_exp_f32 v61, v61\n"
"v_mfma_f32_16x16x32_bf16 v[92:95], a[252:255], a[28:31], v[92:95]\n"
"ds_read_b128 a[252:255], v243 offset:33600\n"
"v_max_f32 v158, v158, v166\n"
"v_mfma_f32_16x16x32_bf16 v[92:95], a[248:251], v[192:195], v[92:95]\n"
"s_waitcnt lgkmcnt(3)\n"
"buffer_load_dwordx4 v248, s[48:51], s80 offen offset:1024 nt lds\n"
"v_mfma_f32_16x16x32_bf16 v[100:103], a[224:227], v[0:3], 0\n"
"s_addk_i32 m0, 0x20\n"
"ds_read_b128 a[248:251], v242 offset:33600\n"
"v_mov_b32 v166, v158\n"
"v_mfma_f32_16x16x32_bf16 v[100:103], a[228:231], v[32:35], v[100:103]\n"
"v_exp_f32 v62, v62\n"
"v_mfma_f32_16x16x32_bf16 v[108:111], a[228:231], v[40:43], 0\n"
"v_fma_f32 v63, v63, s92, v156\n"
"v_max_f32 v157, v157, v165\n"
"v_mfma_f32_16x16x32_bf16 v[108:111], a[224:227], v[8:11], v[108:111]\n"
"v_exp_f32 v63, v63\n"
"v_mfma_f32_16x16x32_bf16 v[116:119], a[224:227], v[16:19], 0\n"
"v_mul_f32 v157, -v157, s92\n"
"v_add_u32 v242, 0xffff3d60, v242\n"
"v_mfma_f32_16x16x32_bf16 v[116:119], a[228:231], v[48:51], v[116:119]\n"
"s_waitcnt lgkmcnt(0)\n"
"s_barrier\n"
"v_mfma_f32_16x16x32_bf16 v[100:103], a[232:235], v[56:59], v[100:103]\n"
"ds_read_b128 a[224:227], v240 offset:0\n"
"v_fma_f32 v12, v12, s92, v157\n"
"v_mfma_f32_16x16x32_bf16 v[100:103], a[236:239], v[88:91], v[100:103]\n"
"ds_read_b128 a[228:231], v241 offset:0\n"
"v_fma_f32 v13, v13, s92, v157\n"
"v_mfma_f32_16x16x32_bf16 v[108:111], a[236:239], v[96:99], v[108:111]\n"
"v_permlane32_swap_b32 v158, v166\n"
"v_mfma_f32_16x16x32_bf16 v[108:111], a[232:235], v[64:67], v[108:111]\n"
"v_exp_f32 v12, v12\n"
"v_mfma_f32_16x16x32_bf16 v[116:119], a[232:235], v[72:75], v[116:119]\n"
"ds_read_b128 a[232:235], v240 offset:128\n"
"v_fma_f32 v14, v14, s92, v157\n"
"v_mfma_f32_16x16x32_bf16 v[116:119], a[236:239], v[104:107], v[116:119]\n"
"ds_read_b128 a[236:239], v241 offset:128\n"
"v_add_u32 v243, 0xffff3d60, v243\n"
"v_mfma_f32_16x16x32_bf16 v[100:103], a[240:243], v[112:115], v[100:103]\n"
"v_exp_f32 v13, v13\n"
"v_mfma_f32_16x16x32_bf16 v[100:103], a[244:247], v[144:147], v[100:103]\n"
"v_exp_f32 v14, v14\n"
"v_mfma_f32_16x16x32_bf16 v[108:111], a[244:247], v[152:155], v[108:111]\n"
"buffer_load_dwordx4 v248, s[48:51], s80 offen offset:2048 nt lds\n"
"v_mfma_f32_16x16x32_bf16 v[108:111], a[240:243], v[120:123], v[108:111]\n"
"v_fma_f32 v15, v15, s92, v157\n"
"v_accvgpr_write_b32 a218, v218\n"
"v_mfma_f32_16x16x32_bf16 v[116:119], a[240:243], v[128:131], v[116:119]\n"
"ds_read_b128 a[240:243], v240 offset:256\n"
"v_accvgpr_write_b32 a219, v219\n"
"v_mfma_f32_16x16x32_bf16 v[116:119], a[244:247], v[160:163], v[116:119]\n"
"ds_read_b128 a[244:247], v241 offset:256\n"
"v_fma_f32 v68, v68, s92, v157\n"
"v_mfma_f32_16x16x32_bf16 v[100:103], a[248:251], v[168:171], v[100:103]\n"
"v_exp_f32 v15, v15\n"
"v_mfma_f32_16x16x32_bf16 v[100:103], a[252:255], v[200:203], v[100:103]\n"
"v_exp_f32 v68, v68\n"
"v_mfma_f32_16x16x32_bf16 v[108:111], a[252:255], v[208:211], v[108:111]\n"
"v_fma_f32 v69, v69, s92, v157\n"
"v_accvgpr_write_b32 a192, v192\n"
"v_mfma_f32_16x16x32_bf16 v[108:111], a[248:251], v[176:179], v[108:111]\n"
"v_accvgpr_write_b32 a193, v193\n"
"v_accvgpr_write_b32 a194, v194\n"
"v_mfma_f32_16x16x32_bf16 v[116:119], a[248:251], v[184:187], v[116:119]\n"
"ds_read_b128 a[248:251], v240 offset:384\n"
"v_accvgpr_write_b32 a195, v195\n"
"v_mfma_f32_16x16x32_bf16 v[116:119], a[252:255], v[216:219], v[116:119]\n"
"s_waitcnt lgkmcnt(3)\n"
"ds_read_b128 a[252:255], v241 offset:384\n"
"v_fma_f32 v70, v70, s92, v157\n"
"v_mfma_f32_16x16x32_bf16 v[124:127], a[224:227], a[0:3], 0\n"
"v_exp_f32 v69, v69\n"
"v_mfma_f32_16x16x32_bf16 v[124:127], a[228:231], a[32:35], v[124:127]\n"
"v_max_f32 v159, v159, v167\n"
"v_fma_f32 v71, v71, s92, v157\n"
"v_mfma_f32_16x16x32_bf16 v[132:135], a[228:231], a[40:43], 0\n"
"v_mov_b32 v167, v159\n"
"v_max_f32 v158, v158, v166\n"
"v_mfma_f32_16x16x32_bf16 v[132:135], a[224:227], a[8:11], v[132:135]\n"
"v_exp_f32 v70, v70\n"
"v_mfma_f32_16x16x32_bf16 v[140:143], a[224:227], a[16:19], 0\n"
"buffer_load_dwordx4 v248, s[48:51], s80 offen offset:3072 nt lds\n"
"v_mfma_f32_16x16x32_bf16 v[140:143], a[228:231], a[48:51], v[140:143]\n"
"s_add_u32 m0, s79, 0x14460\n"
"v_exp_f32 v71, v71\n"
"v_mfma_f32_16x16x32_bf16 v[172:175], a[228:231], a[4:7], 0\n"
"s_add_u32 s80, s9, s29\n"
"ds_read_b128 a[228:231], v243 offset:0\n"
"v_mul_f32 v158, -v158, s92\n"
"v_mfma_f32_16x16x32_bf16 v[172:175], a[224:227], a[24:27], v[172:175]\n"
"s_addk_i32 s9, 0x1000\n"
"ds_read_b128 a[224:227], v242 offset:0\n"
"v_fma_f32 v20, v20, s92, v158\n"
"v_mfma_f32_16x16x32_bf16 v[124:127], a[232:235], a[56:59], v[124:127]\n"
"s_bitset0_b32 s9, 15\n"
"v_permlane32_swap_b32 v159, v167\n"
"v_mfma_f32_16x16x32_bf16 v[124:127], a[236:239], a[88:91], v[124:127]\n"
"v_exp_f32 v20, v20\n"
"v_mfma_f32_16x16x32_bf16 v[132:135], a[236:239], a[96:99], v[132:135]\n"
"v_fma_f32 v21, v21, s92, v158\n"
"v_fma_f32 v22, v22, s92, v158\n"
"v_mfma_f32_16x16x32_bf16 v[132:135], a[232:235], a[64:67], v[132:135]\n"
"v_exp_f32 v21, v21\n"
"v_mfma_f32_16x16x32_bf16 v[140:143], a[232:235], a[72:75], v[140:143]\n"
"v_exp_f32 v22, v22\n"
"v_mfma_f32_16x16x32_bf16 v[140:143], a[236:239], a[104:107], v[140:143]\n"
"v_fma_f32 v23, v23, s92, v158\n"
"v_fma_f32 v76, v76, s92, v158\n"
"v_mfma_f32_16x16x32_bf16 v[172:175], a[236:239], a[12:15], v[172:175]\n"
"ds_read_b128 a[236:239], v243 offset:128\n"
"v_fma_f32 v77, v77, s92, v158\n"
"v_mfma_f32_16x16x32_bf16 v[172:175], a[232:235], a[80:83], v[172:175]\n"
"s_waitcnt lgkmcnt(3)\n"
"ds_read_b128 a[232:235], v242 offset:128\n"
"v_max_f32 v159, v159, v167\n"
"v_mfma_f32_16x16x32_bf16 v[124:127], a[240:243], a[112:115], v[124:127]\n"
"v_exp_f32 v23, v23\n"
"v_mfma_f32_16x16x32_bf16 v[124:127], a[244:247], a[144:147], v[124:127]\n"
"v_exp_f32 v76, v76\n"
"v_mfma_f32_16x16x32_bf16 v[132:135], a[244:247], a[152:155], v[132:135]\n"
"v_exp_f32 v77, v77\n"
"v_mfma_f32_16x16x32_bf16 v[132:135], a[240:243], a[120:123], v[132:135]\n"
"buffer_load_dwordx4 v248, s[48:51], s81 offen offset:0 nt lds\n"
"v_mfma_f32_16x16x32_bf16 v[140:143], a[240:243], a[128:131], v[140:143]\n"
"v_fma_f32 v78, v78, s92, v158\n"
"v_mul_f32 v159, -v159, s92\n"
"v_mfma_f32_16x16x32_bf16 v[140:143], a[244:247], a[160:163], v[140:143]\n"
"v_fma_f32 v28, v28, s92, v159\n"
"v_add_u32 v240, 0xffff7e40, v240\n"
"v_mfma_f32_16x16x32_bf16 v[172:175], a[244:247], a[20:23], v[172:175]\n"
"ds_read_b128 a[244:247], v243 offset:256\n"
"v_add_u32 v241, 0xffff7e40, v241\n"
"v_mfma_f32_16x16x32_bf16 v[172:175], a[240:243], a[136:139], v[172:175]\n"
"ds_read_b128 a[240:243], v242 offset:256\n"
"v_fma_f32 v29, v29, s92, v159\n"
"v_mfma_f32_16x16x32_bf16 v[124:127], a[248:251], a[168:171], v[124:127]\n"
"v_exp_f32 v78, v78\n"
"v_mfma_f32_16x16x32_bf16 v[124:127], a[252:255], a[200:203], v[124:127]\n"
"v_exp_f32 v28, v28\n"
"v_mfma_f32_16x16x32_bf16 v[132:135], a[252:255], a[208:211], v[132:135]\n"
"v_exp_f32 v29, v29\n"
"v_mfma_f32_16x16x32_bf16 v[132:135], a[248:251], a[176:179], v[132:135]\n"
"v_fma_f32 v30, v30, s92, v159\n"
"v_fma_f32 v31, v31, s92, v159\n"
"v_mfma_f32_16x16x32_bf16 v[140:143], a[248:251], a[184:187], v[140:143]\n"
"v_exp_f32 v30, v30\n"
"v_mfma_f32_16x16x32_bf16 v[140:143], a[252:255], a[216:219], v[140:143]\n"
"v_exp_f32 v31, v31\n"
"v_mfma_f32_16x16x32_bf16 v[172:175], a[252:255], a[28:31], v[172:175]\n"
"ds_read_b128 a[252:255], v243 offset:384\n"
"v_fma_f32 v79, v79, s92, v158\n"
"v_mfma_f32_16x16x32_bf16 v[172:175], a[248:251], a[192:195], v[172:175]\n"
"s_waitcnt lgkmcnt(3)\n"
"ds_read_b128 a[248:251], v242 offset:384\n"
"v_mov_b32 v167, v28\n"
"v_mfma_f32_16x16x32_bf16 v[200:203], a[224:227], a[0:3], 0\n"
"v_add_f32 v167, v167, v29\n"
"v_cvt_pk_bf16_f32 v28, v28, v29\n"
"v_mfma_f32_16x16x32_bf16 v[200:203], a[228:231], a[32:35], v[200:203]\n"
"buffer_load_dwordx4 v248, s[48:51], s81 offen offset:1024 nt lds\n"
"v_mfma_f32_16x16x32_bf16 v[208:211], a[228:231], a[40:43], 0\n"
"s_addk_i32 m0, 0x20\n"
"v_cvt_pk_bf16_f32 v29, v30, v31\n"
"v_fma_f32 v36, v36, s92, v156\n"
"v_mfma_f32_16x16x32_bf16 v[208:211], a[224:227], a[8:11], v[208:211]\n"
"v_exp_f32 v79, v79\n"
"v_mfma_f32_16x16x32_bf16 v[216:219], a[224:227], a[16:19], 0\n"
"ds_read_b128 a[224:227], v240 offset:0\n"
"v_fma_f32 v37, v37, s92, v156\n"
"v_mfma_f32_16x16x32_bf16 v[216:219], a[228:231], a[48:51], v[216:219]\n"
"ds_read_b128 a[228:231], v241 offset:0\n"
"v_fma_f32 v38, v38, s92, v156\n"
"v_mfma_f32_16x16x32_bf16 v[200:203], a[232:235], a[56:59], v[200:203]\n"
"ds_write_b64 v253, v[28:29] offset:0\n"
"v_fma_f32 v39, v39, s92, v156\n"
"v_mfma_f32_16x16x32_bf16 v[200:203], a[236:239], a[88:91], v[200:203]\n"
"v_exp_f32 v36, v36\n"
"v_mfma_f32_16x16x32_bf16 v[208:211], a[236:239], a[96:99], v[208:211]\n"
"v_exp_f32 v37, v37\n"
"v_mfma_f32_16x16x32_bf16 v[208:211], a[232:235], a[64:67], v[208:211]\n"
"v_exp_f32 v38, v38\n"
"v_mfma_f32_16x16x32_bf16 v[216:219], a[232:235], a[72:75], v[216:219]\n"
"ds_read_b128 a[232:235], v240 offset:128\n"
"v_fma_f32 v44, v44, s92, v157\n"
"v_mfma_f32_16x16x32_bf16 v[216:219], a[236:239], a[104:107], v[216:219]\n"
"s_waitcnt lgkmcnt(4)\n"
"ds_read_b128 a[236:239], v241 offset:128\n"
"v_fma_f32 v45, v45, s92, v157\n"
"v_mfma_f32_16x16x32_bf16 v[200:203], a[240:243], a[112:115], v[200:203]\n"
"v_exp_f32 v39, v39\n"
"v_mfma_f32_16x16x32_bf16 v[200:203], a[244:247], a[144:147], v[200:203]\n"
"v_exp_f32 v44, v44\n"
"v_mfma_f32_16x16x32_bf16 v[208:211], a[244:247], a[152:155], v[208:211]\n"
"v_exp_f32 v45, v45\n"
"v_mfma_f32_16x16x32_bf16 v[208:211], a[240:243], a[120:123], v[208:211]\n"
"v_fma_f32 v46, v46, s92, v157\n"
"v_fma_f32 v47, v47, s92, v157\n"
"v_mfma_f32_16x16x32_bf16 v[216:219], a[240:243], a[128:131], v[216:219]\n"
"buffer_load_dwordx4 v248, s[48:51], s81 offen offset:2048 nt lds\n"
"v_mfma_f32_16x16x32_bf16 v[216:219], a[244:247], a[160:163], v[216:219]\n"
"ds_read_b128 a[240:243], v240 offset:256\n"
"v_fma_f32 v52, v52, s92, v158\n"
"v_mfma_f32_16x16x32_bf16 v[200:203], a[248:251], a[168:171], v[200:203]\n"
"ds_read_b128 a[244:247], v241 offset:256\n"
"v_fma_f32 v53, v53, s92, v158\n"
"v_mfma_f32_16x16x32_bf16 v[200:203], a[252:255], a[200:203], v[200:203]\n"
"v_exp_f32 v46, v46\n"
"v_mfma_f32_16x16x32_bf16 v[208:211], a[252:255], a[208:211], v[208:211]\n"
"v_exp_f32 v47, v47\n"
"v_mfma_f32_16x16x32_bf16 v[208:211], a[248:251], a[176:179], v[208:211]\n"
"v_exp_f32 v52, v52\n"
"v_mfma_f32_16x16x32_bf16 v[216:219], a[248:251], a[184:187], v[216:219]\n"
"v_exp_f32 v53, v53\n"
"v_mfma_f32_16x16x32_bf16 v[216:219], a[252:255], a[216:219], v[216:219]\n"
"s_waitcnt lgkmcnt(2)\n"
"ds_read_b128 a[248:251], v240 offset:384\n"
"v_fma_f32 v54, v54, s92, v158\n"
"v_mfma_f32_16x16x32_bf16 v[180:183], a[224:227], a[0:3], 0\n"
"v_exp_f32 v54, v54\n"
"v_mfma_f32_16x16x32_bf16 v[180:183], a[228:231], a[32:35], v[180:183]\n"
"ds_read_b128 a[252:255], v241 offset:384\n"
"v_fma_f32 v92, v92, s92, v159\n"
"v_mfma_f32_16x16x32_bf16 v[188:191], a[228:231], a[40:43], 0\n"
"PG5_%=:\n"
"s_cbranch_execz PGB1_%=\n"
"v_exp_f32 v92, v92\n"
"v_mfma_f32_16x16x32_bf16 v[188:191], a[224:227], a[8:11], v[188:191]\n"
"v_fma_f32 v93, v93, s92, v159\n"
"v_fma_f32 v94, v94, s92, v159\n"
"v_mfma_f32_16x16x32_bf16 v[196:199], a[224:227], a[16:19], 0\n"
"v_exp_f32 v93, v93\n"
"v_mfma_f32_16x16x32_bf16 v[196:199], a[228:231], a[48:51], v[196:199]\n"
"v_exp_f32 v94, v94\n"
"v_mfma_f32_16x16x32_bf16 v[148:151], a[228:231], a[4:7], 0\n"
"buffer_load_dwordx4 v248, s[48:51], s81 offen offset:3072 nt lds\n"
"v_mfma_f32_16x16x32_bf16 v[148:151], a[224:227], a[24:27], v[148:151]\n"
"s_add_u32 m0, s79, 0x1c620\n"
"v_fma_f32 v95, v95, s92, v159\n"
"v_add_f32 v167, v167, v30\n"
"v_mfma_f32_16x16x32_bf16 v[180:183], a[232:235], a[56:59], v[180:183]\n"
"s_add_u32 s81, s9, s29\n"
"v_add_f32 v167, v167, v31\n"
"v_fma_f32 v55, v55, s92, v158\n"
"v_mfma_f32_16x16x32_bf16 v[180:183], a[236:239], a[88:91], v[180:183]\n"
"s_addk_i32 s9, 0x1000\n"
"v_exp_f32 v95, v95\n"
"v_mfma_f32_16x16x32_bf16 v[188:191], a[236:239], a[96:99], v[188:191]\n"
"s_bitset0_b32 s9, 15\n"
"v_add_f32 v167, v167, v92\n"
"v_add_u32 v242, 0xffff7e40, v242\n"
"v_mfma_f32_16x16x32_bf16 v[188:191], a[232:235], a[64:67], v[188:191]\n"
"v_add_f32 v167, v167, v93\n"
"v_cvt_pk_bf16_f32 v92, v92, v93\n"
"v_mfma_f32_16x16x32_bf16 v[196:199], a[232:235], a[72:75], v[196:199]\n"
"v_cvt_pk_bf16_f32 v93, v94, v95\n"
"v_add_u32 v243, 0xffff7e40, v243\n"
"v_mfma_f32_16x16x32_bf16 v[196:199], a[236:239], a[104:107], v[196:199]\n"
"v_exp_f32 v55, v55\n"
"v_mfma_f32_16x16x32_bf16 v[148:151], a[236:239], a[12:15], v[148:151]\n"
"ds_write_b64 v253, v[92:93] offset:512\n"
"v_fma_f32 v100, v100, s92, v156\n"
"v_mfma_f32_16x16x32_bf16 v[148:151], a[232:235], a[80:83], v[148:151]\n"
"s_waitcnt lgkmcnt(1)\n"
"ds_read_b128 a[224:227], v242 offset:0\n"
"v_fma_f32 v101, v101, s92, v156\n"
"v_mfma_f32_16x16x32_bf16 v[180:183], a[240:243], a[112:115], v[180:183]\n"
"ds_read_b128 a[228:231], v243 offset:0\n"
"v_mov_b32 v164, v4\n"
"v_mfma_f32_16x16x32_bf16 v[180:183], a[244:247], a[144:147], v[180:183]\n"
"v_exp_f32 v100, v100\n"
"v_mfma_f32_16x16x32_bf16 v[188:191], a[244:247], a[152:155], v[188:191]\n"
"v_add_f32 v164, v164, v5\n"
"v_fma_f32 v102, v102, s92, v156\n"
"v_mfma_f32_16x16x32_bf16 v[188:191], a[240:243], a[120:123], v[188:191]\n"
"v_exp_f32 v101, v101\n"
"v_mfma_f32_16x16x32_bf16 v[196:199], a[240:243], a[128:131], v[196:199]\n"
"v_add_f32 v164, v164, v6\n"
"v_mov_b32 v165, v12\n"
"v_mfma_f32_16x16x32_bf16 v[196:199], a[244:247], a[160:163], v[196:199]\n"
"buffer_load_dwordx4 v248, s[44:47], s80 offen offset:0 nt lds\n"
"v_mfma_f32_16x16x32_bf16 v[148:151], a[244:247], a[20:23], v[148:151]\n"
"v_add_f32 v164, v164, v7\n"
"v_add_f32 v165, v165, v13\n"
"v_mfma_f32_16x16x32_bf16 v[148:151], a[240:243], a[136:139], v[148:151]\n"
"v_add_f32 v164, v164, v60\n"
"v_add_f32 v165, v165, v14\n"
"v_mfma_f32_16x16x32_bf16 v[180:183], a[248:251], a[168:171], v[180:183]\n"
"v_add_f32 v164, v164, v61\n"
"v_cvt_pk_bf16_f32 v4, v4, v5\n"
"v_mfma_f32_16x16x32_bf16 v[180:183], a[252:255], a[200:203], v[180:183]\n"
"v_exp_f32 v102, v102\n"
"v_mfma_f32_16x16x32_bf16 v[188:191], a[252:255], a[208:211], v[188:191]\n"
"v_add_f32 v164, v164, v62\n"
"v_cvt_pk_bf16_f32 v5, v6, v7\n"
"v_mfma_f32_16x16x32_bf16 v[188:191], a[248:251], a[176:179], v[188:191]\n"
"v_add_f32 v164, v164, v63\n"
"v_cvt_pk_bf16_f32 v6, v60, v61\n"
"v_mfma_f32_16x16x32_bf16 v[196:199], a[248:251], a[184:187], v[196:199]\n"
"v_cvt_pk_bf16_f32 v7, v62, v63\n"
"v_add_f32 v165, v165, v15\n"
"v_mfma_f32_16x16x32_bf16 v[196:199], a[252:255], a[216:219], v[196:199]\n"
"ds_read_b128 a[232:235], v242 offset:128\n"
"v_add_f32 v165, v165, v68\n"
"v_mfma_f32_16x16x32_bf16 v[148:151], a[252:255], a[28:31], v[148:151]\n"
"v_mov_b32 v166, v20\n"
"v_add_f32 v165, v165, v69\n"
"v_mfma_f32_16x16x32_bf16 v[148:151], a[248:251], a[192:195], v[148:151]\n"
"s_waitcnt lgkmcnt(1)\n"
"ds_read_b128 a[236:239], v243 offset:128\n"
"v_add_f32 v166, v166, v21\n"
"v_mfma_f32_16x16x32_bf16 v[60:63], a[224:227], a[0:3], 0\n"
"v_fma_f32 v103, v103, s92, v156\n"
"v_add_f32 v165, v165, v70\n"
"v_mfma_f32_16x16x32_bf16 v[60:63], a[228:231], a[32:35], v[60:63]\n"
"ds_read_b128 a[240:243], v242 offset:256\n"
"v_cvt_pk_bf16_f32 v12, v12, v13\n"
"v_cvt_pk_bf16_f32 v13, v14, v15\n"
"v_add_f32 v165, v165, v71\n"
"v_cvt_pk_bf16_f32 v14, v68, v69\n"
"v_cvt_pk_bf16_f32 v15, v70, v71\n"
"v_mfma_f32_16x16x32_bf16 v[68:71], a[228:231], a[40:43], 0\n"
"v_add_f32 v166, v166, v22\n"
"v_fma_f32 v108, v108, s92, v157\n"
"v_add_f32 v166, v166, v23\n"
"v_exp_f32 v103, v103\n"
"v_add_f32 v166, v166, v76\n"
"v_add_f32 v166, v166, v77\n"
"v_exp_f32 v108, v108\n"
"v_fma_f32 v109, v109, s92, v157\n"
"v_mfma_f32_16x16x32_bf16 v[68:71], a[224:227], a[8:11], v[68:71]\n"
"ds_read_b128 a[244:247], v243 offset:256\n"
"v_add_f32 v166, v166, v78\n"
"v_cvt_pk_bf16_f32 v20, v20, v21\n"
"v_cvt_pk_bf16_f32 v21, v22, v23\n"
"v_add_f32 v166, v166, v79\n"
"v_cvt_pk_bf16_f32 v22, v76, v77\n"
"v_cvt_pk_bf16_f32 v23, v78, v79\n"
"v_mfma_f32_16x16x32_bf16 v[76:79], a[224:227], a[16:19], 0\n"
"buffer_load_dwordx4 v248, s[44:47], s80 offen offset:1024 nt lds\n"
"v_exp_f32 v109, v109\n"
"v_fma_f32 v110, v110, s92, v157\n"
"v_exp_f32 v110, v110\n"
"v_mfma_f32_16x16x32_bf16 v[76:79], a[228:231], a[48:51], v[76:79]\n"
"s_waitcnt lgkmcnt(3)\n"
"ds_read_b128 a[252:255], v243 offset:384\n"
"v_fma_f32 v111, v111, s92, v157\n"
"v_mfma_f32_16x16x32_bf16 v[60:63], a[232:235], a[56:59], v[60:63]\n"
"s_waitcnt lgkmcnt(3)\n"
"v_exp_f32 v111, v111\n"
"v_mfma_f32_16x16x32_bf16 v[60:63], a[236:239], a[88:91], v[60:63]\n"
"s_addk_i32 m0, 0x20\n"
"ds_read_b128 a[248:251], v242 offset:384\n"
"v_fma_f32 v116, v116, s92, v158\n"
"v_mfma_f32_16x16x32_bf16 v[68:71], a[236:239], a[96:99], v[68:71]\n"
"v_exp_f32 v116, v116\n"
"v_mfma_f32_16x16x32_bf16 v[68:71], a[232:235], a[64:67], v[68:71]\n"
"v_fma_f32 v117, v117, s92, v158\n"
"v_fma_f32 v118, v118, s92, v158\n"
"v_mfma_f32_16x16x32_bf16 v[76:79], a[232:235], a[72:75], v[76:79]\n"
"v_exp_f32 v117, v117\n"
"v_mfma_f32_16x16x32_bf16 v[76:79], a[236:239], a[104:107], v[76:79]\n"
"s_waitcnt lgkmcnt(3)\n"
"v_exp_f32 v118, v118\n"
"v_mfma_f32_16x16x32_bf16 v[60:63], a[240:243], a[112:115], v[60:63]\n"
"s_waitcnt vmcnt(18) lgkmcnt(0)\n"
"s_barrier\n"
"v_mfma_f32_16x16x32_bf16 v[60:63], a[244:247], a[144:147], v[60:63]\n"
"buffer_load_dwordx4 v248, s[44:47], s80 offen offset:2048 nt lds\n"
"v_mfma_f32_16x16x32_bf16 v[68:71], a[244:247], a[152:155], v[68:71]\n"
"ds_read_b64_tr_b16 a[224:225], v244 offset:0\n"
"v_fma_f32 v119, v119, s92, v158\n"
"v_mfma_f32_16x16x32_bf16 v[68:71], a[240:243], a[120:123], v[68:71]\n"
"ds_read_b64_tr_b16 a[226:227], v246 offset:0\n"
"v_add_f32 v167, v167, v94\n"
"v_mfma_f32_16x16x32_bf16 v[76:79], a[240:243], a[128:131], v[76:79]\n"
"ds_read_b64_tr_b16 a[228:229], v224 offset:0\n"
"v_add_f32 v167, v167, v95\n"
"v_mfma_f32_16x16x32_bf16 v[76:79], a[244:247], a[160:163], v[76:79]\n"
"ds_read_b64_tr_b16 a[230:231], v226 offset:0\n"
"v_add_f32 v164, v164, v36\n"
"v_mfma_f32_16x16x32_bf16 v[60:63], a[248:251], a[168:171], v[60:63]\n"
"v_exp_f32 v119, v119\n"
"v_mfma_f32_16x16x32_bf16 v[60:63], a[252:255], a[200:203], v[60:63]\n"
"ds_read_b64 v[30:31], v254 offset:0\n"
"v_add_f32 v164, v164, v37\n"
"v_mfma_f32_16x16x32_bf16 v[68:71], a[252:255], a[208:211], v[68:71]\n"
"ds_read_b64 v[94:95], v254 offset:512\n"
"v_add_f32 v164, v164, v38\n"
"v_mfma_f32_16x16x32_bf16 v[68:71], a[248:251], a[176:179], v[68:71]\n"
"ds_read_b64_tr_b16 a[232:233], v244 offset:32\n"
"v_add_f32 v165, v165, v44\n"
"v_mfma_f32_16x16x32_bf16 v[76:79], a[248:251], a[184:187], v[76:79]\n"
"ds_read_b64_tr_b16 a[234:235], v246 offset:32\n"
"v_cvt_pk_bf16_f32 v36, v36, v37\n"
"v_mfma_f32_16x16x32_bf16 v[76:79], a[252:255], a[216:219], v[76:79]\n"
"s_waitcnt lgkmcnt(6)\n"
"v_add_f32 v164, v164, v39\n"
"v_cvt_pk_bf16_f32 v37, v38, v39\n"
"v_add_f32 v165, v165, v45\n"
"v_mfma_f32_16x16x32_bf16 v[0:3], a[224:227], v[4:7], 0\n"
"s_waitcnt lgkmcnt(4)\n"
"ds_read_b64_tr_b16 a[236:237], v224 offset:32\n"
"v_cvt_pk_bf16_f32 v38, v100, v101\n"
"v_cvt_pk_bf16_f32 v39, v102, v103\n"
"v_add_f32 v165, v165, v46\n"
"v_cvt_pk_bf16_f32 v44, v44, v45\n"
"v_mfma_f32_16x16x32_bf16 v[0:3], a[228:231], v[36:39], v[0:3]\n"
"ds_read_b64_tr_b16 a[238:239], v226 offset:32\n"
"v_add_f32 v165, v165, v47\n"
"v_cvt_pk_bf16_f32 v45, v46, v47\n"
"v_cvt_pk_bf16_f32 v46, v108, v109\n"
"v_cvt_pk_bf16_f32 v47, v110, v111\n"
"v_add_f32 v166, v166, v52\n"
"v_add_f32 v166, v166, v53\n"
"v_mfma_f32_16x16x32_bf16 v[8:11], a[228:231], v[44:47], 0\n"
"v_add_f32 v166, v166, v54\n"
"v_cvt_pk_bf16_f32 v52, v52, v53\n"
"v_mfma_f32_16x16x32_bf16 v[8:11], a[224:227], v[12:15], v[8:11]\n"
"v_add_f32 v166, v166, v55\n"
"v_cvt_pk_bf16_f32 v53, v54, v55\n"
"v_mfma_f32_16x16x32_bf16 v[16:19], a[224:227], v[20:23], 0\n"
"ds_read_b64_tr_b16 a[240:241], v245 offset:0\n"
"v_cvt_pk_bf16_f32 v54, v116, v117\n"
"v_cvt_pk_bf16_f32 v55, v118, v119\n"
"v_fma_f32 v124, v124, s92, v156\n"
"v_fma_f32 v125, v125, s92, v156\n"
"v_mfma_f32_16x16x32_bf16 v[16:19], a[228:231], v[52:55], v[16:19]\n"
"s_waitcnt lgkmcnt(5)\n"
"ds_read_b64_tr_b16 a[242:243], v247 offset:0\n"
"v_fma_f32 v126, v126, s92, v156\n"
"v_mfma_f32_16x16x32_bf16 v[24:27], a[228:231], v[92:95], 0\n"
"ds_read_b64_tr_b16 a[244:245], v225 offset:0\n"
"v_fma_f32 v127, v127, s92, v156\n"
"v_mfma_f32_16x16x32_bf16 v[24:27], a[224:227], v[28:31], v[24:27]\n"
"s_waitcnt lgkmcnt(5)\n"
"ds_read_b64_tr_b16 a[246:247], v227 offset:0\n"
"v_fma_f32 v132, v132, s92, v157\n"
"v_mfma_f32_16x16x32_bf16 v[220:223], a[232:235], v[28:31], 0\n"
"s_waitcnt lgkmcnt(4)\n"
"ds_read_b64_tr_b16 a[248:249], v245 offset:32\n"
"v_fma_f32 v133, v133, s92, v157\n"
"v_mfma_f32_16x16x32_bf16 v[220:223], a[236:239], v[92:95], v[220:223]\n"
"buffer_load_dwordx4 v248, s[44:47], s80 offen offset:3072 nt lds\n"
"v_mfma_f32_16x16x32_bf16 v[212:215], a[236:239], v[52:55], 0\n"
"s_add_u32 m0, s79, 0x0\n"
"ds_read_b64_tr_b16 a[252:253], v225 offset:32\n"
"v_fma_f32 v134, v134, s92, v157\n"
"v_mfma_f32_16x16x32_bf16 v[212:215], a[232:235], v[20:23], v[212:215]\n"
"s_add_u32 s80, s10, s29\n"
"ds_read_b64_tr_b16 a[254:255], v227 offset:32\n"
"v_fma_f32 v135, v135, s92, v157\n"
"v_mfma_f32_16x16x32_bf16 v[204:207], a[232:235], v[12:15], 0\n"
"s_addk_i32 s10, 0x1000\n"
"ds_read_b64_tr_b16 a[250:251], v247 offset:32\n"
"v_fma_f32 v140, v140, s92, v158\n"
"v_mfma_f32_16x16x32_bf16 v[204:207], a[236:239], v[44:47], v[204:207]\n"
"s_bitset0_b32 s10, 15\n"
"ds_read_b64_tr_b16 a[228:229], v224 offset:128\n"
"v_fma_f32 v141, v141, s92, v158\n"
"v_mfma_f32_16x16x32_bf16 v[84:87], a[236:239], v[36:39], 0\n"
"ds_read_b64_tr_b16 a[224:225], v244 offset:128\n"
"v_fma_f32 v142, v142, s92, v158\n"
"v_mfma_f32_16x16x32_bf16 v[84:87], a[232:235], v[4:7], v[84:87]\n"
"s_waitcnt lgkmcnt(8)\n"
"ds_read_b64_tr_b16 a[226:227], v246 offset:128\n"
"v_fma_f32 v143, v143, s92, v158\n"
"v_mfma_f32_16x16x32_bf16 v[32:35], a[240:243], v[4:7], 0\n"
"s_waitcnt lgkmcnt(7)\n"
"ds_read_b64_tr_b16 a[230:231], v226 offset:128\n"
"v_fma_f32 v200, v200, s92, v156\n"
"v_mfma_f32_16x16x32_bf16 v[32:35], a[244:247], v[36:39], v[32:35]\n"
"ds_read_b64_tr_b16 a[236:237], v224 offset:160\n"
"v_fma_f32 v201, v201, s92, v156\n"
"v_mfma_f32_16x16x32_bf16 v[40:43], a[244:247], v[44:47], 0\n"
"ds_read_b64_tr_b16 a[232:233], v244 offset:160\n"
"v_fma_f32 v202, v202, s92, v156\n"
"v_mfma_f32_16x16x32_bf16 v[40:43], a[240:243], v[12:15], v[40:43]\n"
"ds_read_b64_tr_b16 a[234:235], v246 offset:160\n"
"v_fma_f32 v203, v203, s92, v156\n"
"v_mfma_f32_16x16x32_bf16 v[48:51], a[240:243], v[20:23], 0\n"
"ds_read_b64_tr_b16 a[238:239], v226 offset:160\n"
"v_fma_f32 v208, v208, s92, v157\n"
"v_mfma_f32_16x16x32_bf16 v[48:51], a[244:247], v[52:55], v[48:51]\n"
"s_waitcnt lgkmcnt(9)\n"
"ds_read_b64_tr_b16 a[240:241], v245 offset:128\n"
"v_fma_f32 v209, v209, s92, v157\n"
"v_mfma_f32_16x16x32_bf16 a[52:55], a[252:255], v[52:55], 0\n"
"s_waitcnt lgkmcnt(9)\n"
"ds_read_b64_tr_b16 a[242:243], v247 offset:128\n"
"v_fma_f32 v210, v210, s92, v157\n"
"v_mfma_f32_16x16x32_bf16 a[52:55], a[248:251], v[20:23], a[52:55]\n"
"buffer_load_dwordx4 v248, s[44:47], s81 offen offset:0 nt lds\n"
"v_mfma_f32_16x16x32_bf16 a[44:47], a[248:251], v[12:15], 0\n"
"ds_read_b64_tr_b16 a[244:245], v225 offset:128\n"
"v_fma_f32 v211, v211, s92, v157\n"
"v_mfma_f32_16x16x32_bf16 a[44:47], a[252:255], v[44:47], a[44:47]\n"
"ds_read_b64_tr_b16 a[246:247], v227 offset:128\n"
"v_add_f32 v164, v164, v100\n"
"v_mfma_f32_16x16x32_bf16 a[36:39], a[252:255], v[36:39], 0\n"
"v_exp_f32 v124, v124\n"
"v_mfma_f32_16x16x32_bf16 a[36:39], a[248:251], v[4:7], a[36:39]\n"
"s_waitcnt lgkmcnt(9)\n"
"v_exp_f32 v125, v125\n"
"v_mfma_f32_16x16x32_bf16 v[56:59], a[224:227], v[4:7], 0\n"
"s_waitcnt lgkmcnt(8)\n"
"ds_read_b64_tr_b16 a[252:253], v225 offset:160\n"
"v_add_f32 v164, v164, v101\n"
"v_mfma_f32_16x16x32_bf16 v[56:59], a[228:231], v[36:39], v[56:59]\n"
"ds_read_b64_tr_b16 a[254:255], v227 offset:160\n"
"v_add_f32 v164, v164, v102\n"
"v_mfma_f32_16x16x32_bf16 v[64:67], a[228:231], v[44:47], 0\n"
"ds_read_b64_tr_b16 a[248:249], v245 offset:160\n"
"v_add_f32 v164, v164, v103\n"
"v_mfma_f32_16x16x32_bf16 v[64:67], a[224:227], v[12:15], v[64:67]\n"
"ds_read_b64_tr_b16 a[250:251], v247 offset:160\n"
"v_add_f32 v165, v165, v108\n"
"v_mfma_f32_16x16x32_bf16 v[72:75], a[224:227], v[20:23], 0\n"
"v_exp_f32 v126, v126\n"
"v_mfma_f32_16x16x32_bf16 v[72:75], a[228:231], v[52:55], v[72:75]\n"
"v_exp_f32 v127, v127\n"
"v_mfma_f32_16x16x32_bf16 v[80:83], a[228:231], v[92:95], 0\n"
"ds_read_b64_tr_b16 a[228:229], v224 offset:256\n"
"v_add_f32 v164, v164, v124\n"
"v_mfma_f32_16x16x32_bf16 v[80:83], a[224:227], v[28:31], v[80:83]\n"
"s_waitcnt lgkmcnt(7)\n"
"ds_read_b64_tr_b16 a[224:225], v244 offset:256\n"
"v_add_f32 v165, v165, v109\n"
"v_mfma_f32_16x16x32_bf16 a[84:87], a[232:235], v[28:31], 0\n"
"ds_read_b64_tr_b16 a[226:227], v246 offset:256\n"
"v_add_f32 v164, v164, v125\n"
"v_mfma_f32_16x16x32_bf16 a[84:87], a[236:239], v[92:95], a[84:87]\n"
"ds_read_b64_tr_b16 a[230:231], v226 offset:256\n"
"v_add_f32 v165, v165, v110\n"
"v_mfma_f32_16x16x32_bf16 a[76:79], a[236:239], v[52:55], 0\n"
"buffer_load_dwordx4 v248, s[44:47], s81 offen offset:1024 nt lds\n"
"v_mfma_f32_16x16x32_bf16 a[76:79], a[232:235], v[20:23], a[76:79]\n"
"s_addk_i32 m0, 0x20\n"
"v_exp_f32 v132, v132\n"
"v_mfma_f32_16x16x32_bf16 a[68:71], a[232:235], v[12:15], 0\n"
"v_exp_f32 v133, v133\n"
"v_mfma_f32_16x16x32_bf16 a[68:71], a[236:239], v[44:47], a[68:71]\n"
"v_exp_f32 v134, v134\n"
"v_mfma_f32_16x16x32_bf16 a[60:63], a[236:239], v[36:39], 0\n"
"ds_read_b64_tr_b16 a[236:237], v224 offset:288\n"
"v_add_f32 v164, v164, v126\n"
"v_mfma_f32_16x16x32_bf16 a[60:63], a[232:235], v[4:7], a[60:63]\n"
"ds_read_b64_tr_b16 a[232:233], v244 offset:288\n"
"v_add_f32 v165, v165, v111\n"
"v_mfma_f32_16x16x32_bf16 v[88:91], a[240:243], v[4:7], 0\n"
"s_waitcnt lgkmcnt(6)\n"
"ds_read_b64_tr_b16 a[234:235], v246 offset:288\n"
"v_add_f32 v164, v164, v127\n"
"v_mfma_f32_16x16x32_bf16 v[88:91], a[244:247], v[36:39], v[88:91]\n"
"ds_read_b64_tr_b16 a[238:239], v226 offset:288\n"
"v_add_f32 v166, v166, v116\n"
"v_mfma_f32_16x16x32_bf16 v[96:99], a[244:247], v[44:47], 0\n"
"v_exp_f32 v135, v135\n"
"v_mfma_f32_16x16x32_bf16 v[96:99], a[240:243], v[12:15], v[96:99]\n"
"v_exp_f32 v140, v140\n"
"v_mfma_f32_16x16x32_bf16 v[104:107], a[240:243], v[20:23], 0\n"
"v_exp_f32 v141, v141\n"
"v_mfma_f32_16x16x32_bf16 v[104:107], a[244:247], v[52:55], v[104:107]\n"
"ds_read_b64_tr_b16 a[240:241], v245 offset:256\n"
"v_add_f32 v165, v165, v132\n"
"v_mfma_f32_16x16x32_bf16 a[108:111], a[252:255], v[52:55], 0\n"
"ds_read_b64_tr_b16 a[242:243], v247 offset:256\n"
"v_add_f32 v166, v166, v117\n"
"v_mfma_f32_16x16x32_bf16 a[108:111], a[248:251], v[20:23], a[108:111]\n"
"ds_read_b64_tr_b16 a[244:245], v225 offset:256\n"
"v_add_f32 v165, v165, v133\n"
"v_mfma_f32_16x16x32_bf16 a[100:103], a[248:251], v[12:15], 0\n"
"buffer_load_dwordx4 v248, s[44:47], s81 offen offset:2048 nt lds\n"
"v_mfma_f32_16x16x32_bf16 a[100:103], a[252:255], v[44:47], a[100:103]\n"
"ds_read_b64_tr_b16 a[246:247], v227 offset:256\n"
"v_fma_f32 v216, v216, s92, v158\n"
"v_mfma_f32_16x16x32_bf16 a[92:95], a[252:255], v[36:39], 0\n"
"v_exp_f32 v142, v142\n"
"v_mfma_f32_16x16x32_bf16 a[92:95], a[248:251], v[4:7], a[92:95]\n"
"s_waitcnt lgkmcnt(6)\n"
"v_exp_f32 v143, v143\n"
"v_mfma_f32_16x16x32_bf16 v[112:115], a[224:227], v[4:7], 0\n"
"ds_read_b64_tr_b16 a[252:253], v225 offset:288\n"
"v_add_f32 v166, v166, v118\n"
"v_mfma_f32_16x16x32_bf16 v[112:115], a[228:231], v[36:39], v[112:115]\n"
"ds_read_b64_tr_b16 a[254:255], v227 offset:288\n"
"v_add_f32 v165, v165, v134\n"
"v_mfma_f32_16x16x32_bf16 v[120:123], a[228:231], v[44:47], 0\n"
"ds_read_b64_tr_b16 a[248:249], v245 offset:288\n"
"v_cvt_pk_bf16_f32 v124, v124, v125\n"
"v_mfma_f32_16x16x32_bf16 v[120:123], a[224:227], v[12:15], v[120:123]\n"
"ds_read_b64_tr_b16 a[250:251], v247 offset:288\n"
"v_cvt_pk_bf16_f32 v125, v126, v127\n"
"v_mfma_f32_16x16x32_bf16 v[128:131], a[224:227], v[20:23], 0\n"
"v_exp_f32 v200, v200\n"
"v_mfma_f32_16x16x32_bf16 v[128:131], a[228:231], v[52:55], v[128:131]\n"
"v_exp_f32 v201, v201\n"
"v_mfma_f32_16x16x32_bf16 v[136:139], a[228:231], v[92:95], 0\n"
"ds_read_b64_tr_b16 a[228:229], v224 offset:384\n"
"v_add_f32 v164, v164, v200\n"
"v_mfma_f32_16x16x32_bf16 v[136:139], a[224:227], v[28:31], v[136:139]\n"
"s_waitcnt lgkmcnt(6)\n"
"ds_read_b64_tr_b16 a[224:225], v244 offset:384\n"
"v_add_f32 v164, v164, v201\n"
"v_mfma_f32_16x16x32_bf16 a[140:143], a[232:235], v[28:31], 0\n"
"ds_read_b64_tr_b16 a[226:227], v246 offset:384\n"
"v_add_f32 v166, v166, v119\n"
"v_mfma_f32_16x16x32_bf16 a[140:143], a[236:239], v[92:95], a[140:143]\n"
"ds_read_b64_tr_b16 a[230:231], v226 offset:384\n"
"v_cvt_pk_bf16_f32 v126, v200, v201\n"
"v_mfma_f32_16x16x32_bf16 a[132:135], a[236:239], v[52:55], 0\n"
"v_exp_f32 v202, v202\n"
"v_mfma_f32_16x16x32_bf16 a[132:135], a[232:235], v[20:23], a[132:135]\n"
"buffer_load_dwordx4 v248, s[44:47], s81 offen offset:3072 nt lds\n"
"v_mfma_f32_16x16x32_bf16 a[124:127], a[232:235], v[12:15], 0\n"
"s_add_u32 m0, s79, 0x81c0\n"
"v_exp_f32 v203, v203\n"
"v_mfma_f32_16x16x32_bf16 a[124:127], a[236:239], v[44:47], a[124:127]\n"
"s_add_u32 s81, s10, s29\n"
"v_exp_f32 v208, v208\n"
"v_mfma_f32_16x16x32_bf16 a[116:119], a[236:239], v[36:39], 0\n"
"s_addk_i32 s10, 0x1000\n"
"ds_read_b64_tr_b16 a[236:237], v224 offset:416\n"
"v_add_f32 v164, v164, v202\n"
"v_mfma_f32_16x16x32_bf16 a[116:119], a[232:235], v[4:7], a[116:119]\n"
"s_bitset0_b32 s10, 15\n"
"ds_read_b64_tr_b16 a[232:233], v244 offset:416\n"
"v_add_f32 v164, v164, v203\n"
"v_mfma_f32_16x16x32_bf16 v[144:147], a[240:243], v[4:7], 0\n"
"s_waitcnt lgkmcnt(6)\n"
"ds_read_b64_tr_b16 a[234:235], v246 offset:416\n"
"v_cvt_pk_bf16_f32 v127, v202, v203\n"
"v_mfma_f32_16x16x32_bf16 v[144:147], a[244:247], v[36:39], v[144:147]\n"
"ds_read_b64_tr_b16 a[238:239], v226 offset:416\n"
"v_add_f32 v165, v165, v135\n"
"v_mfma_f32_16x16x32_bf16 v[152:155], a[244:247], v[44:47], 0\n"
"v_exp_f32 v209, v209\n"
"v_mfma_f32_16x16x32_bf16 v[152:155], a[240:243], v[12:15], v[152:155]\n"
"v_exp_f32 v210, v210\n"
"v_mfma_f32_16x16x32_bf16 v[160:163], a[240:243], v[20:23], 0\n"
"v_exp_f32 v211, v211\n"
"v_mfma_f32_16x16x32_bf16 v[160:163], a[244:247], v[52:55], v[160:163]\n"
"ds_read_b64_tr_b16 a[240:241], v245 offset:384\n"
"v_fma_f32 v217, v217, s92, v158\n"
"v_mfma_f32_16x16x32_bf16 a[164:167], a[252:255], v[52:55], 0\n"
"ds_read_b64_tr_b16 a[242:243], v247 offset:384\n"
"v_add_f32 v165, v165, v208\n"
"v_mfma_f32_16x16x32_bf16 a[164:167], a[248:251], v[20:23], a[164:167]\n"
"ds_read_b64_tr_b16 a[244:245], v225 offset:384\n"
"v_add_f32 v166, v166, v140\n"
"v_mfma_f32_16x16x32_bf16 a[156:159], a[248:251], v[12:15], 0\n"
"ds_read_b64_tr_b16 a[246:247], v227 offset:384\n"
"v_add_f32 v165, v165, v209\n"
"v_mfma_f32_16x16x32_bf16 a[156:159], a[252:255], v[44:47], a[156:159]\n"
"buffer_load_dwordx4 v248, s[48:51], s80 offen offset:0 nt lds\n"
"v_mfma_f32_16x16x32_bf16 a[148:151], a[252:255], v[36:39], 0\n"
"ds_read_b64_tr_b16 a[252:253], v225 offset:416\n"
"v_add_f32 v166, v166, v141\n"
"v_mfma_f32_16x16x32_bf16 a[148:151], a[248:251], v[4:7], a[148:151]\n"
"s_waitcnt lgkmcnt(7)\n"
"ds_read_b64_tr_b16 a[254:255], v227 offset:416\n"
"v_fma_f32 v218, v218, s92, v158\n"
"v_mfma_f32_16x16x32_bf16 v[168:171], a[224:227], v[4:7], 0\n"
"ds_read_b64_tr_b16 a[248:249], v245 offset:416\n"
"v_add_f32 v165, v165, v210\n"
"v_mfma_f32_16x16x32_bf16 v[168:171], a[228:231], v[36:39], v[168:171]\n"
"ds_read_b64_tr_b16 a[250:251], v247 offset:416\n"
"v_add_f32 v166, v166, v142\n"
"v_mfma_f32_16x16x32_bf16 v[176:179], a[228:231], v[44:47], 0\n"
"v_exp_f32 v216, v216\n"
"v_mfma_f32_16x16x32_bf16 v[176:179], a[224:227], v[12:15], v[176:179]\n"
"v_exp_f32 v217, v217\n"
"v_mfma_f32_16x16x32_bf16 v[184:187], a[224:227], v[20:23], 0\n"
"v_cvt_pk_bf16_f32 v132, v132, v133\n"
"v_add_f32 v165, v165, v211\n"
"v_mfma_f32_16x16x32_bf16 v[184:187], a[228:231], v[52:55], v[184:187]\n"
"v_cvt_pk_bf16_f32 v133, v134, v135\n"
"v_add_f32 v166, v166, v143\n"
"v_mfma_f32_16x16x32_bf16 v[192:195], a[228:231], v[92:95], 0\n"
"v_cvt_pk_bf16_f32 v134, v208, v209\n"
"v_cvt_pk_bf16_f32 v135, v210, v211\n"
"v_mfma_f32_16x16x32_bf16 v[192:195], a[224:227], v[28:31], v[192:195]\n"
"s_waitcnt lgkmcnt(5)\n"
"v_fma_f32 v219, v219, s92, v158\n"
"v_add_f32 v166, v166, v216\n"
"v_mfma_f32_16x16x32_bf16 a[196:199], a[232:235], v[28:31], 0\n"
"v_exp_f32 v218, v218\n"
"v_mfma_f32_16x16x32_bf16 a[196:199], a[236:239], v[92:95], a[196:199]\n"
"v_add_f32 v166, v166, v217\n"
"v_cvt_pk_bf16_f32 v140, v140, v141\n"
"v_mfma_f32_16x16x32_bf16 a[188:191], a[236:239], v[52:55], 0\n"
"v_exp_f32 v219, v219\n"
"v_mfma_f32_16x16x32_bf16 a[188:191], a[232:235], v[20:23], a[188:191]\n"
"v_add_f32 v166, v166, v218\n"
"v_cvt_pk_bf16_f32 v141, v142, v143\n"
"v_mfma_f32_16x16x32_bf16 a[180:183], a[232:235], v[12:15], 0\n"
"buffer_load_dwordx4 v248, s[48:51], s80 offen offset:1024 nt lds\n"
"v_mfma_f32_16x16x32_bf16 a[180:183], a[236:239], v[44:47], a[180:183]\n"
"s_addk_i32 m0, 0x20\n"
"v_add_f32 v166, v166, v219\n"
"v_cvt_pk_bf16_f32 v142, v216, v217\n"
"v_mfma_f32_16x16x32_bf16 a[172:175], a[236:239], v[36:39], 0\n"
"v_cvt_pk_bf16_f32 v143, v218, v219\n"
"v_add_u32 v240, 0x20700, v240\n"
"v_mfma_f32_16x16x32_bf16 a[172:175], a[232:235], v[4:7], a[172:175]\n"
"s_waitcnt vmcnt(18) lgkmcnt(0)\n"
"s_barrier\n"
"v_mfma_f32_16x16x32_bf16 v[200:203], a[240:243], v[4:7], 0\n"
"ds_read_b128 a[224:227], v240 offset:0\n"
"v_add_u32 v241, 0x20700, v241\n"
"v_mfma_f32_16x16x32_bf16 v[200:203], a[244:247], v[36:39], v[200:203]\n"
"ds_read_b128 a[228:231], v241 offset:0\n"
"v_add_u32 v242, 0x20700, v242\n"
"v_mfma_f32_16x16x32_bf16 v[208:211], a[244:247], v[44:47], 0\n"
"ds_read_b128 a[232:235], v240 offset:128\n"
"v_add_u32 v243, 0x20700, v243\n"
"v_mfma_f32_16x16x32_bf16 v[208:211], a[240:243], v[12:15], v[208:211]\n"
"ds_read_b128 a[236:239], v241 offset:128\n"
"v_fma_f32 v172, v172, s92, v159\n"
"v_mfma_f32_16x16x32_bf16 v[216:219], a[240:243], v[20:23], 0\n"
"ds_read_b128 a[240:243], v240 offset:256\n"
"v_fma_f32 v173, v173, s92, v159\n"
"v_mfma_f32_16x16x32_bf16 v[216:219], a[244:247], v[52:55], v[216:219]\n"
"ds_read_b128 a[244:247], v241 offset:256\n"
"v_fma_f32 v174, v174, s92, v159\n"
"v_mfma_f32_16x16x32_bf16 v[236:239], a[252:255], v[52:55], 0\n"
"v_exp_f32 v172, v172\n"
"v_mfma_f32_16x16x32_bf16 v[236:239], a[248:251], v[20:23], v[236:239]\n"
"v_exp_f32 v173, v173\n"
"v_mfma_f32_16x16x32_bf16 v[232:235], a[248:251], v[12:15], 0\n"
"v_exp_f32 v174, v174\n"
"v_mfma_f32_16x16x32_bf16 v[232:235], a[252:255], v[44:47], v[232:235]\n"
"v_fma_f32 v175, v175, s92, v159\n"
"v_add_f32 v167, v167, v172\n"
"v_mfma_f32_16x16x32_bf16 v[228:231], a[252:255], v[36:39], 0\n"
"ds_read_b128 a[252:255], v241 offset:384\n"
"v_add_f32 v167, v167, v173\n"
"v_mfma_f32_16x16x32_bf16 v[228:231], a[248:251], v[4:7], v[228:231]\n"
"s_waitcnt lgkmcnt(6)\n"
"buffer_load_dwordx4 v248, s[48:51], s80 offen offset:2048 nt lds\n"
"v_mfma_f32_16x16x32_bf16 v[4:7], a[224:227], a[0:3], 0\n"
"s_waitcnt lgkmcnt(5)\n"
"ds_read_b128 a[248:251], v240 offset:384\n"
"v_add_u32 v241, 0xfffebba0, v241\n"
"v_mfma_f32_16x16x32_bf16 v[4:7], a[228:231], a[32:35], v[4:7]\n"
"v_exp_f32 v175, v175\n"
"v_mfma_f32_16x16x32_bf16 v[12:15], a[228:231], a[40:43], 0\n"
"v_add_u32 v240, 0xfffebba0, v240\n"
"v_cvt_pk_bf16_f32 v172, v172, v173\n"
"v_mfma_f32_16x16x32_bf16 v[12:15], a[224:227], a[8:11], v[12:15]\n"
"v_cvt_pk_bf16_f32 v173, v174, v175\n"
"v_fma_f32 v180, v180, s92, v156\n"
"v_mfma_f32_16x16x32_bf16 v[20:23], a[224:227], a[16:19], 0\n"
"ds_write_b64 v253, v[172:173] offset:1024\n"
"v_fma_f32 v181, v181, s92, v156\n"
"v_mfma_f32_16x16x32_bf16 v[20:23], a[228:231], a[48:51], v[20:23]\n"
"v_exp_f32 v180, v180\n"
"v_mfma_f32_16x16x32_bf16 v[28:31], a[228:231], a[4:7], 0\n"
"ds_read_b128 a[228:231], v243 offset:0\n"
"v_fma_f32 v182, v182, s92, v156\n"
"v_mfma_f32_16x16x32_bf16 v[28:31], a[224:227], a[24:27], v[28:31]\n"
"s_waitcnt lgkmcnt(4)\n"
"ds_read_b128 a[224:227], v242 offset:0\n"
"v_fma_f32 v183, v183, s92, v156\n"
"v_mfma_f32_16x16x32_bf16 v[4:7], a[232:235], a[56:59], v[4:7]\n"
"v_exp_f32 v181, v181\n"
"v_mfma_f32_16x16x32_bf16 v[4:7], a[236:239], a[88:91], v[4:7]\n"
"v_exp_f32 v182, v182\n"
"v_mfma_f32_16x16x32_bf16 v[12:15], a[236:239], a[96:99], v[12:15]\n"
"v_exp_f32 v183, v183\n"
"v_mfma_f32_16x16x32_bf16 v[12:15], a[232:235], a[64:67], v[12:15]\n"
"v_fma_f32 v188, v188, s92, v157\n"
"v_fma_f32 v189, v189, s92, v157\n"
"v_mfma_f32_16x16x32_bf16 v[20:23], a[232:235], a[72:75], v[20:23]\n"
"v_exp_f32 v188, v188\n"
"v_mfma_f32_16x16x32_bf16 v[20:23], a[236:239], a[104:107], v[20:23]\n"
"buffer_load_dwordx4 v248, s[48:51], s80 offen offset:3072 nt lds\n"
"v_mfma_f32_16x16x32_bf16 v[28:31], a[236:239], a[12:15], v[28:31]\n"
"s_add_u32 m0, s79, 0x10380\n"
"ds_read_b128 a[236:239], v243 offset:128\n"
"v_fma_f32 v190, v190, s92, v157\n"
"v_mfma_f32_16x16x32_bf16 v[28:31], a[232:235], a[80:83], v[28:31]\n"
"s_add_u32 s80, s9, s24\n"
"ds_read_b128 a[232:235], v242 offset:128\n"
"v_fma_f32 v191, v191, s92, v157\n"
"v_mfma_f32_16x16x32_bf16 v[4:7], a[240:243], a[112:115], v[4:7]\n"
"s_addk_i32 s9, 0x1000\n"
"v_exp_f32 v189, v189\n"
"v_mfma_f32_16x16x32_bf16 v[4:7], a[244:247], a[144:147], v[4:7]\n"
"s_bitset0_b32 s9, 15\n"
"v_exp_f32 v190, v190\n"
"v_mfma_f32_16x16x32_bf16 v[12:15], a[244:247], a[152:155], v[12:15]\n"
"v_exp_f32 v191, v191\n"
"v_mfma_f32_16x16x32_bf16 v[12:15], a[240:243], a[120:123], v[12:15]\n"
"v_fma_f32 v196, v196, s92, v158\n"
"v_fma_f32 v197, v197, s92, v158\n"
"v_mfma_f32_16x16x32_bf16 v[20:23], a[240:243], a[128:131], v[20:23]\n"
"v_exp_f32 v196, v196\n"
"v_mfma_f32_16x16x32_bf16 v[20:23], a[244:247], a[160:163], v[20:23]\n"
"v_exp_f32 v197, v197\n"
"v_mfma_f32_16x16x32_bf16 v[28:31], a[244:247], a[20:23], v[28:31]\n"
"ds_read_b128 a[244:247], v243 offset:256\n"
"v_fma_f32 v198, v198, s92, v158\n"
"v_mfma_f32_16x16x32_bf16 v[28:31], a[240:243], a[136:139], v[28:31]\n"
"s_waitcnt lgkmcnt(3)\n"
"ds_read_b128 a[240:243], v242 offset:256\n"
"v_fma_f32 v199, v199, s92, v158\n"
"v_mfma_f32_16x16x32_bf16 v[4:7], a[248:251], a[168:171], v[4:7]\n"
"v_exp_f32 v198, v198\n"
"v_mfma_f32_16x16x32_bf16 v[4:7], a[252:255], a[200:203], v[4:7]\n"
"v_fma_f32 v148, v148, s92, v159\n"
"v_fma_f32 v149, v149, s92, v159\n"
"v_mfma_f32_16x16x32_bf16 v[12:15], a[252:255], a[208:211], v[12:15]\n"
"v_exp_f32 v199, v199\n"
"v_mfma_f32_16x16x32_bf16 v[12:15], a[248:251], a[176:179], v[12:15]\n"
"v_exp_f32 v148, v148\n"
"v_mfma_f32_16x16x32_bf16 v[20:23], a[248:251], a[184:187], v[20:23]\n"
"buffer_load_dwordx4 v248, s[48:51], s81 offen offset:0 nt lds\n"
"v_mfma_f32_16x16x32_bf16 v[20:23], a[252:255], a[216:219], v[20:23]\n"
"v_exp_f32 v149, v149\n"
"v_mfma_f32_16x16x32_bf16 v[28:31], a[252:255], a[28:31], v[28:31]\n"
"ds_read_b128 a[252:255], v243 offset:384\n"
"v_fma_f32 v150, v150, s92, v159\n"
"v_mfma_f32_16x16x32_bf16 v[28:31], a[248:251], a[192:195], v[28:31]\n"
"ds_read_b128 a[248:251], v242 offset:384\n"
"v_fma_f32 v60, v60, s92, v156\n"
"v_mfma_f32_16x16x32_bf16 v[100:103], a[224:227], a[0:3], 0\n"
"v_exp_f32 v150, v150\n"
"v_mfma_f32_16x16x32_bf16 v[100:103], a[228:231], a[32:35], v[100:103]\n"
"v_exp_f32 v60, v60\n"
"v_mfma_f32_16x16x32_bf16 v[108:111], a[228:231], a[40:43], 0\n"
"v_fma_f32 v61, v61, s92, v156\n"
"v_fma_f32 v62, v62, s92, v156\n"
"v_mfma_f32_16x16x32_bf16 v[108:111], a[224:227], a[8:11], v[108:111]\n"
"PG6_%=:\n"
"s_cbranch_execz PGB1_%=\n"
"v_exp_f32 v61, v61\n"
"v_mfma_f32_16x16x32_bf16 v[116:119], a[224:227], a[16:19], 0\n"
"ds_read_b128 a[224:227], v240 offset:0\n"
"v_fma_f32 v63, v63, s92, v156\n"
"v_mfma_f32_16x16x32_bf16 v[116:119], a[228:231], a[48:51], v[116:119]\n"
"s_waitcnt lgkmcnt(3)\n"
"ds_read_b128 a[228:231], v241 offset:0\n"
"v_fma_f32 v68, v68, s92, v157\n"
"v_mfma_f32_16x16x32_bf16 v[100:103], a[232:235], a[56:59], v[100:103]\n"
"v_exp_f32 v62, v62\n"
"v_mfma_f32_16x16x32_bf16 v[100:103], a[236:239], a[88:91], v[100:103]\n"
"v_exp_f32 v63, v63\n"
"v_mfma_f32_16x16x32_bf16 v[108:111], a[236:239], a[96:99], v[108:111]\n"
"v_exp_f32 v68, v68\n"
"v_mfma_f32_16x16x32_bf16 v[108:111], a[232:235], a[64:67], v[108:111]\n"
"v_fma_f32 v69, v69, s92, v157\n"
"v_add_u32 v242, 0xfffebba0, v242\n"
"v_mfma_f32_16x16x32_bf16 v[116:119], a[232:235], a[72:75], v[116:119]\n"
"buffer_load_dwordx4 v248, s[48:51], s81 offen offset:1024 nt lds\n"
"v_mfma_f32_16x16x32_bf16 v[116:119], a[236:239], a[104:107], v[116:119]\n"
"s_addk_i32 m0, 0x20\n"
"ds_read_b128 a[232:235], v240 offset:128\n"
"v_add_u32 v243, 0xfffebba0, v243\n"
"v_mfma_f32_16x16x32_bf16 v[100:103], a[240:243], a[112:115], v[100:103]\n"
"ds_read_b128 a[236:239], v241 offset:128\n"
"v_fma_f32 v70, v70, s92, v157\n"
"v_mfma_f32_16x16x32_bf16 v[100:103], a[244:247], a[144:147], v[100:103]\n"
"v_exp_f32 v69, v69\n"
"v_mfma_f32_16x16x32_bf16 v[108:111], a[244:247], a[152:155], v[108:111]\n"
"v_exp_f32 v70, v70\n"
"v_mfma_f32_16x16x32_bf16 v[108:111], a[240:243], a[120:123], v[108:111]\n"
"v_fma_f32 v71, v71, s92, v157\n"
"v_add_f32 v164, v164, v180\n"
"v_mfma_f32_16x16x32_bf16 v[116:119], a[240:243], a[128:131], v[116:119]\n"
"ds_read_b128 a[240:243], v240 offset:256\n"
"v_add_f32 v164, v164, v181\n"
"v_mfma_f32_16x16x32_bf16 v[116:119], a[244:247], a[160:163], v[116:119]\n"
"s_waitcnt lgkmcnt(3)\n"
"ds_read_b128 a[244:247], v241 offset:256\n"
"v_fma_f32 v76, v76, s92, v158\n"
"v_mfma_f32_16x16x32_bf16 v[100:103], a[248:251], a[168:171], v[100:103]\n"
"v_exp_f32 v71, v71\n"
"v_mfma_f32_16x16x32_bf16 v[100:103], a[252:255], a[200:203], v[100:103]\n"
"v_add_f32 v164, v164, v182\n"
"v_fma_f32 v77, v77, s92, v158\n"
"v_mfma_f32_16x16x32_bf16 v[108:111], a[252:255], a[208:211], v[108:111]\n"
"v_add_f32 v164, v164, v183\n"
"v_add_f32 v165, v165, v188\n"
"v_mfma_f32_16x16x32_bf16 v[108:111], a[248:251], a[176:179], v[108:111]\n"
"v_exp_f32 v76, v76\n"
"v_mfma_f32_16x16x32_bf16 v[116:119], a[248:251], a[184:187], v[116:119]\n"
"ds_read_b128 a[248:251], v240 offset:384\n"
"v_add_f32 v164, v164, v60\n"
"v_mfma_f32_16x16x32_bf16 v[116:119], a[252:255], a[216:219], v[116:119]\n"
"ds_read_b128 a[252:255], v241 offset:384\n"
"v_add_f32 v164, v164, v61\n"
"v_mfma_f32_16x16x32_bf16 v[36:39], a[224:227], a[0:3], 0\n"
"v_add_f32 v165, v165, v189\n"
"v_add_f32 v164, v164, v62\n"
"v_mfma_f32_16x16x32_bf16 v[36:39], a[228:231], a[32:35], v[36:39]\n"
"buffer_load_dwordx4 v248, s[48:51], s81 offen offset:2048 nt lds\n"
"v_mfma_f32_16x16x32_bf16 v[44:47], a[228:231], a[40:43], 0\n"
"s_cmp_eq_u32 s11, 1\n"
"s_cbranch_scc1 FALL_%=\n"
"v_mfma_f32_16x16x32_bf16 v[44:47], a[224:227], a[8:11], v[44:47]\n"
"v_exp_f32 v77, v77\n"
"v_mfma_f32_16x16x32_bf16 v[52:55], a[224:227], a[16:19], 0\n"
"v_add_f32 v165, v165, v190\n"
"v_cvt_pk_bf16_f32 v180, v180, v181\n"
"v_mfma_f32_16x16x32_bf16 v[52:55], a[228:231], a[48:51], v[52:55]\n"
"v_cvt_pk_bf16_f32 v181, v182, v183\n"
"v_add_f32 v164, v164, v63\n"
"v_mfma_f32_16x16x32_bf16 v[92:95], a[228:231], a[4:7], 0\n"
"ds_read_b128 a[228:231], v243 offset:0\n"
"v_cvt_pk_bf16_f32 v182, v60, v61\n"
"v_mfma_f32_16x16x32_bf16 v[92:95], a[224:227], a[24:27], v[92:95]\n"
"s_waitcnt lgkmcnt(3)\n"
"ds_read_b128 a[224:227], v242 offset:0\n"
"v_cvt_pk_bf16_f32 v183, v62, v63\n"
"v_mfma_f32_16x16x32_bf16 v[36:39], a[232:235], a[56:59], v[36:39]\n"
"v_add_f32 v165, v165, v191\n"
"v_add_f32 v166, v166, v196\n"
"v_mfma_f32_16x16x32_bf16 v[36:39], a[236:239], a[88:91], v[36:39]\n"
"v_add_f32 v165, v165, v68\n"
"v_add_f32 v166, v166, v197\n"
"v_mfma_f32_16x16x32_bf16 v[44:47], a[236:239], a[96:99], v[44:47]\n"
"v_add_f32 v165, v165, v69\n"
"v_fma_f32 v78, v78, s92, v158\n"
"v_mfma_f32_16x16x32_bf16 v[44:47], a[232:235], a[64:67], v[44:47]\n"
"v_add_f32 v165, v165, v70\n"
"v_add_f32 v166, v166, v198\n"
"v_mfma_f32_16x16x32_bf16 v[52:55], a[232:235], a[72:75], v[52:55]\n"
"v_cvt_pk_bf16_f32 v188, v188, v189\n"
"v_add_f32 v165, v165, v71\n"
"v_mfma_f32_16x16x32_bf16 v[52:55], a[236:239], a[104:107], v[52:55]\n"
"v_cvt_pk_bf16_f32 v189, v190, v191\n"
"v_add_f32 v166, v166, v199\n"
"v_mfma_f32_16x16x32_bf16 v[92:95], a[236:239], a[12:15], v[92:95]\n"
"ds_read_b128 a[236:239], v243 offset:128\n"
"v_cvt_pk_bf16_f32 v190, v68, v69\n"
"v_mfma_f32_16x16x32_bf16 v[92:95], a[232:235], a[80:83], v[92:95]\n"
"buffer_load_dwordx4 v248, s[48:51], s81 offen offset:3072 nt lds\n"
"v_mfma_f32_16x16x32_bf16 v[36:39], a[240:243], a[112:115], v[36:39]\n"
"s_add_u32 m0, s79, 0x18540\n"
"s_add_u32 s81, s9, s24\n"
"ds_read_b128 a[232:235], v242 offset:128\n"
"v_mfma_f32_16x16x32_bf16 v[36:39], a[244:247], a[144:147], v[36:39]\n"
"s_addk_i32 s9, 0x1000\n"
"v_cvt_pk_bf16_f32 v191, v70, v71\n"
"v_fma_f32 v79, v79, s92, v158\n"
"v_mfma_f32_16x16x32_bf16 v[44:47], a[244:247], a[152:155], v[44:47]\n"
"s_bitset0_b32 s9, 15\n"
"v_exp_f32 v78, v78\n"
"v_mfma_f32_16x16x32_bf16 v[44:47], a[240:243], a[120:123], v[44:47]\n"
"v_add_f32 v166, v166, v76\n"
"v_fma_f32 v151, v151, s92, v159\n"
"v_mfma_f32_16x16x32_bf16 v[52:55], a[240:243], a[128:131], v[52:55]\n"
"v_add_f32 v166, v166, v77\n"
"v_cvt_pk_bf16_f32 v196, v196, v197\n"
"v_mfma_f32_16x16x32_bf16 v[52:55], a[244:247], a[160:163], v[52:55]\n"
"v_exp_f32 v79, v79\n"
"v_mfma_f32_16x16x32_bf16 v[92:95], a[244:247], a[20:23], v[92:95]\n"
"v_add_f32 v166, v166, v78\n"
"v_cvt_pk_bf16_f32 v197, v198, v199\n"
"v_mfma_f32_16x16x32_bf16 v[92:95], a[240:243], a[136:139], v[92:95]\n"
"s_waitcnt lgkmcnt(2)\n"
"v_add_f32 v166, v166, v79\n"
"v_cvt_pk_bf16_f32 v198, v76, v77\n"
"v_mfma_f32_16x16x32_bf16 v[36:39], a[248:251], a[168:171], v[36:39]\n"
"ds_read_b128 a[244:247], v243 offset:256\n"
"v_cvt_pk_bf16_f32 v199, v78, v79\n"
"v_mfma_f32_16x16x32_bf16 v[36:39], a[252:255], a[200:203], v[36:39]\n"
"v_exp_f32 v151, v151\n"
"v_mfma_f32_16x16x32_bf16 v[44:47], a[252:255], a[208:211], v[44:47]\n"
"ds_read_b128 a[240:243], v242 offset:256\n"
"v_add_f32 v167, v167, v174\n"
"v_mfma_f32_16x16x32_bf16 v[44:47], a[248:251], a[176:179], v[44:47]\n"
"v_add_f32 v167, v167, v175\n"
"v_add_u32 v244, 0xffff3d60, v244\n"
"v_mfma_f32_16x16x32_bf16 v[52:55], a[248:251], a[184:187], v[52:55]\n"
"v_add_f32 v167, v167, v148\n"
"v_add_u32 v246, 0xffff3d60, v246\n"
"v_mfma_f32_16x16x32_bf16 v[52:55], a[252:255], a[216:219], v[52:55]\n"
"v_add_f32 v167, v167, v149\n"
"v_cvt_pk_bf16_f32 v148, v148, v149\n"
"v_mfma_f32_16x16x32_bf16 v[92:95], a[252:255], a[28:31], v[92:95]\n"
"buffer_load_dwordx4 v248, s[44:47], s80 offen offset:0 nt lds\n"
"v_mfma_f32_16x16x32_bf16 v[92:95], a[248:251], a[192:195], v[92:95]\n"
"ds_read_b128 a[252:255], v243 offset:384\n"
"v_mfma_f32_16x16x32_bf16 v[60:63], a[224:227], a[0:3], 0\n"
"ds_read_b128 a[248:251], v242 offset:384\n"
"v_cvt_pk_bf16_f32 v149, v150, v151\n"
"v_mfma_f32_16x16x32_bf16 v[60:63], a[228:231], a[32:35], v[60:63]\n"
"ds_write_b64 v253, v[148:149] offset:1536\n"
"v_add_u32 v224, 0xffffbf20, v224\n"
"v_mfma_f32_16x16x32_bf16 v[68:71], a[228:231], a[40:43], 0\n"
"v_add_u32 v226, 0xffffbf20, v226\n"
"v_add_f32 v167, v167, v150\n"
"v_mfma_f32_16x16x32_bf16 v[68:71], a[224:227], a[8:11], v[68:71]\n"
"v_add_f32 v167, v167, v151\n"
"v_add_u32 v245, 0xffff3d60, v245\n"
"v_mfma_f32_16x16x32_bf16 v[76:79], a[224:227], a[16:19], 0\n"
"v_add_u32 v247, 0xffff3d60, v247\n"
"v_add_u32 v225, 0xffffbf20, v225\n"
"v_mfma_f32_16x16x32_bf16 v[76:79], a[228:231], a[48:51], v[76:79]\n"
"s_waitcnt lgkmcnt(4)\n"
"v_add_u32 v227, 0xffffbf20, v227\n"
"v_add_u32 v240, 0x10380, v240\n"
"v_mfma_f32_16x16x32_bf16 v[60:63], a[232:235], a[56:59], v[60:63]\n"
"s_waitcnt vmcnt(17) lgkmcnt(0)\n"
"s_barrier\n"
"v_mfma_f32_16x16x32_bf16 v[60:63], a[236:239], a[88:91], v[60:63]\n"
"ds_read_b64_tr_b16 a[224:225], v244 offset:0\n"
"v_add_u32 v241, 0x10380, v241\n"
"v_mfma_f32_16x16x32_bf16 v[68:71], a[236:239], a[96:99], v[68:71]\n"
"ds_read_b64_tr_b16 a[226:227], v246 offset:0\n"
"v_fma_f32 v4, v4, s92, v156\n"
"v_mfma_f32_16x16x32_bf16 v[68:71], a[232:235], a[64:67], v[68:71]\n"
"ds_read_b64_tr_b16 a[228:229], v224 offset:0\n"
"v_fma_f32 v5, v5, s92, v156\n"
"v_mfma_f32_16x16x32_bf16 v[76:79], a[232:235], a[72:75], v[76:79]\n"
"ds_read_b64_tr_b16 a[230:231], v226 offset:0\n"
"v_fma_f32 v6, v6, s92, v156\n"
"v_mfma_f32_16x16x32_bf16 v[76:79], a[236:239], a[104:107], v[76:79]\n"
"ds_read_b64 v[174:175], v254 offset:1024\n"
"v_fma_f32 v7, v7, s92, v156\n"
"v_mfma_f32_16x16x32_bf16 v[60:63], a[240:243], a[112:115], v[60:63]\n"
"buffer_load_dwordx4 v248, s[44:47], s80 offen offset:1024 nt lds\n"
"v_mfma_f32_16x16x32_bf16 v[60:63], a[244:247], a[144:147], v[60:63]\n"
"s_addk_i32 m0, 0x20\n"
"ds_read_b64 v[150:151], v254 offset:1536\n"
"v_mfma_f32_16x16x32_bf16 v[68:71], a[244:247], a[152:155], v[68:71]\n"
"ds_read_b64_tr_b16 a[232:233], v244 offset:32\n"
"v_fma_f32 v12, v12, s92, v157\n"
"v_mfma_f32_16x16x32_bf16 v[68:71], a[240:243], a[120:123], v[68:71]\n"
"ds_read_b64_tr_b16 a[234:235], v246 offset:32\n"
"v_fma_f32 v13, v13, s92, v157\n"
"v_mfma_f32_16x16x32_bf16 v[76:79], a[240:243], a[128:131], v[76:79]\n"
"ds_read_b64_tr_b16 a[236:237], v224 offset:32\n"
"v_fma_f32 v14, v14, s92, v157\n"
"v_mfma_f32_16x16x32_bf16 v[76:79], a[244:247], a[160:163], v[76:79]\n"
"ds_read_b64_tr_b16 a[238:239], v226 offset:32\n"
"v_fma_f32 v15, v15, s92, v157\n"
"v_mfma_f32_16x16x32_bf16 v[60:63], a[248:251], a[168:171], v[60:63]\n"
"ds_read_b64_tr_b16 a[240:241], v245 offset:0\n"
"v_fma_f32 v20, v20, s92, v158\n"
"v_mfma_f32_16x16x32_bf16 v[60:63], a[252:255], a[200:203], v[60:63]\n"
"ds_read_b64_tr_b16 a[242:243], v247 offset:0\n"
"v_fma_f32 v21, v21, s92, v158\n"
"v_mfma_f32_16x16x32_bf16 v[68:71], a[252:255], a[208:211], v[68:71]\n"
"ds_read_b64_tr_b16 a[244:245], v225 offset:0\n"
"v_fma_f32 v22, v22, s92, v158\n"
"v_mfma_f32_16x16x32_bf16 v[68:71], a[248:251], a[176:179], v[68:71]\n"
"ds_read_b64_tr_b16 a[246:247], v227 offset:0\n"
"v_fma_f32 v23, v23, s92, v158\n"
"v_mfma_f32_16x16x32_bf16 v[76:79], a[248:251], a[184:187], v[76:79]\n"
"ds_read_b64_tr_b16 a[248:249], v245 offset:32\n"
"v_fma_f32 v100, v100, s92, v156\n"
"v_mfma_f32_16x16x32_bf16 v[76:79], a[252:255], a[216:219], v[76:79]\n"
"s_waitcnt lgkmcnt(10)\n"
"ds_read_b64_tr_b16 a[252:253], v225 offset:32\n"
"v_fma_f32 v101, v101, s92, v156\n"
"v_mfma_f32_16x16x32_bf16 v[0:3], a[224:227], v[124:127], v[0:3]\n"
"ds_read_b64_tr_b16 a[254:255], v227 offset:32\n"
"v_fma_f32 v102, v102, s92, v156\n"
"v_mfma_f32_16x16x32_bf16 v[0:3], a[228:231], v[180:183], v[0:3]\n"
"ds_read_b64_tr_b16 a[250:251], v247 offset:32\n"
"v_fma_f32 v103, v103, s92, v156\n"
"v_mfma_f32_16x16x32_bf16 v[8:11], a[228:231], v[188:191], v[8:11]\n"
"v_exp_f32 v4, v4\n"
"v_mfma_f32_16x16x32_bf16 v[8:11], a[224:227], v[132:135], v[8:11]\n"
"buffer_load_dwordx4 v248, s[44:47], s80 offen offset:2048 nt lds\n"
"v_mfma_f32_16x16x32_bf16 v[16:19], a[224:227], v[140:143], v[16:19]\n"
"v_mfma_f32_16x16x32_bf16 v[16:19], a[228:231], v[196:199], v[16:19]\n"
"s_waitcnt lgkmcnt(8)\n"
"v_exp_f32 v5, v5\n"
"v_mfma_f32_16x16x32_bf16 v[24:27], a[228:231], v[148:151], v[24:27]\n"
"ds_read_b64_tr_b16 a[228:229], v224 offset:128\n"
"v_fma_f32 v108, v108, s92, v157\n"
"v_mfma_f32_16x16x32_bf16 v[24:27], a[224:227], v[172:175], v[24:27]\n"
"ds_read_b64_tr_b16 a[224:225], v244 offset:128\n"
"v_fma_f32 v109, v109, s92, v157\n"
"v_mfma_f32_16x16x32_bf16 v[220:223], a[232:235], v[172:175], v[220:223]\n"
"ds_read_b64_tr_b16 a[226:227], v246 offset:128\n"
"v_fma_f32 v110, v110, s92, v157\n"
"v_mfma_f32_16x16x32_bf16 v[220:223], a[236:239], v[148:151], v[220:223]\n"
"ds_read_b64_tr_b16 a[230:231], v226 offset:128\n"
"v_add_u32 v242, 0x10380, v242\n"
"v_mfma_f32_16x16x32_bf16 v[212:215], a[236:239], v[196:199], v[212:215]\n"
"v_exp_f32 v6, v6\n"
"v_mfma_f32_16x16x32_bf16 v[212:215], a[232:235], v[140:143], v[212:215]\n"
"v_exp_f32 v7, v7\n"
"v_mfma_f32_16x16x32_bf16 v[204:207], a[232:235], v[132:135], v[204:207]\n"
"v_exp_f32 v12, v12\n"
"v_mfma_f32_16x16x32_bf16 v[204:207], a[236:239], v[188:191], v[204:207]\n"
"v_exp_f32 v13, v13\n"
"v_mfma_f32_16x16x32_bf16 v[84:87], a[236:239], v[180:183], v[84:87]\n"
"ds_read_b64_tr_b16 a[236:237], v224 offset:160\n"
"v_add_u32 v243, 0x10380, v243\n"
"v_mfma_f32_16x16x32_bf16 v[84:87], a[232:235], v[124:127], v[84:87]\n"
"s_waitcnt lgkmcnt(5)\n"
"ds_read_b64_tr_b16 a[232:233], v244 offset:160\n"
"v_fma_f32 v111, v111, s92, v157\n"
"v_mfma_f32_16x16x32_bf16 v[32:35], a[240:243], v[124:127], v[32:35]\n"
"ds_read_b64_tr_b16 a[234:235], v246 offset:160\n"
"v_fma_f32 v116, v116, s92, v158\n"
"v_mfma_f32_16x16x32_bf16 v[32:35], a[244:247], v[180:183], v[32:35]\n"
"ds_read_b64_tr_b16 a[238:239], v226 offset:160\n"
"v_add_f32 v164, v164, v4\n"
"v_mfma_f32_16x16x32_bf16 v[40:43], a[244:247], v[188:191], v[40:43]\n"
"buffer_load_dwordx4 v248, s[44:47], s80 offen offset:3072 nt lds\n"
"v_mfma_f32_16x16x32_bf16 v[40:43], a[240:243], v[132:135], v[40:43]\n"
"s_add_u32 m0, s79, 0xc2a0\n"
"s_add_u32 s80, s10, s29\n"
"v_mfma_f32_16x16x32_bf16 v[48:51], a[240:243], v[140:143], v[48:51]\n"
"s_addk_i32 s10, 0x1000\n"
"ds_read_b64_tr_b16 a[240:241], v245 offset:128\n"
"v_add_f32 v164, v164, v5\n"
"v_mfma_f32_16x16x32_bf16 v[48:51], a[244:247], v[196:199], v[48:51]\n"
"s_bitset0_b32 s10, 15\n"
"ds_read_b64_tr_b16 a[242:243], v247 offset:128\n"
"v_fma_f32 v117, v117, s92, v158\n"
"v_mfma_f32_16x16x32_bf16 a[52:55], a[252:255], v[196:199], a[52:55]\n"
"ds_read_b64_tr_b16 a[244:245], v225 offset:128\n"
"v_add_f32 v164, v164, v6\n"
"v_mfma_f32_16x16x32_bf16 a[52:55], a[248:251], v[140:143], a[52:55]\n"
"ds_read_b64_tr_b16 a[246:247], v227 offset:128\n"
"v_add_f32 v164, v164, v7\n"
"v_mfma_f32_16x16x32_bf16 a[44:47], a[248:251], v[132:135], a[44:47]\n"
"v_exp_f32 v14, v14\n"
"v_mfma_f32_16x16x32_bf16 a[44:47], a[252:255], v[188:191], a[44:47]\n"
"v_exp_f32 v15, v15\n"
"v_mfma_f32_16x16x32_bf16 a[36:39], a[252:255], v[180:183], a[36:39]\n"
"ds_read_b64_tr_b16 a[252:253], v225 offset:160\n"
"v_add_f32 v165, v165, v12\n"
"v_mfma_f32_16x16x32_bf16 a[36:39], a[248:251], v[124:127], a[36:39]\n"
"s_waitcnt lgkmcnt(7)\n"
"ds_read_b64_tr_b16 a[254:255], v227 offset:160\n"
"v_add_f32 v165, v165, v13\n"
"v_mfma_f32_16x16x32_bf16 v[56:59], a[224:227], v[124:127], v[56:59]\n"
"ds_read_b64_tr_b16 a[248:249], v245 offset:160\n"
"v_add_f32 v165, v165, v14\n"
"v_mfma_f32_16x16x32_bf16 v[56:59], a[228:231], v[180:183], v[56:59]\n"
"ds_read_b64_tr_b16 a[250:251], v247 offset:160\n"
"v_cvt_pk_bf16_f32 v4, v4, v5\n"
"v_mfma_f32_16x16x32_bf16 v[64:67], a[228:231], v[188:191], v[64:67]\n"
"v_exp_f32 v20, v20\n"
"v_mfma_f32_16x16x32_bf16 v[64:67], a[224:227], v[132:135], v[64:67]\n"
"v_exp_f32 v21, v21\n"
"v_mfma_f32_16x16x32_bf16 v[72:75], a[224:227], v[140:143], v[72:75]\n"
"buffer_load_dwordx4 v248, s[44:47], s81 offen offset:0 nt lds\n"
"v_mfma_f32_16x16x32_bf16 v[72:75], a[228:231], v[196:199], v[72:75]\n"
"v_mfma_f32_16x16x32_bf16 v[80:83], a[228:231], v[148:151], v[80:83]\n"
"ds_read_b64_tr_b16 a[228:229], v224 offset:256\n"
"v_cvt_pk_bf16_f32 v5, v6, v7\n"
"v_mfma_f32_16x16x32_bf16 v[80:83], a[224:227], v[172:175], v[80:83]\n"
"s_waitcnt lgkmcnt(5)\n"
"ds_read_b64_tr_b16 a[224:225], v244 offset:256\n"
"v_add_f32 v165, v165, v15\n"
"v_mfma_f32_16x16x32_bf16 a[84:87], a[232:235], v[172:175], a[84:87]\n"
"ds_read_b64_tr_b16 a[226:227], v246 offset:256\n"
"v_add_f32 v166, v166, v20\n"
"v_mfma_f32_16x16x32_bf16 a[84:87], a[236:239], v[148:151], a[84:87]\n"
"ds_read_b64_tr_b16 a[230:231], v226 offset:256\n"
"v_add_f32 v166, v166, v21\n"
"v_mfma_f32_16x16x32_bf16 a[76:79], a[236:239], v[196:199], a[76:79]\n"
"v_exp_f32 v22, v22\n"
"v_mfma_f32_16x16x32_bf16 a[76:79], a[232:235], v[140:143], a[76:79]\n"
"v_exp_f32 v23, v23\n"
"v_mfma_f32_16x16x32_bf16 a[68:71], a[232:235], v[132:135], a[68:71]\n"
"v_exp_f32 v100, v100\n"
"v_mfma_f32_16x16x32_bf16 a[68:71], a[236:239], v[188:191], a[68:71]\n"
"v_exp_f32 v101, v101\n"
"v_mfma_f32_16x16x32_bf16 a[60:63], a[236:239], v[180:183], a[60:63]\n"
"ds_read_b64_tr_b16 a[236:237], v224 offset:288\n"
"v_add_f32 v164, v164, v100\n"
"v_mfma_f32_16x16x32_bf16 a[60:63], a[232:235], v[124:127], a[60:63]\n"
"ds_read_b64_tr_b16 a[232:233], v244 offset:288\n"
"v_add_f32 v164, v164, v101\n"
"v_mfma_f32_16x16x32_bf16 v[88:91], a[240:243], v[124:127], v[88:91]\n"
"ds_read_b64_tr_b16 a[234:235], v246 offset:288\n"
"v_cvt_pk_bf16_f32 v6, v100, v101\n"
"v_mfma_f32_16x16x32_bf16 v[88:91], a[244:247], v[180:183], v[88:91]\n"
"ds_read_b64_tr_b16 a[238:239], v226 offset:288\n"
"v_fma_f32 v118, v118, s92, v158\n"
"v_mfma_f32_16x16x32_bf16 v[96:99], a[244:247], v[188:191], v[96:99]\n"
"v_exp_f32 v102, v102\n"
"v_mfma_f32_16x16x32_bf16 v[96:99], a[240:243], v[132:135], v[96:99]\n"
"buffer_load_dwordx4 v248, s[44:47], s81 offen offset:1024 nt lds\n"
"v_mfma_f32_16x16x32_bf16 v[104:107], a[240:243], v[140:143], v[104:107]\n"
"s_addk_i32 m0, 0x20\n"
"ds_read_b64_tr_b16 a[240:241], v245 offset:256\n"
"v_mfma_f32_16x16x32_bf16 v[104:107], a[244:247], v[196:199], v[104:107]\n"
"s_waitcnt lgkmcnt(5)\n"
"ds_read_b64_tr_b16 a[242:243], v247 offset:256\n"
"v_add_f32 v164, v164, v102\n"
"v_mfma_f32_16x16x32_bf16 a[108:111], a[252:255], v[196:199], a[108:111]\n"
"ds_read_b64_tr_b16 a[244:245], v225 offset:256\n"
"v_add_f32 v166, v166, v22\n"
"v_mfma_f32_16x16x32_bf16 a[108:111], a[248:251], v[140:143], a[108:111]\n"
"ds_read_b64_tr_b16 a[246:247], v227 offset:256\n"
"v_cvt_pk_bf16_f32 v12, v12, v13\n"
"v_mfma_f32_16x16x32_bf16 a[100:103], a[248:251], v[132:135], a[100:103]\n"
"v_exp_f32 v103, v103\n"
"v_mfma_f32_16x16x32_bf16 a[100:103], a[252:255], v[188:191], a[100:103]\n"
"v_exp_f32 v108, v108\n"
"v_mfma_f32_16x16x32_bf16 a[92:95], a[252:255], v[180:183], a[92:95]\n"
"ds_read_b64_tr_b16 a[252:253], v225 offset:288\n"
"v_add_f32 v164, v164, v103\n"
"v_mfma_f32_16x16x32_bf16 a[92:95], a[248:251], v[124:127], a[92:95]\n"
"ds_read_b64_tr_b16 a[254:255], v227 offset:288\n"
"v_cvt_pk_bf16_f32 v7, v102, v103\n"
"v_mfma_f32_16x16x32_bf16 v[112:115], a[224:227], v[124:127], v[112:115]\n"
"ds_read_b64_tr_b16 a[248:249], v245 offset:288\n"
"v_add_f32 v165, v165, v108\n"
"v_mfma_f32_16x16x32_bf16 v[112:115], a[228:231], v[180:183], v[112:115]\n"
"ds_read_b64_tr_b16 a[250:251], v247 offset:288\n"
"v_cvt_pk_bf16_f32 v13, v14, v15\n"
"v_mfma_f32_16x16x32_bf16 v[120:123], a[228:231], v[188:191], v[120:123]\n"
"v_exp_f32 v109, v109\n"
"v_mfma_f32_16x16x32_bf16 v[120:123], a[224:227], v[132:135], v[120:123]\n"
"v_exp_f32 v110, v110\n"
"v_mfma_f32_16x16x32_bf16 v[128:131], a[224:227], v[140:143], v[128:131]\n"
"v_exp_f32 v111, v111\n"
"v_mfma_f32_16x16x32_bf16 v[128:131], a[228:231], v[196:199], v[128:131]\n"
"buffer_load_dwordx4 v248, s[44:47], s81 offen offset:2048 nt lds\n"
"v_mfma_f32_16x16x32_bf16 v[136:139], a[228:231], v[148:151], v[136:139]\n"
"ds_read_b64_tr_b16 a[228:229], v224 offset:384\n"
"v_mfma_f32_16x16x32_bf16 v[136:139], a[224:227], v[172:175], v[136:139]\n"
"s_waitcnt lgkmcnt(5)\n"
"ds_read_b64_tr_b16 a[224:225], v244 offset:384\n"
"v_add_f32 v165, v165, v109\n"
"v_mfma_f32_16x16x32_bf16 a[140:143], a[232:235], v[172:175], a[140:143]\n"
"ds_read_b64_tr_b16 a[226:227], v246 offset:384\n"
"v_add_f32 v165, v165, v110\n"
"v_mfma_f32_16x16x32_bf16 a[140:143], a[236:239], v[148:151], a[140:143]\n"
"ds_read_b64_tr_b16 a[230:231], v226 offset:384\n"
"v_add_f32 v165, v165, v111\n"
"v_mfma_f32_16x16x32_bf16 a[132:135], a[236:239], v[196:199], a[132:135]\n"
"v_exp_f32 v116, v116\n"
"v_mfma_f32_16x16x32_bf16 a[132:135], a[232:235], v[140:143], a[132:135]\n"
"v_exp_f32 v117, v117\n"
"v_mfma_f32_16x16x32_bf16 a[124:127], a[232:235], v[132:135], a[124:127]\n"
"v_cvt_pk_bf16_f32 v14, v108, v109\n"
"v_cvt_pk_bf16_f32 v15, v110, v111\n"
"v_mfma_f32_16x16x32_bf16 a[124:127], a[236:239], v[188:191], a[124:127]\n"
"v_add_f32 v166, v166, v23\n"
"v_fma_f32 v119, v119, s92, v158\n"
"v_mfma_f32_16x16x32_bf16 a[116:119], a[236:239], v[180:183], a[116:119]\n"
"ds_read_b64_tr_b16 a[236:237], v224 offset:416\n"
"v_add_f32 v166, v166, v116\n"
"v_mfma_f32_16x16x32_bf16 a[116:119], a[232:235], v[124:127], a[116:119]\n"
"ds_read_b64_tr_b16 a[232:233], v244 offset:416\n"
"v_add_f32 v166, v166, v117\n"
"v_mfma_f32_16x16x32_bf16 v[144:147], a[240:243], v[124:127], v[144:147]\n"
"ds_read_b64_tr_b16 a[234:235], v246 offset:416\n"
"v_cvt_pk_bf16_f32 v20, v20, v21\n"
"v_mfma_f32_16x16x32_bf16 v[144:147], a[244:247], v[180:183], v[144:147]\n"
"ds_read_b64_tr_b16 a[238:239], v226 offset:416\n"
"v_cvt_pk_bf16_f32 v21, v22, v23\n"
"v_mfma_f32_16x16x32_bf16 v[152:155], a[244:247], v[188:191], v[152:155]\n"
"v_exp_f32 v118, v118\n"
"v_mfma_f32_16x16x32_bf16 v[152:155], a[240:243], v[132:135], v[152:155]\n"
"v_exp_f32 v119, v119\n"
"v_mfma_f32_16x16x32_bf16 v[160:163], a[240:243], v[140:143], v[160:163]\n"
"buffer_load_dwordx4 v248, s[44:47], s81 offen offset:3072 nt lds\n"
"v_mfma_f32_16x16x32_bf16 v[160:163], a[244:247], v[196:199], v[160:163]\n"
"s_waitcnt lgkmcnt(4)\n"
"s_add_u32 m0, s79, 0x20700\n"
"ds_read_b64_tr_b16 a[240:241], v245 offset:384\n"
"v_mfma_f32_16x16x32_bf16 a[164:167], a[252:255], v[196:199], a[164:167]\n"
"s_add_u32 s81, s10, s29\n"
"ds_read_b64_tr_b16 a[242:243], v247 offset:384\n"
"v_add_f32 v166, v166, v118\n"
"v_mfma_f32_16x16x32_bf16 a[164:167], a[248:251], v[140:143], a[164:167]\n"
"s_addk_i32 s10, 0x1000\n"
"ds_read_b64_tr_b16 a[244:245], v225 offset:384\n"
"v_add_f32 v166, v166, v119\n"
"v_mfma_f32_16x16x32_bf16 a[156:159], a[248:251], v[132:135], a[156:159]\n"
"s_bitset0_b32 s10, 15\n"
"ds_read_b64_tr_b16 a[246:247], v227 offset:384\n"
"v_cvt_pk_bf16_f32 v22, v116, v117\n"
"v_mfma_f32_16x16x32_bf16 a[156:159], a[252:255], v[188:191], a[156:159]\n"
"v_cvt_pk_bf16_f32 v23, v118, v119\n"
"v_fma_f32 v28, v28, s92, v159\n"
"v_mfma_f32_16x16x32_bf16 a[148:151], a[252:255], v[180:183], a[148:151]\n"
"ds_read_b64_tr_b16 a[252:253], v225 offset:416\n"
"v_fma_f32 v29, v29, s92, v159\n"
"v_mfma_f32_16x16x32_bf16 a[148:151], a[248:251], v[124:127], a[148:151]\n"
"ds_read_b64_tr_b16 a[254:255], v227 offset:416\n"
"v_fma_f32 v30, v30, s92, v159\n"
"v_mfma_f32_16x16x32_bf16 v[168:171], a[224:227], v[124:127], v[168:171]\n"
"ds_read_b64_tr_b16 a[248:249], v245 offset:416\n"
"v_fma_f32 v31, v31, s92, v159\n"
"v_mfma_f32_16x16x32_bf16 v[168:171], a[228:231], v[180:183], v[168:171]\n"
"ds_read_b64_tr_b16 a[250:251], v247 offset:416\n"
"v_fma_f32 v36, v36, s92, v156\n"
"v_mfma_f32_16x16x32_bf16 v[176:179], a[228:231], v[188:191], v[176:179]\n"
"v_exp_f32 v28, v28\n"
"v_mfma_f32_16x16x32_bf16 v[176:179], a[224:227], v[132:135], v[176:179]\n"
"v_exp_f32 v29, v29\n"
"v_mfma_f32_16x16x32_bf16 v[184:187], a[224:227], v[140:143], v[184:187]\n"
"v_exp_f32 v30, v30\n"
"v_mfma_f32_16x16x32_bf16 v[184:187], a[228:231], v[196:199], v[184:187]\n"
"v_exp_f32 v31, v31\n"
"v_mfma_f32_16x16x32_bf16 v[192:195], a[228:231], v[148:151], v[192:195]\n"
"buffer_load_dwordx4 v248, s[48:51], s80 offen offset:0 nt lds\n"
"v_mfma_f32_16x16x32_bf16 v[192:195], a[224:227], v[172:175], v[192:195]\n"
"s_waitcnt vmcnt(17) lgkmcnt(0)\n"
"s_barrier\n"
"v_mfma_f32_16x16x32_bf16 a[196:199], a[232:235], v[172:175], a[196:199]\n"
"ds_read_b128 a[224:227], v240 offset:0\n"
"v_fma_f32 v37, v37, s92, v156\n"
"v_mfma_f32_16x16x32_bf16 a[196:199], a[236:239], v[148:151], a[196:199]\n"
"ds_read_b128 a[228:231], v241 offset:0\n"
"v_fma_f32 v38, v38, s92, v156\n"
"v_mfma_f32_16x16x32_bf16 a[188:191], a[236:239], v[196:199], a[188:191]\n"
"v_exp_f32 v36, v36\n"
"v_mfma_f32_16x16x32_bf16 a[188:191], a[232:235], v[140:143], a[188:191]\n"
"v_exp_f32 v37, v37\n"
"v_mfma_f32_16x16x32_bf16 a[180:183], a[232:235], v[132:135], a[180:183]\n"
"v_exp_f32 v38, v38\n"
"v_mfma_f32_16x16x32_bf16 a[180:183], a[236:239], v[188:191], a[180:183]\n"
"v_fma_f32 v39, v39, s92, v156\n"
"v_fma_f32 v44, v44, s92, v157\n"
"v_mfma_f32_16x16x32_bf16 a[172:175], a[236:239], v[180:183], a[172:175]\n"
"ds_read_b128 a[236:239], v241 offset:128\n"
"v_fma_f32 v45, v45, s92, v157\n"
"v_mfma_f32_16x16x32_bf16 a[172:175], a[232:235], v[124:127], a[172:175]\n"
"ds_read_b128 a[232:235], v240 offset:128\n"
"v_fma_f32 v46, v46, s92, v157\n"
"v_mfma_f32_16x16x32_bf16 v[200:203], a[240:243], v[124:127], v[200:203]\n"
"v_exp_f32 v39, v39\n"
"v_mfma_f32_16x16x32_bf16 v[200:203], a[244:247], v[180:183], v[200:203]\n"
"v_exp_f32 v44, v44\n"
"v_mfma_f32_16x16x32_bf16 v[208:211], a[244:247], v[188:191], v[208:211]\n"
"v_exp_f32 v45, v45\n"
"v_mfma_f32_16x16x32_bf16 v[208:211], a[240:243], v[132:135], v[208:211]\n"
"v_exp_f32 v46, v46\n"
"v_mfma_f32_16x16x32_bf16 v[216:219], a[240:243], v[140:143], v[216:219]\n"
"ds_read_b128 a[240:243], v240 offset:256\n"
"v_fma_f32 v47, v47, s92, v157\n"
"v_mfma_f32_16x16x32_bf16 v[216:219], a[244:247], v[196:199], v[216:219]\n"
"buffer_load_dwordx4 v248, s[48:51], s80 offen offset:1024 nt lds\n"
"v_mfma_f32_16x16x32_bf16 v[236:239], a[252:255], v[196:199], v[236:239]\n"
"s_addk_i32 m0, 0x20\n"
"ds_read_b128 a[244:247], v241 offset:256\n"
"v_mfma_f32_16x16x32_bf16 v[236:239], a[248:251], v[140:143], v[236:239]\n"
"v_exp_f32 v47, v47\n"
"v_mfma_f32_16x16x32_bf16 v[232:235], a[248:251], v[132:135], v[232:235]\n"
"v_fma_f32 v52, v52, s92, v158\n"
"v_fma_f32 v53, v53, s92, v158\n"
"v_mfma_f32_16x16x32_bf16 v[232:235], a[252:255], v[188:191], v[232:235]\n"
"v_exp_f32 v52, v52\n"
"v_mfma_f32_16x16x32_bf16 v[228:231], a[252:255], v[180:183], v[228:231]\n"
"ds_read_b128 a[252:255], v241 offset:384\n"
"v_fma_f32 v54, v54, s92, v158\n"
"v_mfma_f32_16x16x32_bf16 v[228:231], a[248:251], v[124:127], v[228:231]\n"
"s_waitcnt lgkmcnt(3)\n"
"ds_read_b128 a[248:251], v240 offset:384\n"
"v_add_u32 v241, 0xfffe39e0, v241\n"
"v_mfma_f32_16x16x32_bf16 v[124:127], a[224:227], a[0:3], 0\n"
"v_add_u32 v240, 0xfffe39e0, v240\n"
"v_fma_f32 v55, v55, s92, v158\n"
"v_mfma_f32_16x16x32_bf16 v[124:127], a[228:231], a[32:35], v[124:127]\n"
"v_exp_f32 v53, v53\n"
"v_mfma_f32_16x16x32_bf16 v[132:135], a[228:231], a[40:43], 0\n"
"v_exp_f32 v54, v54\n"
"v_mfma_f32_16x16x32_bf16 v[132:135], a[224:227], a[8:11], v[132:135]\n"
"v_exp_f32 v55, v55\n"
"v_mfma_f32_16x16x32_bf16 v[140:143], a[224:227], a[16:19], 0\n"
"v_fma_f32 v92, v92, s92, v159\n"
"v_fma_f32 v93, v93, s92, v159\n"
"v_mfma_f32_16x16x32_bf16 v[140:143], a[228:231], a[48:51], v[140:143]\n"
"v_exp_f32 v92, v92\n"
"v_mfma_f32_16x16x32_bf16 v[172:175], a[228:231], a[4:7], 0\n"
"ds_read_b128 a[228:231], v243 offset:0\n"
"v_fma_f32 v94, v94, s92, v159\n"
"v_mfma_f32_16x16x32_bf16 v[172:175], a[224:227], a[24:27], v[172:175]\n"
"buffer_load_dwordx4 v248, s[48:51], s80 offen offset:2048 nt lds\n"
"v_mfma_f32_16x16x32_bf16 v[124:127], a[232:235], a[56:59], v[124:127]\n"
"ds_read_b128 a[224:227], v242 offset:0\n"
"v_mfma_f32_16x16x32_bf16 v[124:127], a[236:239], a[88:91], v[124:127]\n"
"v_exp_f32 v93, v93\n"
"v_mfma_f32_16x16x32_bf16 v[132:135], a[236:239], a[96:99], v[132:135]\n"
"v_exp_f32 v94, v94\n"
"v_mfma_f32_16x16x32_bf16 v[132:135], a[232:235], a[64:67], v[132:135]\n"
"v_fma_f32 v95, v95, s92, v159\n"
"v_fma_f32 v60, v60, s92, v156\n"
"v_mfma_f32_16x16x32_bf16 v[140:143], a[232:235], a[72:75], v[140:143]\n"
"v_exp_f32 v95, v95\n"
"v_mfma_f32_16x16x32_bf16 v[140:143], a[236:239], a[104:107], v[140:143]\n"
"v_exp_f32 v60, v60\n"
"v_mfma_f32_16x16x32_bf16 v[172:175], a[236:239], a[12:15], v[172:175]\n"
"ds_read_b128 a[236:239], v243 offset:128\n"
"v_fma_f32 v61, v61, s92, v156\n"
"v_mfma_f32_16x16x32_bf16 v[172:175], a[232:235], a[80:83], v[172:175]\n"
"s_waitcnt lgkmcnt(3)\n"
"ds_read_b128 a[232:235], v242 offset:128\n"
"v_fma_f32 v62, v62, s92, v156\n"
"v_mfma_f32_16x16x32_bf16 v[124:127], a[240:243], a[112:115], v[124:127]\n"
"v_exp_f32 v61, v61\n"
"v_mfma_f32_16x16x32_bf16 v[124:127], a[244:247], a[144:147], v[124:127]\n"
"v_exp_f32 v62, v62\n"
"v_mfma_f32_16x16x32_bf16 v[132:135], a[244:247], a[152:155], v[132:135]\n"
"v_fma_f32 v63, v63, s92, v156\n"
"v_fma_f32 v68, v68, s92, v157\n"
"v_mfma_f32_16x16x32_bf16 v[132:135], a[240:243], a[120:123], v[132:135]\n"
"v_exp_f32 v63, v63\n"
"v_mfma_f32_16x16x32_bf16 v[140:143], a[240:243], a[128:131], v[140:143]\n"
"v_exp_f32 v68, v68\n"
"v_mfma_f32_16x16x32_bf16 v[140:143], a[244:247], a[160:163], v[140:143]\n"
"v_fma_f32 v69, v69, s92, v157\n"
"v_fma_f32 v70, v70, s92, v157\n"
"v_mfma_f32_16x16x32_bf16 v[172:175], a[244:247], a[20:23], v[172:175]\n"
"buffer_load_dwordx4 v248, s[48:51], s80 offen offset:3072 nt lds\n"
"v_mfma_f32_16x16x32_bf16 v[172:175], a[240:243], a[136:139], v[172:175]\n"
"s_add_u32 m0, s79, 0x40e0\n"
"s_add_u32 s80, s9, s24\n"
"ds_read_b128 a[240:243], v242 offset:256\n"
"v_mfma_f32_16x16x32_bf16 v[124:127], a[248:251], a[168:171], v[124:127]\n"
"s_addk_i32 s9, 0x1000\n"
"ds_read_b128 a[244:247], v243 offset:256\n"
"v_fma_f32 v71, v71, s92, v157\n"
"v_mfma_f32_16x16x32_bf16 v[124:127], a[252:255], a[200:203], v[124:127]\n"
"s_bitset0_b32 s9, 15\n"
"v_exp_f32 v69, v69\n"
"v_mfma_f32_16x16x32_bf16 v[132:135], a[252:255], a[208:211], v[132:135]\n"
"PG7_%=:\n"
"s_cbranch_execz PGB1_%=\n"
"v_exp_f32 v70, v70\n"
"v_mfma_f32_16x16x32_bf16 v[132:135], a[248:251], a[176:179], v[132:135]\n"
"v_exp_f32 v71, v71\n"
"v_mfma_f32_16x16x32_bf16 v[140:143], a[248:251], a[184:187], v[140:143]\n"
"v_fma_f32 v76, v76, s92, v158\n"
"v_add_f32 v164, v164, v36\n"
"v_mfma_f32_16x16x32_bf16 v[140:143], a[252:255], a[216:219], v[140:143]\n"
"v_add_f32 v164, v164, v37\n"
"v_fma_f32 v77, v77, s92, v158\n"
"v_mfma_f32_16x16x32_bf16 v[172:175], a[252:255], a[28:31], v[172:175]\n"
"ds_read_b128 a[252:255], v243 offset:384\n"
"v_add_f32 v164, v164, v38\n"
"v_mfma_f32_16x16x32_bf16 v[172:175], a[248:251], a[192:195], v[172:175]\n"
"s_waitcnt lgkmcnt(3)\n"
"ds_read_b128 a[248:251], v242 offset:384\n"
"v_add_u32 v243, 0xfffe39e0, v243\n"
"v_mfma_f32_16x16x32_bf16 v[100:103], a[224:227], a[0:3], 0\n"
"v_add_u32 v242, 0xfffe39e0, v242\n"
"v_add_f32 v164, v164, v39\n"
"v_mfma_f32_16x16x32_bf16 v[100:103], a[228:231], a[32:35], v[100:103]\n"
"v_exp_f32 v76, v76\n"
"v_mfma_f32_16x16x32_bf16 v[108:111], a[228:231], a[40:43], 0\n"
"v_exp_f32 v77, v77\n"
"v_mfma_f32_16x16x32_bf16 v[108:111], a[224:227], a[8:11], v[108:111]\n"
"v_add_f32 v164, v164, v60\n"
"v_add_f32 v165, v165, v44\n"
"v_mfma_f32_16x16x32_bf16 v[116:119], a[224:227], a[16:19], 0\n"
"ds_read_b128 a[224:227], v240 offset:0\n"
"v_add_f32 v164, v164, v61\n"
"v_mfma_f32_16x16x32_bf16 v[116:119], a[228:231], a[48:51], v[116:119]\n"
"buffer_load_dwordx4 v248, s[48:51], s81 offen offset:0 nt lds\n"
"v_mfma_f32_16x16x32_bf16 v[100:103], a[232:235], a[56:59], v[100:103]\n"
"ds_read_b128 a[228:231], v241 offset:0\n"
"v_mfma_f32_16x16x32_bf16 v[100:103], a[236:239], a[88:91], v[100:103]\n"
"v_add_f32 v165, v165, v45\n"
"v_add_f32 v164, v164, v62\n"
"v_mfma_f32_16x16x32_bf16 v[108:111], a[236:239], a[96:99], v[108:111]\n"
"v_add_f32 v165, v165, v46\n"
"v_cvt_pk_bf16_f32 v36, v36, v37\n"
"v_mfma_f32_16x16x32_bf16 v[108:111], a[232:235], a[64:67], v[108:111]\n"
"v_cvt_pk_bf16_f32 v37, v38, v39\n"
"v_add_f32 v164, v164, v63\n"
"v_mfma_f32_16x16x32_bf16 v[116:119], a[232:235], a[72:75], v[116:119]\n"
"ds_read_b128 a[232:235], v240 offset:128\n"
"v_cvt_pk_bf16_f32 v38, v60, v61\n"
"v_mfma_f32_16x16x32_bf16 v[116:119], a[236:239], a[104:107], v[116:119]\n"
"s_waitcnt lgkmcnt(3)\n"
"ds_read_b128 a[236:239], v241 offset:128\n"
"v_cvt_pk_bf16_f32 v39, v62, v63\n"
"v_mfma_f32_16x16x32_bf16 v[100:103], a[240:243], a[112:115], v[100:103]\n"
"v_add_f32 v165, v165, v47\n"
"v_add_f32 v166, v166, v52\n"
"v_mfma_f32_16x16x32_bf16 v[100:103], a[244:247], a[144:147], v[100:103]\n"
"v_add_f32 v165, v165, v68\n"
"v_add_f32 v166, v166, v53\n"
"v_mfma_f32_16x16x32_bf16 v[108:111], a[244:247], a[152:155], v[108:111]\n"
"v_add_f32 v165, v165, v69\n"
"v_fma_f32 v78, v78, s92, v158\n"
"v_mfma_f32_16x16x32_bf16 v[108:111], a[240:243], a[120:123], v[108:111]\n"
"v_add_f32 v165, v165, v70\n"
"v_add_f32 v166, v166, v54\n"
"v_mfma_f32_16x16x32_bf16 v[116:119], a[240:243], a[128:131], v[116:119]\n"
"ds_read_b128 a[240:243], v240 offset:256\n"
"v_add_f32 v167, v167, v28\n"
"v_mfma_f32_16x16x32_bf16 v[116:119], a[244:247], a[160:163], v[116:119]\n"
"ds_read_b128 a[244:247], v241 offset:256\n"
"v_cvt_pk_bf16_f32 v44, v44, v45\n"
"v_mfma_f32_16x16x32_bf16 v[100:103], a[248:251], a[168:171], v[100:103]\n"
"v_cvt_pk_bf16_f32 v45, v46, v47\n"
"v_add_f32 v165, v165, v71\n"
"v_mfma_f32_16x16x32_bf16 v[100:103], a[252:255], a[200:203], v[100:103]\n"
"buffer_load_dwordx4 v248, s[48:51], s81 offen offset:1024 nt lds\n"
"v_mfma_f32_16x16x32_bf16 v[108:111], a[252:255], a[208:211], v[108:111]\n"
"s_addk_i32 m0, 0x20\n"
"v_mfma_f32_16x16x32_bf16 v[108:111], a[248:251], a[176:179], v[108:111]\n"
"v_cvt_pk_bf16_f32 v46, v68, v69\n"
"v_cvt_pk_bf16_f32 v47, v70, v71\n"
"v_mfma_f32_16x16x32_bf16 v[116:119], a[248:251], a[184:187], v[116:119]\n"
"ds_read_b128 a[248:251], v240 offset:384\n"
"v_add_f32 v166, v166, v55\n"
"v_mfma_f32_16x16x32_bf16 v[116:119], a[252:255], a[216:219], v[116:119]\n"
"s_waitcnt lgkmcnt(3)\n"
"ds_read_b128 a[252:255], v241 offset:384\n"
"v_fma_f32 v79, v79, s92, v158\n"
"v_mfma_f32_16x16x32_bf16 v[180:183], a[224:227], a[0:3], 0\n"
"v_add_f32 v167, v167, v29\n"
"v_add_f32 v166, v166, v76\n"
"v_mfma_f32_16x16x32_bf16 v[180:183], a[228:231], a[32:35], v[180:183]\n"
"v_exp_f32 v78, v78\n"
"v_mfma_f32_16x16x32_bf16 v[188:191], a[228:231], a[40:43], 0\n"
"v_add_f32 v167, v167, v30\n"
"v_add_f32 v166, v166, v77\n"
"v_mfma_f32_16x16x32_bf16 v[188:191], a[224:227], a[8:11], v[188:191]\n"
"v_exp_f32 v79, v79\n"
"v_mfma_f32_16x16x32_bf16 v[196:199], a[224:227], a[16:19], 0\n"
"v_add_f32 v167, v167, v31\n"
"v_cvt_pk_bf16_f32 v28, v28, v29\n"
"v_mfma_f32_16x16x32_bf16 v[196:199], a[228:231], a[48:51], v[196:199]\n"
"v_cvt_pk_bf16_f32 v29, v30, v31\n"
"v_add_f32 v166, v166, v78\n"
"v_mfma_f32_16x16x32_bf16 v[148:151], a[228:231], a[4:7], 0\n"
"ds_read_b128 a[228:231], v243 offset:0\n"
"v_add_f32 v167, v167, v92\n"
"v_mfma_f32_16x16x32_bf16 v[148:151], a[224:227], a[24:27], v[148:151]\n"
"ds_read_b128 a[224:227], v242 offset:0\n"
"v_cvt_pk_bf16_f32 v52, v52, v53\n"
"v_mfma_f32_16x16x32_bf16 v[180:183], a[232:235], a[56:59], v[180:183]\n"
"v_cvt_pk_bf16_f32 v53, v54, v55\n"
"v_add_f32 v166, v166, v79\n"
"v_mfma_f32_16x16x32_bf16 v[180:183], a[236:239], a[88:91], v[180:183]\n"
"v_cvt_pk_bf16_f32 v54, v76, v77\n"
"v_cvt_pk_bf16_f32 v55, v78, v79\n"
"v_mfma_f32_16x16x32_bf16 v[188:191], a[236:239], a[96:99], v[188:191]\n"
"buffer_load_dwordx4 v248, s[48:51], s81 offen offset:2048 nt lds\n"
"v_mfma_f32_16x16x32_bf16 v[188:191], a[232:235], a[64:67], v[188:191]\n"
"ds_write_b64 v253, v[28:29] offset:0\n"
"v_mfma_f32_16x16x32_bf16 v[196:199], a[232:235], a[72:75], v[196:199]\n"
"v_add_f32 v167, v167, v93\n"
"v_cvt_pk_bf16_f32 v92, v92, v93\n"
"v_mfma_f32_16x16x32_bf16 v[196:199], a[236:239], a[104:107], v[196:199]\n"
"v_cvt_pk_bf16_f32 v93, v94, v95\n"
"v_add_u32 v224, 0xffffbf20, v224\n"
"v_mfma_f32_16x16x32_bf16 v[148:151], a[236:239], a[12:15], v[148:151]\n"
"ds_read_b128 a[236:239], v243 offset:128\n"
"v_add_u32 v226, 0xffffbf20, v226\n"
"v_mfma_f32_16x16x32_bf16 v[148:151], a[232:235], a[80:83], v[148:151]\n"
"s_waitcnt lgkmcnt(4)\n"
"ds_read_b128 a[232:235], v242 offset:128\n"
"v_add_f32 v167, v167, v94\n"
"v_mfma_f32_16x16x32_bf16 v[180:183], a[240:243], a[112:115], v[180:183]\n"
"ds_write_b64 v253, v[92:93] offset:512\n"
"v_add_f32 v167, v167, v95\n"
"v_mfma_f32_16x16x32_bf16 v[180:183], a[244:247], a[144:147], v[180:183]\n"
"v_add_u32 v225, 0xffffbf20, v225\n"
"v_add_u32 v227, 0xffffbf20, v227\n"
"v_mfma_f32_16x16x32_bf16 v[188:191], a[244:247], a[152:155], v[188:191]\n"
"v_add_u32 v240, 0x18540, v240\n"
"v_add_u32 v241, 0x18540, v241\n"
"v_mfma_f32_16x16x32_bf16 v[188:191], a[240:243], a[120:123], v[188:191]\n"
"v_fma_f32 v124, v124, s92, v156\n"
"v_fma_f32 v125, v125, s92, v156\n"
"v_mfma_f32_16x16x32_bf16 v[196:199], a[240:243], a[128:131], v[196:199]\n"
"v_exp_f32 v124, v124\n"
"v_mfma_f32_16x16x32_bf16 v[196:199], a[244:247], a[160:163], v[196:199]\n"
"v_exp_f32 v125, v125\n"
"v_mfma_f32_16x16x32_bf16 v[148:151], a[244:247], a[20:23], v[148:151]\n"
"ds_read_b128 a[244:247], v243 offset:256\n"
"v_fma_f32 v126, v126, s92, v156\n"
"v_mfma_f32_16x16x32_bf16 v[148:151], a[240:243], a[136:139], v[148:151]\n"
"ds_read_b128 a[240:243], v242 offset:256\n"
"v_fma_f32 v127, v127, s92, v156\n"
"v_mfma_f32_16x16x32_bf16 v[180:183], a[248:251], a[168:171], v[180:183]\n"
"buffer_load_dwordx4 v248, s[48:51], s81 offen offset:3072 nt lds\n"
"v_mfma_f32_16x16x32_bf16 v[180:183], a[252:255], a[200:203], v[180:183]\n"
"s_add_u32 m0, s79, 0x14460\n"
"s_add_u32 s81, s9, s24\n"
"v_mfma_f32_16x16x32_bf16 v[188:191], a[252:255], a[208:211], v[188:191]\n"
"s_addk_i32 s9, 0x1000\n"
"v_exp_f32 v126, v126\n"
"v_mfma_f32_16x16x32_bf16 v[188:191], a[248:251], a[176:179], v[188:191]\n"
"s_bitset0_b32 s9, 15\n"
"v_exp_f32 v127, v127\n"
"v_mfma_f32_16x16x32_bf16 v[196:199], a[248:251], a[184:187], v[196:199]\n"
"v_fma_f32 v132, v132, s92, v157\n"
"v_fma_f32 v133, v133, s92, v157\n"
"v_mfma_f32_16x16x32_bf16 v[196:199], a[252:255], a[216:219], v[196:199]\n"
"v_exp_f32 v132, v132\n"
"v_mfma_f32_16x16x32_bf16 v[148:151], a[252:255], a[28:31], v[148:151]\n"
"ds_read_b128 a[252:255], v243 offset:384\n"
"v_fma_f32 v134, v134, s92, v157\n"
"v_mfma_f32_16x16x32_bf16 v[148:151], a[248:251], a[192:195], v[148:151]\n"
"s_waitcnt lgkmcnt(3)\n"
"ds_read_b128 a[248:251], v242 offset:384\n"
"v_fma_f32 v135, v135, s92, v157\n"
"v_mfma_f32_16x16x32_bf16 v[60:63], a[224:227], a[0:3], 0\n"
"v_exp_f32 v133, v133\n"
"v_mfma_f32_16x16x32_bf16 v[60:63], a[228:231], a[32:35], v[60:63]\n"
"v_exp_f32 v134, v134\n"
"v_mfma_f32_16x16x32_bf16 v[68:71], a[228:231], a[40:43], 0\n"
"v_exp_f32 v135, v135\n"
"v_mfma_f32_16x16x32_bf16 v[68:71], a[224:227], a[8:11], v[68:71]\n"
"v_fma_f32 v140, v140, s92, v158\n"
"v_fma_f32 v141, v141, s92, v158\n"
"v_mfma_f32_16x16x32_bf16 v[76:79], a[224:227], a[16:19], 0\n"
"s_waitcnt vmcnt(16) lgkmcnt(0)\n"
"s_barrier\n"
"v_mfma_f32_16x16x32_bf16 v[76:79], a[228:231], a[48:51], v[76:79]\n"
"ds_read_b64_tr_b16 a[224:225], v244 offset:16608\n"
"v_fma_f32 v142, v142, s92, v158\n"
"v_mfma_f32_16x16x32_bf16 v[60:63], a[232:235], a[56:59], v[60:63]\n"
"ds_read_b64_tr_b16 a[226:227], v246 offset:16608\n"
"v_fma_f32 v143, v143, s92, v158\n"
"v_mfma_f32_16x16x32_bf16 v[60:63], a[236:239], a[88:91], v[60:63]\n"
"ds_read_b64_tr_b16 a[228:229], v224 offset:0\n"
"v_fma_f32 v100, v100, s92, v156\n"
"v_mfma_f32_16x16x32_bf16 v[68:71], a[236:239], a[96:99], v[68:71]\n"
"ds_read_b64_tr_b16 a[230:231], v226 offset:0\n"
"v_fma_f32 v101, v101, s92, v156\n"
"v_mfma_f32_16x16x32_bf16 v[68:71], a[232:235], a[64:67], v[68:71]\n"
"buffer_load_dwordx4 v248, s[44:47], s80 offen offset:0 nt lds\n"
"v_mfma_f32_16x16x32_bf16 v[76:79], a[232:235], a[72:75], v[76:79]\n"
"ds_read_b64 v[30:31], v254 offset:0\n"
"v_mfma_f32_16x16x32_bf16 v[76:79], a[236:239], a[104:107], v[76:79]\n"
"ds_read_b64 v[94:95], v254 offset:512\n"
"v_fma_f32 v102, v102, s92, v156\n"
"v_mfma_f32_16x16x32_bf16 v[60:63], a[240:243], a[112:115], v[60:63]\n"
"ds_read_b64_tr_b16 a[232:233], v244 offset:16640\n"
"v_fma_f32 v103, v103, s92, v156\n"
"v_mfma_f32_16x16x32_bf16 v[60:63], a[244:247], a[144:147], v[60:63]\n"
"ds_read_b64_tr_b16 a[234:235], v246 offset:16640\n"
"v_fma_f32 v108, v108, s92, v157\n"
"v_mfma_f32_16x16x32_bf16 v[68:71], a[244:247], a[152:155], v[68:71]\n"
"ds_read_b64_tr_b16 a[236:237], v224 offset:32\n"
"v_fma_f32 v109, v109, s92, v157\n"
"v_mfma_f32_16x16x32_bf16 v[68:71], a[240:243], a[120:123], v[68:71]\n"
"ds_read_b64_tr_b16 a[238:239], v226 offset:32\n"
"v_fma_f32 v110, v110, s92, v157\n"
"v_mfma_f32_16x16x32_bf16 v[76:79], a[240:243], a[128:131], v[76:79]\n"
"ds_read_b64_tr_b16 a[240:241], v245 offset:16608\n"
"v_add_u32 v242, 0x18540, v242\n"
"v_mfma_f32_16x16x32_bf16 v[76:79], a[244:247], a[160:163], v[76:79]\n"
"ds_read_b64_tr_b16 a[242:243], v247 offset:16608\n"
"v_add_u32 v243, 0x18540, v243\n"
"v_mfma_f32_16x16x32_bf16 v[60:63], a[248:251], a[168:171], v[60:63]\n"
"ds_read_b64_tr_b16 a[244:245], v225 offset:0\n"
"v_fma_f32 v111, v111, s92, v157\n"
"v_mfma_f32_16x16x32_bf16 v[60:63], a[252:255], a[200:203], v[60:63]\n"
"ds_read_b64_tr_b16 a[246:247], v227 offset:0\n"
"v_fma_f32 v116, v116, s92, v158\n"
"v_mfma_f32_16x16x32_bf16 v[68:71], a[252:255], a[208:211], v[68:71]\n"
"v_exp_f32 v140, v140\n"
"v_mfma_f32_16x16x32_bf16 v[68:71], a[248:251], a[176:179], v[68:71]\n"
"buffer_load_dwordx4 v248, s[44:47], s80 offen offset:1024 nt lds\n"
"v_mfma_f32_16x16x32_bf16 v[76:79], a[248:251], a[184:187], v[76:79]\n"
"s_addk_i32 m0, 0x20\n"
"ds_read_b64_tr_b16 a[248:249], v245 offset:16640\n"
"v_mfma_f32_16x16x32_bf16 v[76:79], a[252:255], a[216:219], v[76:79]\n"
"s_waitcnt lgkmcnt(9)\n"
"ds_read_b64_tr_b16 a[252:253], v225 offset:32\n"
"v_add_f32 v164, v164, v124\n"
"v_mfma_f32_16x16x32_bf16 v[0:3], a[224:227], v[4:7], v[0:3]\n"
"ds_read_b64_tr_b16 a[254:255], v227 offset:32\n"
"v_add_f32 v164, v164, v125\n"
"v_mfma_f32_16x16x32_bf16 v[0:3], a[228:231], v[36:39], v[0:3]\n"
"ds_read_b64_tr_b16 a[250:251], v247 offset:16640\n"
"v_fma_f32 v117, v117, s92, v158\n"
"v_mfma_f32_16x16x32_bf16 v[8:11], a[228:231], v[44:47], v[8:11]\n"
"v_exp_f32 v141, v141\n"
"v_mfma_f32_16x16x32_bf16 v[8:11], a[224:227], v[12:15], v[8:11]\n"
"v_exp_f32 v142, v142\n"
"v_mfma_f32_16x16x32_bf16 v[16:19], a[224:227], v[20:23], v[16:19]\n"
"v_exp_f32 v143, v143\n"
"v_mfma_f32_16x16x32_bf16 v[16:19], a[228:231], v[52:55], v[16:19]\n"
"v_exp_f32 v100, v100\n"
"v_mfma_f32_16x16x32_bf16 v[24:27], a[228:231], v[92:95], v[24:27]\n"
"ds_read_b64_tr_b16 a[228:229], v224 offset:128\n"
"v_add_f32 v164, v164, v126\n"
"v_mfma_f32_16x16x32_bf16 v[24:27], a[224:227], v[28:31], v[24:27]\n"
"s_waitcnt lgkmcnt(5)\n"
"ds_read_b64_tr_b16 a[224:225], v244 offset:16736\n"
"v_add_f32 v164, v164, v127\n"
"v_mfma_f32_16x16x32_bf16 v[220:223], a[232:235], v[28:31], v[220:223]\n"
"ds_read_b64_tr_b16 a[226:227], v246 offset:16736\n"
"v_add_f32 v164, v164, v100\n"
"v_mfma_f32_16x16x32_bf16 v[220:223], a[236:239], v[92:95], v[220:223]\n"
"ds_read_b64_tr_b16 a[230:231], v226 offset:128\n"
"v_add_f32 v165, v165, v132\n"
"v_mfma_f32_16x16x32_bf16 v[212:215], a[236:239], v[52:55], v[212:215]\n"
"v_exp_f32 v101, v101\n"
"v_mfma_f32_16x16x32_bf16 v[212:215], a[232:235], v[20:23], v[212:215]\n"
"v_exp_f32 v102, v102\n"
"v_mfma_f32_16x16x32_bf16 v[204:207], a[232:235], v[12:15], v[204:207]\n"
"buffer_load_dwordx4 v248, s[44:47], s80 offen offset:2048 nt lds\n"
"v_mfma_f32_16x16x32_bf16 v[204:207], a[236:239], v[44:47], v[204:207]\n"
"v_mfma_f32_16x16x32_bf16 v[84:87], a[236:239], v[36:39], v[84:87]\n"
"ds_read_b64_tr_b16 a[236:237], v224 offset:160\n"
"v_add_f32 v164, v164, v101\n"
"v_mfma_f32_16x16x32_bf16 v[84:87], a[232:235], v[4:7], v[84:87]\n"
"ds_read_b64_tr_b16 a[232:233], v244 offset:16768\n"
"v_add_f32 v165, v165, v133\n"
"v_mfma_f32_16x16x32_bf16 v[32:35], a[240:243], v[4:7], v[32:35]\n"
"ds_read_b64_tr_b16 a[234:235], v246 offset:16768\n"
"v_add_f32 v164, v164, v102\n"
"v_mfma_f32_16x16x32_bf16 v[32:35], a[244:247], v[36:39], v[32:35]\n"
"ds_read_b64_tr_b16 a[238:239], v226 offset:160\n"
"v_add_f32 v165, v165, v134\n"
"v_mfma_f32_16x16x32_bf16 v[40:43], a[244:247], v[44:47], v[40:43]\n"
"v_exp_f32 v103, v103\n"
"v_mfma_f32_16x16x32_bf16 v[40:43], a[240:243], v[12:15], v[40:43]\n"
"v_exp_f32 v108, v108\n"
"v_mfma_f32_16x16x32_bf16 v[48:51], a[240:243], v[20:23], v[48:51]\n"
"ds_read_b64_tr_b16 a[240:241], v245 offset:16736\n"
"v_cvt_pk_bf16_f32 v124, v124, v125\n"
"v_mfma_f32_16x16x32_bf16 v[48:51], a[244:247], v[52:55], v[48:51]\n"
"s_waitcnt lgkmcnt(5)\n"
"ds_read_b64_tr_b16 a[242:243], v247 offset:16736\n"
"v_cvt_pk_bf16_f32 v125, v126, v127\n"
"v_mfma_f32_16x16x32_bf16 a[52:55], a[252:255], v[52:55], a[52:55]\n"
"ds_read_b64_tr_b16 a[244:245], v225 offset:128\n"
"v_add_f32 v164, v164, v103\n"
"v_mfma_f32_16x16x32_bf16 a[52:55], a[248:251], v[20:23], a[52:55]\n"
"ds_read_b64_tr_b16 a[246:247], v227 offset:128\n"
"v_cvt_pk_bf16_f32 v126, v100, v101\n"
"v_mfma_f32_16x16x32_bf16 a[44:47], a[248:251], v[12:15], a[44:47]\n"
"v_exp_f32 v109, v109\n"
"v_mfma_f32_16x16x32_bf16 a[44:47], a[252:255], v[44:47], a[44:47]\n"
"v_exp_f32 v110, v110\n"
"v_mfma_f32_16x16x32_bf16 a[36:39], a[252:255], v[36:39], a[36:39]\n"
"buffer_load_dwordx4 v248, s[44:47], s80 offen offset:3072 nt lds\n"
"v_mfma_f32_16x16x32_bf16 a[36:39], a[248:251], v[4:7], a[36:39]\n"
"s_add_u32 m0, s79, 0x0\n"
"s_add_u32 s80, s10, s24\n"
"ds_read_b64_tr_b16 a[252:253], v225 offset:160\n"
"v_mfma_f32_16x16x32_bf16 v[56:59], a[224:227], v[4:7], v[56:59]\n"
"s_addk_i32 s10, 0x1000\n"
"ds_read_b64_tr_b16 a[254:255], v227 offset:160\n"
"v_cvt_pk_bf16_f32 v127, v102, v103\n"
"v_mfma_f32_16x16x32_bf16 v[56:59], a[228:231], v[36:39], v[56:59]\n"
"s_bitset0_b32 s10, 15\n"
"ds_read_b64_tr_b16 a[248:249], v245 offset:16768\n"
"v_add_f32 v165, v165, v135\n"
"v_mfma_f32_16x16x32_bf16 v[64:67], a[228:231], v[44:47], v[64:67]\n"
"ds_read_b64_tr_b16 a[250:251], v247 offset:16768\n"
"v_add_f32 v165, v165, v108\n"
"v_mfma_f32_16x16x32_bf16 v[64:67], a[224:227], v[12:15], v[64:67]\n"
"v_exp_f32 v111, v111\n"
"v_mfma_f32_16x16x32_bf16 v[72:75], a[224:227], v[20:23], v[72:75]\n"
"v_exp_f32 v116, v116\n"
"v_mfma_f32_16x16x32_bf16 v[72:75], a[228:231], v[52:55], v[72:75]\n"
"v_exp_f32 v117, v117\n"
"v_mfma_f32_16x16x32_bf16 v[80:83], a[228:231], v[92:95], v[80:83]\n"
"ds_read_b64_tr_b16 a[228:229], v224 offset:256\n"
"v_add_f32 v166, v166, v140\n"
"v_mfma_f32_16x16x32_bf16 v[80:83], a[224:227], v[28:31], v[80:83]\n"
"s_waitcnt lgkmcnt(5)\n"
"ds_read_b64_tr_b16 a[224:225], v244 offset:16864\n"
"v_add_f32 v165, v165, v109\n"
"v_mfma_f32_16x16x32_bf16 a[84:87], a[232:235], v[28:31], a[84:87]\n"
"ds_read_b64_tr_b16 a[226:227], v246 offset:16864\n"
"v_add_f32 v166, v166, v141\n"
"v_mfma_f32_16x16x32_bf16 a[84:87], a[236:239], v[92:95], a[84:87]\n"
"ds_read_b64_tr_b16 a[230:231], v226 offset:256\n"
"v_fma_f32 v118, v118, s92, v158\n"
"v_mfma_f32_16x16x32_bf16 a[76:79], a[236:239], v[52:55], a[76:79]\n"
"v_add_f32 v165, v165, v110\n"
"v_add_f32 v166, v166, v142\n"
"v_mfma_f32_16x16x32_bf16 a[76:79], a[232:235], v[20:23], a[76:79]\n"
"v_cvt_pk_bf16_f32 v132, v132, v133\n"
"v_add_f32 v165, v165, v111\n"
"v_mfma_f32_16x16x32_bf16 a[68:71], a[232:235], v[12:15], a[68:71]\n"
"v_cvt_pk_bf16_f32 v133, v134, v135\n"
"v_add_f32 v166, v166, v143\n"
"v_mfma_f32_16x16x32_bf16 a[68:71], a[236:239], v[44:47], a[68:71]\n"
"buffer_load_dwordx4 v248, s[44:47], s81 offen offset:0 nt lds\n"
"v_mfma_f32_16x16x32_bf16 a[60:63], a[236:239], v[36:39], a[60:63]\n"
"ds_read_b64_tr_b16 a[236:237], v224 offset:288\n"
"v_mfma_f32_16x16x32_bf16 a[60:63], a[232:235], v[4:7], a[60:63]\n"
"ds_read_b64_tr_b16 a[232:233], v244 offset:16896\n"
"v_cvt_pk_bf16_f32 v134, v108, v109\n"
"v_mfma_f32_16x16x32_bf16 v[88:91], a[240:243], v[4:7], v[88:91]\n"
"ds_read_b64_tr_b16 a[234:235], v246 offset:16896\n"
"v_cvt_pk_bf16_f32 v135, v110, v111\n"
"v_mfma_f32_16x16x32_bf16 v[88:91], a[244:247], v[36:39], v[88:91]\n"
"ds_read_b64_tr_b16 a[238:239], v226 offset:288\n"
"v_fma_f32 v119, v119, s92, v158\n"
"v_mfma_f32_16x16x32_bf16 v[96:99], a[244:247], v[44:47], v[96:99]\n"
"v_exp_f32 v118, v118\n"
"v_mfma_f32_16x16x32_bf16 v[96:99], a[240:243], v[12:15], v[96:99]\n"
"v_add_f32 v166, v166, v116\n"
"v_cvt_pk_bf16_f32 v140, v140, v141\n"
"v_mfma_f32_16x16x32_bf16 v[104:107], a[240:243], v[20:23], v[104:107]\n"
"ds_read_b64_tr_b16 a[240:241], v245 offset:16864\n"
"v_add_f32 v166, v166, v117\n"
"v_mfma_f32_16x16x32_bf16 v[104:107], a[244:247], v[52:55], v[104:107]\n"
"s_waitcnt lgkmcnt(5)\n"
"ds_read_b64_tr_b16 a[242:243], v247 offset:16864\n"
"v_add_f32 v166, v166, v118\n"
"v_mfma_f32_16x16x32_bf16 a[108:111], a[252:255], v[52:55], a[108:111]\n"
"ds_read_b64_tr_b16 a[244:245], v225 offset:256\n"
"v_cvt_pk_bf16_f32 v141, v142, v143\n"
"v_mfma_f32_16x16x32_bf16 a[108:111], a[248:251], v[20:23], a[108:111]\n"
"ds_read_b64_tr_b16 a[246:247], v227 offset:256\n"
"v_cvt_pk_bf16_f32 v142, v116, v117\n"
"v_mfma_f32_16x16x32_bf16 a[100:103], a[248:251], v[12:15], a[100:103]\n"
"v_exp_f32 v119, v119\n"
"v_mfma_f32_16x16x32_bf16 a[100:103], a[252:255], v[44:47], a[100:103]\n"
"v_add_f32 v166, v166, v119\n"
"v_cvt_pk_bf16_f32 v143, v118, v119\n"
"v_mfma_f32_16x16x32_bf16 a[92:95], a[252:255], v[36:39], a[92:95]\n"
"ds_read_b64_tr_b16 a[252:253], v225 offset:288\n"
"v_fma_f32 v172, v172, s92, v159\n"
"v_mfma_f32_16x16x32_bf16 a[92:95], a[248:251], v[4:7], a[92:95]\n"
"ds_read_b64_tr_b16 a[254:255], v227 offset:288\n"
"v_fma_f32 v173, v173, s92, v159\n"
"v_mfma_f32_16x16x32_bf16 v[112:115], a[224:227], v[4:7], v[112:115]\n"
"buffer_load_dwordx4 v248, s[44:47], s81 offen offset:1024 nt lds\n"
"v_mfma_f32_16x16x32_bf16 v[112:115], a[228:231], v[36:39], v[112:115]\n"
"s_addk_i32 m0, 0x20\n"
"ds_read_b64_tr_b16 a[248:249], v245 offset:16896\n"
"v_mfma_f32_16x16x32_bf16 v[120:123], a[228:231], v[44:47], v[120:123]\n"
"ds_read_b64_tr_b16 a[250:251], v247 offset:16896\n"
"v_fma_f32 v174, v174, s92, v159\n"
"v_mfma_f32_16x16x32_bf16 v[120:123], a[224:227], v[12:15], v[120:123]\n"
"v_exp_f32 v172, v172\n"
"v_mfma_f32_16x16x32_bf16 v[128:131], a[224:227], v[20:23], v[128:131]\n"
"v_exp_f32 v173, v173\n"
"v_mfma_f32_16x16x32_bf16 v[128:131], a[228:231], v[52:55], v[128:131]\n"
"v_exp_f32 v174, v174\n"
"v_mfma_f32_16x16x32_bf16 v[136:139], a[228:231], v[92:95], v[136:139]\n"
"ds_read_b64_tr_b16 a[228:229], v224 offset:384\n"
"v_fma_f32 v175, v175, s92, v159\n"
"v_mfma_f32_16x16x32_bf16 v[136:139], a[224:227], v[28:31], v[136:139]\n"
"s_waitcnt lgkmcnt(5)\n"
"ds_read_b64_tr_b16 a[230:231], v226 offset:384\n"
"v_add_f32 v167, v167, v172\n"
"v_mfma_f32_16x16x32_bf16 a[140:143], a[232:235], v[28:31], a[140:143]\n"
"ds_read_b64_tr_b16 a[224:225], v244 offset:16992\n"
"v_add_f32 v167, v167, v173\n"
"v_mfma_f32_16x16x32_bf16 a[140:143], a[236:239], v[92:95], a[140:143]\n"
"ds_read_b64_tr_b16 a[226:227], v246 offset:16992\n"
"v_cvt_pk_bf16_f32 v172, v172, v173\n"
"v_mfma_f32_16x16x32_bf16 a[132:135], a[236:239], v[52:55], a[132:135]\n"
"v_exp_f32 v175, v175\n"
"v_mfma_f32_16x16x32_bf16 a[132:135], a[232:235], v[20:23], a[132:135]\n"
"v_cvt_pk_bf16_f32 v173, v174, v175\n"
"v_fma_f32 v180, v180, s92, v156\n"
"v_mfma_f32_16x16x32_bf16 a[124:127], a[232:235], v[12:15], a[124:127]\n"
"ds_write_b64 v253, v[172:173] offset:1024\n"
"v_fma_f32 v181, v181, s92, v156\n"
"v_mfma_f32_16x16x32_bf16 a[124:127], a[236:239], v[44:47], a[124:127]\n"
"v_exp_f32 v180, v180\n"
"v_mfma_f32_16x16x32_bf16 a[116:119], a[236:239], v[36:39], a[116:119]\n"
"buffer_load_dwordx4 v248, s[44:47], s81 offen offset:2048 nt lds\n"
"v_mfma_f32_16x16x32_bf16 a[116:119], a[232:235], v[4:7], a[116:119]\n"
"ds_read_b64_tr_b16 a[236:237], v224 offset:416\n"
"v_mfma_f32_16x16x32_bf16 v[144:147], a[240:243], v[4:7], v[144:147]\n"
"ds_read_b64_tr_b16 a[232:233], v244 offset:17024\n"
"v_fma_f32 v182, v182, s92, v156\n"
"v_mfma_f32_16x16x32_bf16 v[144:147], a[244:247], v[36:39], v[144:147]\n"
"ds_read_b64_tr_b16 a[234:235], v246 offset:17024\n"
"v_fma_f32 v183, v183, s92, v156\n"
"v_mfma_f32_16x16x32_bf16 v[152:155], a[244:247], v[44:47], v[152:155]\n"
"ds_read_b64_tr_b16 a[238:239], v226 offset:416\n"
"v_fma_f32 v188, v188, s92, v157\n"
"v_mfma_f32_16x16x32_bf16 v[152:155], a[240:243], v[12:15], v[152:155]\n"
"v_exp_f32 v181, v181\n"
"v_mfma_f32_16x16x32_bf16 v[160:163], a[240:243], v[20:23], v[160:163]\n"
"ds_read_b64_tr_b16 a[240:241], v245 offset:16992\n"
"v_fma_f32 v189, v189, s92, v157\n"
"v_mfma_f32_16x16x32_bf16 v[160:163], a[244:247], v[52:55], v[160:163]\n"
"s_waitcnt lgkmcnt(6)\n"
"ds_read_b64_tr_b16 a[242:243], v247 offset:16992\n"
"v_fma_f32 v190, v190, s92, v157\n"
"v_mfma_f32_16x16x32_bf16 a[164:167], a[252:255], v[52:55], a[164:167]\n"
"ds_read_b64_tr_b16 a[244:245], v225 offset:384\n"
"v_fma_f32 v191, v191, s92, v157\n"
"v_mfma_f32_16x16x32_bf16 a[164:167], a[248:251], v[20:23], a[164:167]\n"
"ds_read_b64_tr_b16 a[246:247], v227 offset:384\n"
"v_fma_f32 v196, v196, s92, v158\n"
"v_mfma_f32_16x16x32_bf16 a[156:159], a[248:251], v[12:15], a[156:159]\n"
"v_exp_f32 v182, v182\n"
"v_mfma_f32_16x16x32_bf16 a[156:159], a[252:255], v[44:47], a[156:159]\n"
"v_exp_f32 v183, v183\n"
"v_mfma_f32_16x16x32_bf16 a[148:151], a[252:255], v[36:39], a[148:151]\n"
"ds_read_b64_tr_b16 a[252:253], v225 offset:416\n"
"v_fma_f32 v197, v197, s92, v158\n"
"v_mfma_f32_16x16x32_bf16 a[148:151], a[248:251], v[4:7], a[148:151]\n"
"ds_read_b64_tr_b16 a[254:255], v227 offset:416\n"
"v_fma_f32 v198, v198, s92, v158\n"
"v_mfma_f32_16x16x32_bf16 v[168:171], a[224:227], v[4:7], v[168:171]\n"
"ds_read_b64_tr_b16 a[248:249], v245 offset:17024\n"
"v_fma_f32 v199, v199, s92, v158\n"
"v_mfma_f32_16x16x32_bf16 v[168:171], a[228:231], v[36:39], v[168:171]\n"
"buffer_load_dwordx4 v248, s[44:47], s81 offen offset:3072 nt lds\n"
"v_mfma_f32_16x16x32_bf16 v[176:179], a[228:231], v[44:47], v[176:179]\n"
"s_add_u32 m0, s79, 0x1c620\n"
"s_add_u32 s81, s10, s24\n"
"ds_read_b64_tr_b16 a[250:251], v247 offset:17024\n"
"v_mfma_f32_16x16x32_bf16 v[176:179], a[224:227], v[12:15], v[176:179]\n"
"s_addk_i32 s10, 0x1000\n"
"v_exp_f32 v188, v188\n"
"v_mfma_f32_16x16x32_bf16 v[184:187], a[224:227], v[20:23], v[184:187]\n"
"s_bitset0_b32 s10, 15\n"
"v_exp_f32 v189, v189\n"
"v_mfma_f32_16x16x32_bf16 v[184:187], a[228:231], v[52:55], v[184:187]\n"
"v_exp_f32 v190, v190\n"
"v_mfma_f32_16x16x32_bf16 v[192:195], a[228:231], v[92:95], v[192:195]\n"
"v_exp_f32 v191, v191\n"
"v_mfma_f32_16x16x32_bf16 v[192:195], a[224:227], v[28:31], v[192:195]\n"
"s_waitcnt vmcnt(16) lgkmcnt(0)\n"
"s_barrier\n"
"v_mfma_f32_16x16x32_bf16 a[196:199], a[232:235], v[28:31], a[196:199]\n"
"ds_read_b128 a[224:227], v240 offset:0\n"
"v_fma_f32 v148, v148, s92, v159\n"
"v_mfma_f32_16x16x32_bf16 a[196:199], a[236:239], v[92:95], a[196:199]\n"
"ds_read_b128 a[228:231], v241 offset:0\n"
"v_fma_f32 v149, v149, s92, v159\n"
"v_mfma_f32_16x16x32_bf16 a[188:191], a[236:239], v[52:55], a[188:191]\n"
"v_exp_f32 v196, v196\n"
"v_mfma_f32_16x16x32_bf16 a[188:191], a[232:235], v[20:23], a[188:191]\n"
"v_exp_f32 v197, v197\n"
"v_mfma_f32_16x16x32_bf16 a[180:183], a[232:235], v[12:15], a[180:183]\n"
"v_exp_f32 v198, v198\n"
"v_mfma_f32_16x16x32_bf16 a[180:183], a[236:239], v[44:47], a[180:183]\n"
"v_exp_f32 v199, v199\n"
"v_mfma_f32_16x16x32_bf16 a[172:175], a[236:239], v[36:39], a[172:175]\n"
"ds_read_b128 a[236:239], v241 offset:128\n"
"v_fma_f32 v60, v60, s92, v156\n"
"v_mfma_f32_16x16x32_bf16 a[172:175], a[232:235], v[4:7], a[172:175]\n"
"buffer_load_dwordx4 v248, s[48:51], s80 offen offset:0 nt lds\n"
"v_mfma_f32_16x16x32_bf16 v[200:203], a[240:243], v[4:7], v[200:203]\n"
"ds_read_b128 a[232:235], v240 offset:128\n"
"v_mfma_f32_16x16x32_bf16 v[200:203], a[244:247], v[36:39], v[200:203]\n"
"v_exp_f32 v148, v148\n"
"v_mfma_f32_16x16x32_bf16 v[208:211], a[244:247], v[44:47], v[208:211]\n"
"v_exp_f32 v149, v149\n"
"v_mfma_f32_16x16x32_bf16 v[208:211], a[240:243], v[12:15], v[208:211]\n"
"v_exp_f32 v60, v60\n"
"v_mfma_f32_16x16x32_bf16 v[216:219], a[240:243], v[20:23], v[216:219]\n"
"ds_read_b128 a[240:243], v240 offset:256\n"
"v_fma_f32 v61, v61, s92, v156\n"
"v_mfma_f32_16x16x32_bf16 v[216:219], a[244:247], v[52:55], v[216:219]\n"
"ds_read_b128 a[244:247], v241 offset:256\n"
"v_fma_f32 v62, v62, s92, v156\n"
"v_mfma_f32_16x16x32_bf16 v[236:239], a[252:255], v[52:55], v[236:239]\n"
"v_exp_f32 v61, v61\n"
"v_mfma_f32_16x16x32_bf16 v[236:239], a[248:251], v[20:23], v[236:239]\n"
"v_exp_f32 v62, v62\n"
"v_mfma_f32_16x16x32_bf16 v[232:235], a[248:251], v[12:15], v[232:235]\n"
"v_fma_f32 v63, v63, s92, v156\n"
"v_fma_f32 v68, v68, s92, v157\n"
"v_mfma_f32_16x16x32_bf16 v[232:235], a[252:255], v[44:47], v[232:235]\n"
"v_exp_f32 v63, v63\n"
"v_mfma_f32_16x16x32_bf16 v[228:231], a[252:255], v[36:39], v[228:231]\n"
"ds_read_b128 a[252:255], v241 offset:384\n"
"v_fma_f32 v69, v69, s92, v157\n"
"v_mfma_f32_16x16x32_bf16 v[228:231], a[248:251], v[4:7], v[228:231]\n"
"s_waitcnt lgkmcnt(4)\n"
"ds_read_b128 a[248:251], v240 offset:384\n"
"v_add_u32 v241, 0xffff3d60, v241\n"
"v_mfma_f32_16x16x32_bf16 v[4:7], a[224:227], a[0:3], 0\n"
"v_add_u32 v240, 0xffff3d60, v240\n"
"v_fma_f32 v70, v70, s92, v157\n"
"v_mfma_f32_16x16x32_bf16 v[4:7], a[228:231], a[32:35], v[4:7]\n"
"v_exp_f32 v68, v68\n"
"v_mfma_f32_16x16x32_bf16 v[12:15], a[228:231], a[40:43], 0\n"
"v_exp_f32 v69, v69\n"
"v_mfma_f32_16x16x32_bf16 v[12:15], a[224:227], a[8:11], v[12:15]\n"
"v_exp_f32 v70, v70\n"
"v_mfma_f32_16x16x32_bf16 v[20:23], a[224:227], a[16:19], 0\n"
"buffer_load_dwordx4 v248, s[48:51], s80 offen offset:1024 nt lds\n"
"v_mfma_f32_16x16x32_bf16 v[20:23], a[228:231], a[48:51], v[20:23]\n"
"s_addk_i32 m0, 0x20\n"
"v_mfma_f32_16x16x32_bf16 v[28:31], a[228:231], a[4:7], 0\n"
"ds_read_b128 a[228:231], v243 offset:0\n"
"v_fma_f32 v71, v71, s92, v157\n"
"v_mfma_f32_16x16x32_bf16 v[28:31], a[224:227], a[24:27], v[28:31]\n"
"s_waitcnt lgkmcnt(3)\n"
"ds_read_b128 a[224:227], v242 offset:0\n"
"v_add_f32 v164, v164, v180\n"
"v_mfma_f32_16x16x32_bf16 v[4:7], a[232:235], a[56:59], v[4:7]\n"
"v_add_f32 v164, v164, v181\n"
"v_fma_f32 v76, v76, s92, v158\n"
"v_mfma_f32_16x16x32_bf16 v[4:7], a[236:239], a[88:91], v[4:7]\n"
"v_exp_f32 v71, v71\n"
"v_mfma_f32_16x16x32_bf16 v[12:15], a[236:239], a[96:99], v[12:15]\n"
"v_add_f32 v164, v164, v182\n"
"v_fma_f32 v77, v77, s92, v158\n"
"v_mfma_f32_16x16x32_bf16 v[12:15], a[232:235], a[64:67], v[12:15]\n"
"v_add_f32 v164, v164, v183\n"
"v_add_f32 v165, v165, v188\n"
"v_mfma_f32_16x16x32_bf16 v[20:23], a[232:235], a[72:75], v[20:23]\n"
"v_exp_f32 v76, v76\n"
"v_mfma_f32_16x16x32_bf16 v[20:23], a[236:239], a[104:107], v[20:23]\n"
"v_add_f32 v164, v164, v60\n"
"v_add_f32 v165, v165, v189\n"
"v_mfma_f32_16x16x32_bf16 v[28:31], a[236:239], a[12:15], v[28:31]\n"
"ds_read_b128 a[236:239], v243 offset:128\n"
"v_add_f32 v164, v164, v61\n"
"v_mfma_f32_16x16x32_bf16 v[28:31], a[232:235], a[80:83], v[28:31]\n"
"ds_read_b128 a[232:235], v242 offset:128\n"
"v_add_f32 v164, v164, v62\n"
"v_mfma_f32_16x16x32_bf16 v[4:7], a[240:243], a[112:115], v[4:7]\n"
"v_exp_f32 v77, v77\n"
"v_mfma_f32_16x16x32_bf16 v[4:7], a[244:247], a[144:147], v[4:7]\n"
"v_add_f32 v165, v165, v190\n"
"v_cvt_pk_bf16_f32 v180, v180, v181\n"
"v_mfma_f32_16x16x32_bf16 v[12:15], a[244:247], a[152:155], v[12:15]\n"
"PG8_%=:\n"
"s_cbranch_execz PGB1_%=\n"
"v_cvt_pk_bf16_f32 v181, v182, v183\n"
"v_add_f32 v164, v164, v63\n"
"v_mfma_f32_16x16x32_bf16 v[12:15], a[240:243], a[120:123], v[12:15]\n"
"v_cvt_pk_bf16_f32 v182, v60, v61\n"
"v_cvt_pk_bf16_f32 v183, v62, v63\n"
"v_mfma_f32_16x16x32_bf16 v[20:23], a[240:243], a[128:131], v[20:23]\n"
"v_add_f32 v165, v165, v191\n"
"v_add_f32 v166, v166, v196\n"
"v_mfma_f32_16x16x32_bf16 v[20:23], a[244:247], a[160:163], v[20:23]\n"
"buffer_load_dwordx4 v248, s[48:51], s80 offen offset:2048 nt lds\n"
"v_mfma_f32_16x16x32_bf16 v[28:31], a[244:247], a[20:23], v[28:31]\n"
"ds_read_b128 a[244:247], v243 offset:256\n"
"v_mfma_f32_16x16x32_bf16 v[28:31], a[240:243], a[136:139], v[28:31]\n"
"s_waitcnt lgkmcnt(3)\n"
"ds_read_b128 a[240:243], v242 offset:256\n"
"v_add_f32 v165, v165, v68\n"
"v_mfma_f32_16x16x32_bf16 v[4:7], a[248:251], a[168:171], v[4:7]\n"
"v_add_f32 v165, v165, v69\n"
"v_add_f32 v166, v166, v197\n"
"v_mfma_f32_16x16x32_bf16 v[4:7], a[252:255], a[200:203], v[4:7]\n"
"v_fma_f32 v78, v78, s92, v158\n"
"v_add_f32 v165, v165, v70\n"
"v_mfma_f32_16x16x32_bf16 v[12:15], a[252:255], a[208:211], v[12:15]\n"
"v_add_f32 v166, v166, v198\n"
"v_cvt_pk_bf16_f32 v188, v188, v189\n"
"v_mfma_f32_16x16x32_bf16 v[12:15], a[248:251], a[176:179], v[12:15]\n"
"v_cvt_pk_bf16_f32 v189, v190, v191\n"
"v_add_f32 v165, v165, v71\n"
"v_mfma_f32_16x16x32_bf16 v[20:23], a[248:251], a[184:187], v[20:23]\n"
"v_cvt_pk_bf16_f32 v190, v68, v69\n"
"v_cvt_pk_bf16_f32 v191, v70, v71\n"
"v_mfma_f32_16x16x32_bf16 v[20:23], a[252:255], a[216:219], v[20:23]\n"
"v_add_f32 v166, v166, v199\n"
"v_fma_f32 v79, v79, s92, v158\n"
"v_mfma_f32_16x16x32_bf16 v[28:31], a[252:255], a[28:31], v[28:31]\n"
"ds_read_b128 a[252:255], v243 offset:384\n"
"v_add_f32 v166, v166, v76\n"
"v_mfma_f32_16x16x32_bf16 v[28:31], a[248:251], a[192:195], v[28:31]\n"
"ds_read_b128 a[248:251], v242 offset:384\n"
"v_add_u32 v243, 0xffff3d60, v243\n"
"v_mfma_f32_16x16x32_bf16 v[100:103], a[224:227], a[0:3], 0\n"
"v_add_u32 v242, 0xffff3d60, v242\n"
"v_add_f32 v166, v166, v77\n"
"v_mfma_f32_16x16x32_bf16 v[100:103], a[228:231], a[32:35], v[100:103]\n"
"v_exp_f32 v78, v78\n"
"v_mfma_f32_16x16x32_bf16 v[108:111], a[228:231], a[40:43], 0\n"
"v_exp_f32 v79, v79\n"
"v_mfma_f32_16x16x32_bf16 v[108:111], a[224:227], a[8:11], v[108:111]\n"
"v_fma_f32 v150, v150, s92, v159\n"
"v_add_f32 v166, v166, v78\n"
"v_mfma_f32_16x16x32_bf16 v[116:119], a[224:227], a[16:19], 0\n"
"ds_read_b128 a[224:227], v240 offset:0\n"
"v_cvt_pk_bf16_f32 v196, v196, v197\n"
"v_mfma_f32_16x16x32_bf16 v[116:119], a[228:231], a[48:51], v[116:119]\n"
"s_waitcnt lgkmcnt(3)\n"
"ds_read_b128 a[228:231], v241 offset:0\n"
"v_cvt_pk_bf16_f32 v197, v198, v199\n"
"v_mfma_f32_16x16x32_bf16 v[100:103], a[232:235], a[56:59], v[100:103]\n"
"buffer_load_dwordx4 v248, s[48:51], s80 offen offset:3072 nt lds\n"
"v_mfma_f32_16x16x32_bf16 v[100:103], a[236:239], a[88:91], v[100:103]\n"
"s_add_u32 m0, s79, 0x81c0\n"
"s_add_u32 s80, s9, s24\n"
"v_mfma_f32_16x16x32_bf16 v[108:111], a[236:239], a[96:99], v[108:111]\n"
"s_addk_i32 s9, 0x1000\n"
"v_add_f32 v166, v166, v79\n"
"v_cvt_pk_bf16_f32 v198, v76, v77\n"
"v_mfma_f32_16x16x32_bf16 v[108:111], a[232:235], a[64:67], v[108:111]\n"
"s_bitset0_b32 s9, 15\n"
"v_cvt_pk_bf16_f32 v199, v78, v79\n"
"v_fma_f32 v151, v151, s92, v159\n"
"v_mfma_f32_16x16x32_bf16 v[116:119], a[232:235], a[72:75], v[116:119]\n"
"ds_read_b128 a[232:235], v240 offset:128\n"
"v_add_f32 v167, v167, v174\n"
"v_mfma_f32_16x16x32_bf16 v[116:119], a[236:239], a[104:107], v[116:119]\n"
"ds_read_b128 a[236:239], v241 offset:128\n"
"v_add_f32 v167, v167, v175\n"
"v_mfma_f32_16x16x32_bf16 v[100:103], a[240:243], a[112:115], v[100:103]\n"
"v_exp_f32 v150, v150\n"
"v_mfma_f32_16x16x32_bf16 v[100:103], a[244:247], a[144:147], v[100:103]\n"
"v_exp_f32 v151, v151\n"
"v_mfma_f32_16x16x32_bf16 v[108:111], a[244:247], a[152:155], v[108:111]\n"
"v_add_f32 v167, v167, v148\n"
"v_add_u32 v244, 0x1c620, v244\n"
"v_mfma_f32_16x16x32_bf16 v[108:111], a[240:243], a[120:123], v[108:111]\n"
"v_add_f32 v167, v167, v149\n"
"v_cvt_pk_bf16_f32 v148, v148, v149\n"
"v_mfma_f32_16x16x32_bf16 v[116:119], a[240:243], a[128:131], v[116:119]\n"
"ds_read_b128 a[240:243], v240 offset:256\n"
"v_cvt_pk_bf16_f32 v149, v150, v151\n"
"v_mfma_f32_16x16x32_bf16 v[116:119], a[244:247], a[160:163], v[116:119]\n"
"s_waitcnt lgkmcnt(3)\n"
"ds_read_b128 a[244:247], v241 offset:256\n"
"v_add_u32 v246, 0x1c620, v246\n"
"v_mfma_f32_16x16x32_bf16 v[100:103], a[248:251], a[168:171], v[100:103]\n"
"ds_write_b64 v253, v[148:149] offset:1536\n"
"v_add_u32 v224, 0xffff3d60, v224\n"
"v_mfma_f32_16x16x32_bf16 v[100:103], a[252:255], a[200:203], v[100:103]\n"
"v_add_u32 v226, 0xffff3d60, v226\n"
"v_add_f32 v167, v167, v150\n"
"v_mfma_f32_16x16x32_bf16 v[108:111], a[252:255], a[208:211], v[108:111]\n"
"v_add_f32 v167, v167, v151\n"
"v_add_u32 v245, 0x1c620, v245\n"
"v_mfma_f32_16x16x32_bf16 v[108:111], a[248:251], a[176:179], v[108:111]\n"
"v_add_u32 v247, 0x1c620, v247\n"
"v_add_u32 v225, 0xffff3d60, v225\n"
"v_mfma_f32_16x16x32_bf16 v[116:119], a[248:251], a[184:187], v[116:119]\n"
"ds_read_b128 a[248:251], v240 offset:384\n"
"v_add_u32 v227, 0xffff3d60, v227\n"
"v_mfma_f32_16x16x32_bf16 v[116:119], a[252:255], a[216:219], v[116:119]\n"
"buffer_load_dwordx4 v248, s[48:51], s81 offen offset:0 nt lds\n"
"v_mfma_f32_16x16x32_bf16 v[36:39], a[224:227], a[0:3], 0\n"
"ds_read_b128 a[252:255], v241 offset:384\n"
"v_mfma_f32_16x16x32_bf16 v[36:39], a[228:231], a[32:35], v[36:39]\n"
"v_fma_f32 v4, v4, s92, v156\n"
"v_fma_f32 v5, v5, s92, v156\n"
"v_mfma_f32_16x16x32_bf16 v[44:47], a[228:231], a[40:43], 0\n"
"v_exp_f32 v4, v4\n"
"v_mfma_f32_16x16x32_bf16 v[44:47], a[224:227], a[8:11], v[44:47]\n"
"v_exp_f32 v5, v5\n"
"v_mfma_f32_16x16x32_bf16 v[52:55], a[224:227], a[16:19], 0\n"
"v_fma_f32 v6, v6, s92, v156\n"
"v_fma_f32 v7, v7, s92, v156\n"
"v_mfma_f32_16x16x32_bf16 v[52:55], a[228:231], a[48:51], v[52:55]\n"
"v_exp_f32 v6, v6\n"
"v_mfma_f32_16x16x32_bf16 v[92:95], a[228:231], a[4:7], 0\n"
"ds_read_b128 a[228:231], v243 offset:0\n"
"v_fma_f32 v12, v12, s92, v157\n"
"v_mfma_f32_16x16x32_bf16 v[92:95], a[224:227], a[24:27], v[92:95]\n"
"s_waitcnt lgkmcnt(3)\n"
"ds_read_b128 a[224:227], v242 offset:0\n"
"v_fma_f32 v13, v13, s92, v157\n"
"v_mfma_f32_16x16x32_bf16 v[36:39], a[232:235], a[56:59], v[36:39]\n"
"v_exp_f32 v7, v7\n"
"v_mfma_f32_16x16x32_bf16 v[36:39], a[236:239], a[88:91], v[36:39]\n"
"v_exp_f32 v12, v12\n"
"v_mfma_f32_16x16x32_bf16 v[44:47], a[236:239], a[96:99], v[44:47]\n"
"v_exp_f32 v13, v13\n"
"v_mfma_f32_16x16x32_bf16 v[44:47], a[232:235], a[64:67], v[44:47]\n"
"v_fma_f32 v14, v14, s92, v157\n"
"v_fma_f32 v15, v15, s92, v157\n"
"v_mfma_f32_16x16x32_bf16 v[52:55], a[232:235], a[72:75], v[52:55]\n"
"v_exp_f32 v14, v14\n"
"v_mfma_f32_16x16x32_bf16 v[52:55], a[236:239], a[104:107], v[52:55]\n"
"v_exp_f32 v15, v15\n"
"v_mfma_f32_16x16x32_bf16 v[92:95], a[236:239], a[12:15], v[92:95]\n"
"ds_read_b128 a[236:239], v243 offset:128\n"
"v_fma_f32 v20, v20, s92, v158\n"
"v_mfma_f32_16x16x32_bf16 v[92:95], a[232:235], a[80:83], v[92:95]\n"
"ds_read_b128 a[232:235], v242 offset:128\n"
"v_fma_f32 v21, v21, s92, v158\n"
"v_mfma_f32_16x16x32_bf16 v[36:39], a[240:243], a[112:115], v[36:39]\n"
"buffer_load_dwordx4 v248, s[48:51], s81 offen offset:1024 nt lds\n"
"v_mfma_f32_16x16x32_bf16 v[36:39], a[244:247], a[144:147], v[36:39]\n"
"s_addk_i32 m0, 0x20\n"
"v_mfma_f32_16x16x32_bf16 v[44:47], a[244:247], a[152:155], v[44:47]\n"
"v_exp_f32 v20, v20\n"
"v_mfma_f32_16x16x32_bf16 v[44:47], a[240:243], a[120:123], v[44:47]\n"
"v_exp_f32 v21, v21\n"
"v_mfma_f32_16x16x32_bf16 v[52:55], a[240:243], a[128:131], v[52:55]\n"
"v_fma_f32 v22, v22, s92, v158\n"
"v_fma_f32 v23, v23, s92, v158\n"
"v_mfma_f32_16x16x32_bf16 v[52:55], a[244:247], a[160:163], v[52:55]\n"
"v_exp_f32 v22, v22\n"
"v_mfma_f32_16x16x32_bf16 v[92:95], a[244:247], a[20:23], v[92:95]\n"
"ds_read_b128 a[244:247], v243 offset:256\n"
"v_fma_f32 v100, v100, s92, v156\n"
"v_mfma_f32_16x16x32_bf16 v[92:95], a[240:243], a[136:139], v[92:95]\n"
"s_waitcnt lgkmcnt(3)\n"
"ds_read_b128 a[240:243], v242 offset:256\n"
"v_fma_f32 v101, v101, s92, v156\n"
"v_mfma_f32_16x16x32_bf16 v[36:39], a[248:251], a[168:171], v[36:39]\n"
"v_exp_f32 v23, v23\n"
"v_mfma_f32_16x16x32_bf16 v[36:39], a[252:255], a[200:203], v[36:39]\n"
"v_exp_f32 v100, v100\n"
"v_mfma_f32_16x16x32_bf16 v[44:47], a[252:255], a[208:211], v[44:47]\n"
"v_exp_f32 v101, v101\n"
"v_mfma_f32_16x16x32_bf16 v[44:47], a[248:251], a[176:179], v[44:47]\n"
"v_fma_f32 v102, v102, s92, v156\n"
"v_fma_f32 v103, v103, s92, v156\n"
"v_mfma_f32_16x16x32_bf16 v[52:55], a[248:251], a[184:187], v[52:55]\n"
"v_exp_f32 v102, v102\n"
"v_mfma_f32_16x16x32_bf16 v[52:55], a[252:255], a[216:219], v[52:55]\n"
"v_exp_f32 v103, v103\n"
"v_mfma_f32_16x16x32_bf16 v[92:95], a[252:255], a[28:31], v[92:95]\n"
"ds_read_b128 a[252:255], v243 offset:384\n"
"v_fma_f32 v108, v108, s92, v157\n"
"v_mfma_f32_16x16x32_bf16 v[92:95], a[248:251], a[192:195], v[92:95]\n"
"ds_read_b128 a[248:251], v242 offset:384\n"
"v_fma_f32 v109, v109, s92, v157\n"
"v_mfma_f32_16x16x32_bf16 v[60:63], a[224:227], a[0:3], 0\n"
"v_exp_f32 v108, v108\n"
"v_mfma_f32_16x16x32_bf16 v[60:63], a[228:231], a[32:35], v[60:63]\n"
"buffer_load_dwordx4 v248, s[48:51], s81 offen offset:2048 nt lds\n"
"v_mfma_f32_16x16x32_bf16 v[68:71], a[228:231], a[40:43], 0\n"
"v_mfma_f32_16x16x32_bf16 v[68:71], a[224:227], a[8:11], v[68:71]\n"
"v_exp_f32 v109, v109\n"
"v_mfma_f32_16x16x32_bf16 v[76:79], a[224:227], a[16:19], 0\n"
"s_waitcnt vmcnt(15) lgkmcnt(0)\n"
"s_barrier\n"
"v_mfma_f32_16x16x32_bf16 v[76:79], a[228:231], a[48:51], v[76:79]\n"
"ds_read_b64_tr_b16 a[224:225], v244 offset:0\n"
"v_fma_f32 v110, v110, s92, v157\n"
"v_mfma_f32_16x16x32_bf16 v[60:63], a[232:235], a[56:59], v[60:63]\n"
"ds_read_b64_tr_b16 a[226:227], v246 offset:0\n"
"v_fma_f32 v111, v111, s92, v157\n"
"v_mfma_f32_16x16x32_bf16 v[60:63], a[236:239], a[88:91], v[60:63]\n"
"ds_read_b64_tr_b16 a[228:229], v224 offset:0\n"
"v_add_f32 v164, v164, v4\n"
"v_mfma_f32_16x16x32_bf16 v[68:71], a[236:239], a[96:99], v[68:71]\n"
"ds_read_b64_tr_b16 a[230:231], v226 offset:0\n"
"v_add_f32 v164, v164, v5\n"
"v_mfma_f32_16x16x32_bf16 v[68:71], a[232:235], a[64:67], v[68:71]\n"
"ds_read_b64 v[174:175], v254 offset:1024\n"
"v_fma_f32 v116, v116, s92, v158\n"
"v_mfma_f32_16x16x32_bf16 v[76:79], a[232:235], a[72:75], v[76:79]\n"
"ds_read_b64 v[150:151], v254 offset:1536\n"
"v_add_f32 v164, v164, v6\n"
"v_mfma_f32_16x16x32_bf16 v[76:79], a[236:239], a[104:107], v[76:79]\n"
"ds_read_b64_tr_b16 a[232:233], v244 offset:32\n"
"v_add_f32 v164, v164, v7\n"
"v_mfma_f32_16x16x32_bf16 v[60:63], a[240:243], a[112:115], v[60:63]\n"
"ds_read_b64_tr_b16 a[234:235], v246 offset:32\n"
"v_fma_f32 v117, v117, s92, v158\n"
"v_mfma_f32_16x16x32_bf16 v[60:63], a[244:247], a[144:147], v[60:63]\n"
"ds_read_b64_tr_b16 a[236:237], v224 offset:32\n"
"v_add_f32 v164, v164, v100\n"
"v_mfma_f32_16x16x32_bf16 v[68:71], a[244:247], a[152:155], v[68:71]\n"
"ds_read_b64_tr_b16 a[238:239], v226 offset:32\n"
"v_add_f32 v165, v165, v12\n"
"v_mfma_f32_16x16x32_bf16 v[68:71], a[240:243], a[120:123], v[68:71]\n"
"v_exp_f32 v110, v110\n"
"v_mfma_f32_16x16x32_bf16 v[76:79], a[240:243], a[128:131], v[76:79]\n"
"ds_read_b64_tr_b16 a[240:241], v245 offset:0\n"
"v_add_f32 v164, v164, v101\n"
"v_mfma_f32_16x16x32_bf16 v[76:79], a[244:247], a[160:163], v[76:79]\n"
"ds_read_b64_tr_b16 a[242:243], v247 offset:0\n"
"v_add_f32 v165, v165, v13\n"
"v_mfma_f32_16x16x32_bf16 v[60:63], a[248:251], a[168:171], v[60:63]\n"
"buffer_load_dwordx4 v248, s[48:51], s81 offen offset:3072 nt lds\n"
"v_mfma_f32_16x16x32_bf16 v[60:63], a[252:255], a[200:203], v[60:63]\n"
"s_add_u32 m0, s79, 0x10380\n"
"s_add_u32 s81, s9, s24\n"
"ds_read_b64_tr_b16 a[244:245], v225 offset:0\n"
"v_mfma_f32_16x16x32_bf16 v[68:71], a[252:255], a[208:211], v[68:71]\n"
"s_addk_i32 s9, 0x1000\n"
"ds_read_b64_tr_b16 a[246:247], v227 offset:0\n"
"v_add_f32 v164, v164, v102\n"
"v_mfma_f32_16x16x32_bf16 v[68:71], a[248:251], a[176:179], v[68:71]\n"
"s_bitset0_b32 s9, 15\n"
"v_exp_f32 v111, v111\n"
"v_mfma_f32_16x16x32_bf16 v[76:79], a[248:251], a[184:187], v[76:79]\n"
"ds_read_b64_tr_b16 a[248:249], v245 offset:32\n"
"v_add_f32 v165, v165, v14\n"
"v_mfma_f32_16x16x32_bf16 v[76:79], a[252:255], a[216:219], v[76:79]\n"
"s_waitcnt lgkmcnt(8)\n"
"ds_read_b64_tr_b16 a[252:253], v225 offset:32\n"
"v_cvt_pk_bf16_f32 v4, v4, v5\n"
"v_mfma_f32_16x16x32_bf16 v[0:3], a[224:227], v[124:127], v[0:3]\n"
"ds_read_b64_tr_b16 a[254:255], v227 offset:32\n"
"v_cvt_pk_bf16_f32 v5, v6, v7\n"
"v_mfma_f32_16x16x32_bf16 v[0:3], a[228:231], v[180:183], v[0:3]\n"
"ds_read_b64_tr_b16 a[250:251], v247 offset:32\n"
"v_add_f32 v164, v164, v103\n"
"v_mfma_f32_16x16x32_bf16 v[8:11], a[228:231], v[188:191], v[8:11]\n"
"v_exp_f32 v116, v116\n"
"v_mfma_f32_16x16x32_bf16 v[8:11], a[224:227], v[132:135], v[8:11]\n"
"v_exp_f32 v117, v117\n"
"v_mfma_f32_16x16x32_bf16 v[16:19], a[224:227], v[140:143], v[16:19]\n"
"v_cvt_pk_bf16_f32 v6, v100, v101\n"
"v_cvt_pk_bf16_f32 v7, v102, v103\n"
"v_mfma_f32_16x16x32_bf16 v[16:19], a[228:231], v[196:199], v[16:19]\n"
"v_add_f32 v165, v165, v15\n"
"v_add_f32 v166, v166, v20\n"
"v_mfma_f32_16x16x32_bf16 v[24:27], a[228:231], v[148:151], v[24:27]\n"
"ds_read_b64_tr_b16 a[228:229], v224 offset:128\n"
"v_add_f32 v165, v165, v108\n"
"v_mfma_f32_16x16x32_bf16 v[24:27], a[224:227], v[172:175], v[24:27]\n"
"s_waitcnt lgkmcnt(6)\n"
"ds_read_b64_tr_b16 a[224:225], v244 offset:128\n"
"v_add_f32 v165, v165, v109\n"
"v_mfma_f32_16x16x32_bf16 v[220:223], a[232:235], v[172:175], v[220:223]\n"
"ds_read_b64_tr_b16 a[226:227], v246 offset:128\n"
"v_add_f32 v166, v166, v21\n"
"v_mfma_f32_16x16x32_bf16 v[220:223], a[236:239], v[148:151], v[220:223]\n"
"ds_read_b64_tr_b16 a[230:231], v226 offset:128\n"
"v_fma_f32 v118, v118, s92, v158\n"
"v_mfma_f32_16x16x32_bf16 v[212:215], a[236:239], v[196:199], v[212:215]\n"
"v_add_f32 v165, v165, v110\n"
"v_add_f32 v166, v166, v22\n"
"v_mfma_f32_16x16x32_bf16 v[212:215], a[232:235], v[140:143], v[212:215]\n"
"buffer_load_dwordx4 v248, s[44:47], s80 offen offset:0 nt lds\n"
"v_mfma_f32_16x16x32_bf16 v[204:207], a[232:235], v[132:135], v[204:207]\n"
"v_mfma_f32_16x16x32_bf16 v[204:207], a[236:239], v[188:191], v[204:207]\n"
"v_cvt_pk_bf16_f32 v12, v12, v13\n"
"v_add_f32 v165, v165, v111\n"
"v_mfma_f32_16x16x32_bf16 v[84:87], a[236:239], v[180:183], v[84:87]\n"
"ds_read_b64_tr_b16 a[236:237], v224 offset:160\n"
"v_cvt_pk_bf16_f32 v13, v14, v15\n"
"v_mfma_f32_16x16x32_bf16 v[84:87], a[232:235], v[124:127], v[84:87]\n"
"ds_read_b64_tr_b16 a[232:233], v244 offset:160\n"
"v_cvt_pk_bf16_f32 v14, v108, v109\n"
"v_mfma_f32_16x16x32_bf16 v[32:35], a[240:243], v[124:127], v[32:35]\n"
"s_waitcnt lgkmcnt(6)\n"
"ds_read_b64_tr_b16 a[234:235], v246 offset:160\n"
"v_cvt_pk_bf16_f32 v15, v110, v111\n"
"v_mfma_f32_16x16x32_bf16 v[32:35], a[244:247], v[180:183], v[32:35]\n"
"ds_read_b64_tr_b16 a[238:239], v226 offset:160\n"
"v_add_f32 v166, v166, v23\n"
"v_mfma_f32_16x16x32_bf16 v[40:43], a[244:247], v[188:191], v[40:43]\n"
"v_fma_f32 v119, v119, s92, v158\n"
"v_add_f32 v166, v166, v116\n"
"v_mfma_f32_16x16x32_bf16 v[40:43], a[240:243], v[132:135], v[40:43]\n"
"v_exp_f32 v118, v118\n"
"v_mfma_f32_16x16x32_bf16 v[48:51], a[240:243], v[140:143], v[48:51]\n"
"ds_read_b64_tr_b16 a[240:241], v245 offset:128\n"
"v_add_f32 v166, v166, v117\n"
"v_mfma_f32_16x16x32_bf16 v[48:51], a[244:247], v[196:199], v[48:51]\n"
"ds_read_b64_tr_b16 a[242:243], v247 offset:128\n"
"v_add_f32 v166, v166, v118\n"
"v_mfma_f32_16x16x32_bf16 a[52:55], a[252:255], v[196:199], a[52:55]\n"
"ds_read_b64_tr_b16 a[244:245], v225 offset:128\n"
"v_cvt_pk_bf16_f32 v20, v20, v21\n"
"v_mfma_f32_16x16x32_bf16 a[52:55], a[248:251], v[140:143], a[52:55]\n"
"ds_read_b64_tr_b16 a[246:247], v227 offset:128\n"
"v_cvt_pk_bf16_f32 v21, v22, v23\n"
"v_mfma_f32_16x16x32_bf16 a[44:47], a[248:251], v[132:135], a[44:47]\n"
"v_exp_f32 v119, v119\n"
"v_mfma_f32_16x16x32_bf16 a[44:47], a[252:255], v[188:191], a[44:47]\n"
"v_add_f32 v166, v166, v119\n"
"v_cvt_pk_bf16_f32 v22, v116, v117\n"
"v_mfma_f32_16x16x32_bf16 a[36:39], a[252:255], v[180:183], a[36:39]\n"
"ds_read_b64_tr_b16 a[252:253], v225 offset:160\n"
"v_cvt_pk_bf16_f32 v23, v118, v119\n"
"v_mfma_f32_16x16x32_bf16 a[36:39], a[248:251], v[124:127], a[36:39]\n"
"s_waitcnt lgkmcnt(7)\n"
"ds_read_b64_tr_b16 a[254:255], v227 offset:160\n"
"v_fma_f32 v28, v28, s92, v159\n"
"v_mfma_f32_16x16x32_bf16 v[56:59], a[224:227], v[124:127], v[56:59]\n"
"buffer_load_dwordx4 v248, s[44:47], s80 offen offset:1024 nt lds\n"
"v_mfma_f32_16x16x32_bf16 v[56:59], a[228:231], v[180:183], v[56:59]\n"
"s_addk_i32 m0, 0x20\n"
"ds_read_b64_tr_b16 a[248:249], v245 offset:160\n"
"v_mfma_f32_16x16x32_bf16 v[64:67], a[228:231], v[188:191], v[64:67]\n"
"ds_read_b64_tr_b16 a[250:251], v247 offset:160\n"
"v_fma_f32 v29, v29, s92, v159\n"
"v_mfma_f32_16x16x32_bf16 v[64:67], a[224:227], v[132:135], v[64:67]\n"
"v_exp_f32 v28, v28\n"
"v_mfma_f32_16x16x32_bf16 v[72:75], a[224:227], v[140:143], v[72:75]\n"
"v_exp_f32 v29, v29\n"
"v_mfma_f32_16x16x32_bf16 v[72:75], a[228:231], v[196:199], v[72:75]\n"
"v_fma_f32 v30, v30, s92, v159\n"
"v_fma_f32 v31, v31, s92, v159\n"
"v_mfma_f32_16x16x32_bf16 v[80:83], a[228:231], v[148:151], v[80:83]\n"
"ds_read_b64_tr_b16 a[228:229], v224 offset:256\n"
"v_fma_f32 v36, v36, s92, v156\n"
"v_mfma_f32_16x16x32_bf16 v[80:83], a[224:227], v[172:175], v[80:83]\n"
"s_waitcnt lgkmcnt(5)\n"
"ds_read_b64_tr_b16 a[224:225], v244 offset:256\n"
"v_fma_f32 v37, v37, s92, v156\n"
"v_mfma_f32_16x16x32_bf16 a[84:87], a[232:235], v[172:175], a[84:87]\n"
"ds_read_b64_tr_b16 a[226:227], v246 offset:256\n"
"v_fma_f32 v38, v38, s92, v156\n"
"v_mfma_f32_16x16x32_bf16 a[84:87], a[236:239], v[148:151], a[84:87]\n"
"ds_read_b64_tr_b16 a[230:231], v226 offset:256\n"
"v_fma_f32 v39, v39, s92, v156\n"
"v_mfma_f32_16x16x32_bf16 a[76:79], a[236:239], v[196:199], a[76:79]\n"
"v_exp_f32 v30, v30\n"
"v_mfma_f32_16x16x32_bf16 a[76:79], a[232:235], v[140:143], a[76:79]\n"
"v_exp_f32 v31, v31\n"
"v_mfma_f32_16x16x32_bf16 a[68:71], a[232:235], v[132:135], a[68:71]\n"
"v_exp_f32 v36, v36\n"
"v_mfma_f32_16x16x32_bf16 a[68:71], a[236:239], v[188:191], a[68:71]\n"
"v_exp_f32 v37, v37\n"
"v_mfma_f32_16x16x32_bf16 a[60:63], a[236:239], v[180:183], a[60:63]\n"
"ds_read_b64_tr_b16 a[236:237], v224 offset:288\n"
"v_fma_f32 v44, v44, s92, v157\n"
"v_mfma_f32_16x16x32_bf16 a[60:63], a[232:235], v[124:127], a[60:63]\n"
"ds_read_b64_tr_b16 a[232:233], v244 offset:288\n"
"v_fma_f32 v45, v45, s92, v157\n"
"v_mfma_f32_16x16x32_bf16 v[88:91], a[240:243], v[124:127], v[88:91]\n"
"ds_read_b64_tr_b16 a[234:235], v246 offset:288\n"
"v_fma_f32 v46, v46, s92, v157\n"
"v_mfma_f32_16x16x32_bf16 v[88:91], a[244:247], v[180:183], v[88:91]\n"
"buffer_load_dwordx4 v248, s[44:47], s80 offen offset:2048 nt lds\n"
"v_mfma_f32_16x16x32_bf16 v[96:99], a[244:247], v[188:191], v[96:99]\n"
"ds_read_b64_tr_b16 a[238:239], v226 offset:288\n"
"v_mfma_f32_16x16x32_bf16 v[96:99], a[240:243], v[132:135], v[96:99]\n"
"v_exp_f32 v38, v38\n"
"v_mfma_f32_16x16x32_bf16 v[104:107], a[240:243], v[140:143], v[104:107]\n"
"ds_read_b64_tr_b16 a[240:241], v245 offset:256\n"
"v_fma_f32 v47, v47, s92, v157\n"
"v_mfma_f32_16x16x32_bf16 v[104:107], a[244:247], v[196:199], v[104:107]\n"
"s_waitcnt lgkmcnt(5)\n"
"ds_read_b64_tr_b16 a[242:243], v247 offset:256\n"
"v_fma_f32 v52, v52, s92, v158\n"
"v_mfma_f32_16x16x32_bf16 a[108:111], a[252:255], v[196:199], a[108:111]\n"
"ds_read_b64_tr_b16 a[244:245], v225 offset:256\n"
"v_fma_f32 v53, v53, s92, v158\n"
"v_mfma_f32_16x16x32_bf16 a[108:111], a[248:251], v[140:143], a[108:111]\n"
"ds_read_b64_tr_b16 a[246:247], v227 offset:256\n"
"v_fma_f32 v54, v54, s92, v158\n"
"v_mfma_f32_16x16x32_bf16 a[100:103], a[248:251], v[132:135], a[100:103]\n"
"v_exp_f32 v39, v39\n"
"v_mfma_f32_16x16x32_bf16 a[100:103], a[252:255], v[188:191], a[100:103]\n"
"v_exp_f32 v44, v44\n"
"v_mfma_f32_16x16x32_bf16 a[92:95], a[252:255], v[180:183], a[92:95]\n"
"ds_read_b64_tr_b16 a[252:253], v225 offset:288\n"
"v_fma_f32 v55, v55, s92, v158\n"
"v_mfma_f32_16x16x32_bf16 a[92:95], a[248:251], v[124:127], a[92:95]\n"
"ds_read_b64_tr_b16 a[254:255], v227 offset:288\n"
"v_fma_f32 v92, v92, s92, v159\n"
"v_mfma_f32_16x16x32_bf16 v[112:115], a[224:227], v[124:127], v[112:115]\n"
"ds_read_b64_tr_b16 a[248:249], v245 offset:288\n"
"v_fma_f32 v93, v93, s92, v159\n"
"v_mfma_f32_16x16x32_bf16 v[112:115], a[228:231], v[180:183], v[112:115]\n"
"ds_read_b64_tr_b16 a[250:251], v247 offset:288\n"
"v_fma_f32 v94, v94, s92, v159\n"
"v_mfma_f32_16x16x32_bf16 v[120:123], a[228:231], v[188:191], v[120:123]\n"
"v_exp_f32 v45, v45\n"
"v_mfma_f32_16x16x32_bf16 v[120:123], a[224:227], v[132:135], v[120:123]\n"
"v_exp_f32 v46, v46\n"
"v_mfma_f32_16x16x32_bf16 v[128:131], a[224:227], v[140:143], v[128:131]\n"
"v_exp_f32 v47, v47\n"
"v_mfma_f32_16x16x32_bf16 v[128:131], a[228:231], v[196:199], v[128:131]\n"
"buffer_load_dwordx4 v248, s[44:47], s80 offen offset:3072 nt lds\n"
"v_mfma_f32_16x16x32_bf16 v[136:139], a[228:231], v[148:151], v[136:139]\n"
"s_add_u32 m0, s79, 0xc2a0\n"
"s_add_u32 s80, s10, s24\n"
"ds_read_b64_tr_b16 a[228:229], v224 offset:384\n"
"v_mfma_f32_16x16x32_bf16 v[136:139], a[224:227], v[172:175], v[136:139]\n"
"s_waitcnt lgkmcnt(5)\n"
"ds_read_b64_tr_b16 a[224:225], v244 offset:384\n"
"v_fma_f32 v95, v95, s92, v159\n"
"v_mfma_f32_16x16x32_bf16 a[140:143], a[232:235], v[172:175], a[140:143]\n"
"s_addk_i32 s10, 0x1000\n"
"ds_read_b64_tr_b16 a[226:227], v246 offset:384\n"
"v_fma_f32 v60, v60, s92, v156\n"
"v_mfma_f32_16x16x32_bf16 a[140:143], a[236:239], v[148:151], a[140:143]\n"
"s_bitset0_b32 s10, 15\n"
"ds_read_b64_tr_b16 a[230:231], v226 offset:384\n"
"v_fma_f32 v61, v61, s92, v156\n"
"v_mfma_f32_16x16x32_bf16 a[132:135], a[236:239], v[196:199], a[132:135]\n"
"v_exp_f32 v52, v52\n"
"v_mfma_f32_16x16x32_bf16 a[132:135], a[232:235], v[140:143], a[132:135]\n"
"v_exp_f32 v53, v53\n"
"v_mfma_f32_16x16x32_bf16 a[124:127], a[232:235], v[132:135], a[124:127]\n"
"v_exp_f32 v54, v54\n"
"v_mfma_f32_16x16x32_bf16 a[124:127], a[236:239], v[188:191], a[124:127]\n"
"v_exp_f32 v55, v55\n"
"v_mfma_f32_16x16x32_bf16 a[116:119], a[236:239], v[180:183], a[116:119]\n"
"ds_read_b64_tr_b16 a[236:237], v224 offset:416\n"
"v_fma_f32 v62, v62, s92, v156\n"
"v_mfma_f32_16x16x32_bf16 a[116:119], a[232:235], v[124:127], a[116:119]\n"
"ds_read_b64_tr_b16 a[232:233], v244 offset:416\n"
"v_fma_f32 v63, v63, s92, v156\n"
"v_mfma_f32_16x16x32_bf16 v[144:147], a[240:243], v[124:127], v[144:147]\n"
"ds_read_b64_tr_b16 a[234:235], v246 offset:416\n"
"v_fma_f32 v68, v68, s92, v157\n"
"v_mfma_f32_16x16x32_bf16 v[144:147], a[244:247], v[180:183], v[144:147]\n"
"ds_read_b64_tr_b16 a[238:239], v226 offset:416\n"
"v_fma_f32 v69, v69, s92, v157\n"
"v_mfma_f32_16x16x32_bf16 v[152:155], a[244:247], v[188:191], v[152:155]\n"
"v_exp_f32 v92, v92\n"
"v_mfma_f32_16x16x32_bf16 v[152:155], a[240:243], v[132:135], v[152:155]\n"
"v_exp_f32 v93, v93\n"
"v_mfma_f32_16x16x32_bf16 v[160:163], a[240:243], v[140:143], v[160:163]\n"
"ds_read_b64_tr_b16 a[240:241], v245 offset:384\n"
"v_fma_f32 v70, v70, s92, v157\n"
"v_mfma_f32_16x16x32_bf16 v[160:163], a[244:247], v[196:199], v[160:163]\n"
"s_waitcnt lgkmcnt(5)\n"
"ds_read_b64_tr_b16 a[242:243], v247 offset:384\n"
"v_fma_f32 v71, v71, s92, v157\n"
"v_mfma_f32_16x16x32_bf16 a[164:167], a[252:255], v[196:199], a[164:167]\n"
"buffer_load_dwordx4 v248, s[44:47], s81 offen offset:0 nt lds\n"
"v_mfma_f32_16x16x32_bf16 a[164:167], a[248:251], v[140:143], a[164:167]\n"
"ds_read_b64_tr_b16 a[244:245], v225 offset:384\n"
"v_mfma_f32_16x16x32_bf16 a[156:159], a[248:251], v[132:135], a[156:159]\n"
"ds_read_b64_tr_b16 a[246:247], v227 offset:384\n"
"v_add_f32 v164, v164, v36\n"
"v_mfma_f32_16x16x32_bf16 a[156:159], a[252:255], v[188:191], a[156:159]\n"
"v_exp_f32 v94, v94\n"
"v_mfma_f32_16x16x32_bf16 a[148:151], a[252:255], v[180:183], a[148:151]\n"
"ds_read_b64_tr_b16 a[252:253], v225 offset:416\n"
"v_add_f32 v164, v164, v37\n"
"v_mfma_f32_16x16x32_bf16 a[148:151], a[248:251], v[124:127], a[148:151]\n"
"ds_read_b64_tr_b16 a[254:255], v227 offset:416\n"
"v_fma_f32 v76, v76, s92, v158\n"
"v_mfma_f32_16x16x32_bf16 v[168:171], a[224:227], v[124:127], v[168:171]\n"
"ds_read_b64_tr_b16 a[248:249], v245 offset:416\n"
"v_add_f32 v164, v164, v38\n"
"v_mfma_f32_16x16x32_bf16 v[168:171], a[228:231], v[180:183], v[168:171]\n"
"ds_read_b64_tr_b16 a[250:251], v247 offset:416\n"
"v_add_f32 v164, v164, v39\n"
"v_mfma_f32_16x16x32_bf16 v[176:179], a[228:231], v[188:191], v[176:179]\n"
"v_exp_f32 v95, v95\n"
"v_mfma_f32_16x16x32_bf16 v[176:179], a[224:227], v[132:135], v[176:179]\n"
"v_exp_f32 v60, v60\n"
"v_mfma_f32_16x16x32_bf16 v[184:187], a[224:227], v[140:143], v[184:187]\n"
"v_exp_f32 v61, v61\n"
"v_mfma_f32_16x16x32_bf16 v[184:187], a[228:231], v[196:199], v[184:187]\n"
"v_exp_f32 v62, v62\n"
"v_mfma_f32_16x16x32_bf16 v[192:195], a[228:231], v[148:151], v[192:195]\n"
"s_waitcnt vmcnt(13) lgkmcnt(0)\n"
"s_barrier\n"
"v_mfma_f32_16x16x32_bf16 v[192:195], a[224:227], v[172:175], v[192:195]\n"
"ds_read_b128 a[228:231], v241 offset:33216\n"
"v_fma_f32 v77, v77, s92, v158\n"
"v_mfma_f32_16x16x32_bf16 a[196:199], a[232:235], v[172:175], a[196:199]\n"
"ds_read_b128 a[224:227], v240 offset:33216\n"
"v_add_f32 v164, v164, v60\n"
"v_mfma_f32_16x16x32_bf16 a[196:199], a[236:239], v[148:151], a[196:199]\n"
"v_exp_f32 v63, v63\n"
"v_mfma_f32_16x16x32_bf16 a[188:191], a[236:239], v[196:199], a[188:191]\n"
"v_exp_f32 v68, v68\n"
"v_mfma_f32_16x16x32_bf16 a[188:191], a[232:235], v[140:143], a[188:191]\n"
"v_exp_f32 v69, v69\n"
"v_mfma_f32_16x16x32_bf16 a[180:183], a[232:235], v[132:135], a[180:183]\n"
"buffer_load_dwordx4 v248, s[44:47], s81 offen offset:1024 nt lds\n"
"v_mfma_f32_16x16x32_bf16 a[180:183], a[236:239], v[188:191], a[180:183]\n"
"s_addk_i32 m0, 0x20\n"
"v_mfma_f32_16x16x32_bf16 a[172:175], a[236:239], v[180:183], a[172:175]\n"
"ds_read_b128 a[236:239], v241 offset:33344\n"
"v_add_f32 v165, v165, v44\n"
"v_mfma_f32_16x16x32_bf16 a[172:175], a[232:235], v[124:127], a[172:175]\n"
"ds_read_b128 a[232:235], v240 offset:33344\n"
"v_add_f32 v164, v164, v61\n"
"v_mfma_f32_16x16x32_bf16 v[200:203], a[240:243], v[124:127], v[200:203]\n"
"v_exp_f32 v70, v70\n"
"v_mfma_f32_16x16x32_bf16 v[200:203], a[244:247], v[180:183], v[200:203]\n"
"v_exp_f32 v71, v71\n"
"v_mfma_f32_16x16x32_bf16 v[208:211], a[244:247], v[188:191], v[208:211]\n"
"v_exp_f32 v76, v76\n"
"v_mfma_f32_16x16x32_bf16 v[208:211], a[240:243], v[132:135], v[208:211]\n"
"v_add_f32 v165, v165, v45\n"
"v_add_f32 v164, v164, v62\n"
"v_mfma_f32_16x16x32_bf16 v[216:219], a[240:243], v[140:143], v[216:219]\n"
"ds_read_b128 a[240:243], v240 offset:33472\n"
"v_add_f32 v165, v165, v46\n"
"v_mfma_f32_16x16x32_bf16 v[216:219], a[244:247], v[196:199], v[216:219]\n"
"ds_read_b128 a[244:247], v241 offset:33472\n"
"v_cvt_pk_bf16_f32 v36, v36, v37\n"
"v_mfma_f32_16x16x32_bf16 v[236:239], a[252:255], v[196:199], v[236:239]\n"
"v_exp_f32 v77, v77\n"
"v_mfma_f32_16x16x32_bf16 v[236:239], a[248:251], v[140:143], v[236:239]\n"
"v_cvt_pk_bf16_f32 v37, v38, v39\n"
"v_add_f32 v164, v164, v63\n"
"v_mfma_f32_16x16x32_bf16 v[232:235], a[248:251], v[132:135], v[232:235]\n"
"v_cvt_pk_bf16_f32 v38, v60, v61\n"
"v_cvt_pk_bf16_f32 v39, v62, v63\n"
"v_mfma_f32_16x16x32_bf16 v[232:235], a[252:255], v[188:191], v[232:235]\n"
"v_add_f32 v165, v165, v47\n"
"v_add_f32 v166, v166, v52\n"
"v_mfma_f32_16x16x32_bf16 v[228:231], a[252:255], v[180:183], v[228:231]\n"
"ds_read_b128 a[252:255], v241 offset:33600\n"
"v_add_f32 v165, v165, v68\n"
"v_mfma_f32_16x16x32_bf16 v[228:231], a[248:251], v[124:127], v[228:231]\n"
"s_waitcnt lgkmcnt(3)\n"
"ds_read_b128 a[248:251], v240 offset:33600\n"
"v_add_u32 v241, 0xffff3d60, v241\n"
"v_mfma_f32_16x16x32_bf16 v[124:127], a[224:227], a[0:3], 0\n"
"buffer_load_dwordx4 v248, s[44:47], s81 offen offset:2048 nt lds\n"
"v_mfma_f32_16x16x32_bf16 v[124:127], a[228:231], a[32:35], v[124:127]\n"
"v_mfma_f32_16x16x32_bf16 v[132:135], a[228:231], a[40:43], 0\n"
"v_add_u32 v240, 0xffff3d60, v240\n"
"v_add_f32 v165, v165, v69\n"
"v_mfma_f32_16x16x32_bf16 v[132:135], a[224:227], a[8:11], v[132:135]\n"
"v_add_f32 v166, v166, v53\n"
"v_fma_f32 v78, v78, s92, v158\n"
"v_mfma_f32_16x16x32_bf16 v[140:143], a[224:227], a[16:19], 0\n"
"v_add_f32 v165, v165, v70\n"
"v_add_f32 v166, v166, v54\n"
"v_mfma_f32_16x16x32_bf16 v[140:143], a[228:231], a[48:51], v[140:143]\n"
"v_add_f32 v167, v167, v28\n"
"v_cvt_pk_bf16_f32 v44, v44, v45\n"
"v_mfma_f32_16x16x32_bf16 v[172:175], a[228:231], a[4:7], 0\n"
"ds_read_b128 a[228:231], v243 offset:33216\n"
"v_cvt_pk_bf16_f32 v45, v46, v47\n"
"v_mfma_f32_16x16x32_bf16 v[172:175], a[224:227], a[24:27], v[172:175]\n"
"ds_read_b128 a[224:227], v242 offset:33216\n"
"v_add_f32 v165, v165, v71\n"
"v_mfma_f32_16x16x32_bf16 v[124:127], a[232:235], a[56:59], v[124:127]\n"
"v_cvt_pk_bf16_f32 v46, v68, v69\n"
"v_cvt_pk_bf16_f32 v47, v70, v71\n"
"v_mfma_f32_16x16x32_bf16 v[124:127], a[236:239], a[88:91], v[124:127]\n"
"v_add_f32 v166, v166, v55\n"
"v_fma_f32 v79, v79, s92, v158\n"
"v_mfma_f32_16x16x32_bf16 v[132:135], a[236:239], a[96:99], v[132:135]\n"
"v_add_f32 v167, v167, v29\n"
"v_add_f32 v166, v166, v76\n"
"v_mfma_f32_16x16x32_bf16 v[132:135], a[232:235], a[64:67], v[132:135]\n"
"PG9_%=:\n"
"s_cbranch_execz PGB2_%=\n"
"v_exp_f32 v78, v78\n"
"v_mfma_f32_16x16x32_bf16 v[140:143], a[232:235], a[72:75], v[140:143]\n"
"v_add_f32 v167, v167, v30\n"
"v_add_f32 v166, v166, v77\n"
"v_mfma_f32_16x16x32_bf16 v[140:143], a[236:239], a[104:107], v[140:143]\n"
"v_exp_f32 v79, v79\n"
"v_mfma_f32_16x16x32_bf16 v[172:175], a[236:239], a[12:15], v[172:175]\n"
"ds_read_b128 a[236:239], v243 offset:33344\n"
"v_add_f32 v167, v167, v31\n"
"v_mfma_f32_16x16x32_bf16 v[172:175], a[232:235], a[80:83], v[172:175]\n"
"s_waitcnt lgkmcnt(3)\n"
"ds_read_b128 a[232:235], v242 offset:33344\n"
"v_cvt_pk_bf16_f32 v28, v28, v29\n"
"v_mfma_f32_16x16x32_bf16 v[124:127], a[240:243], a[112:115], v[124:127]\n"
"v_cvt_pk_bf16_f32 v29, v30, v31\n"
"v_add_f32 v166, v166, v78\n"
"v_mfma_f32_16x16x32_bf16 v[124:127], a[244:247], a[144:147], v[124:127]\n"
"buffer_load_dwordx4 v248, s[44:47], s81 offen offset:3072 nt lds\n"
"v_mfma_f32_16x16x32_bf16 v[132:135], a[244:247], a[152:155], v[132:135]\n"
"s_add_u32 m0, s79, 0x18540\n"
"s_add_u32 s81, s10, s24\n"
"ds_write_b64 v253, v[28:29] offset:0\n"
"v_mfma_f32_16x16x32_bf16 v[132:135], a[240:243], a[120:123], v[132:135]\n"
"s_addk_i32 s10, 0x1000\n"
"v_add_f32 v167, v167, v92\n"
"v_cvt_pk_bf16_f32 v52, v52, v53\n"
"v_mfma_f32_16x16x32_bf16 v[140:143], a[240:243], a[128:131], v[140:143]\n"
"s_bitset0_b32 s10, 15\n"
"v_cvt_pk_bf16_f32 v53, v54, v55\n"
"v_add_f32 v166, v166, v79\n"
"v_mfma_f32_16x16x32_bf16 v[140:143], a[244:247], a[160:163], v[140:143]\n"
"v_cvt_pk_bf16_f32 v54, v76, v77\n"
"v_cvt_pk_bf16_f32 v55, v78, v79\n"
"v_mfma_f32_16x16x32_bf16 v[172:175], a[244:247], a[20:23], v[172:175]\n"
"ds_read_b128 a[244:247], v243 offset:33472\n"
"v_add_f32 v167, v167, v93\n"
"v_mfma_f32_16x16x32_bf16 v[172:175], a[240:243], a[136:139], v[172:175]\n"
"ds_read_b128 a[240:243], v242 offset:33472\n"
"v_cvt_pk_bf16_f32 v92, v92, v93\n"
"v_mfma_f32_16x16x32_bf16 v[124:127], a[248:251], a[168:171], v[124:127]\n"
"v_cvt_pk_bf16_f32 v93, v94, v95\n"
"v_add_u32 v244, 0xffffbf20, v244\n"
"v_mfma_f32_16x16x32_bf16 v[124:127], a[252:255], a[200:203], v[124:127]\n"
"ds_write_b64 v253, v[92:93] offset:512\n"
"v_add_u32 v246, 0xffffbf20, v246\n"
"v_mfma_f32_16x16x32_bf16 v[132:135], a[252:255], a[208:211], v[132:135]\n"
"v_add_f32 v167, v167, v94\n"
"v_add_u32 v245, 0xffffbf20, v245\n"
"v_mfma_f32_16x16x32_bf16 v[132:135], a[248:251], a[176:179], v[132:135]\n"
"v_add_f32 v167, v167, v95\n"
"v_add_u32 v247, 0xffffbf20, v247\n"
"v_mfma_f32_16x16x32_bf16 v[140:143], a[248:251], a[184:187], v[140:143]\n"
"v_fma_f32 v124, v124, s92, v156\n"
"v_fma_f32 v125, v125, s92, v156\n"
"v_mfma_f32_16x16x32_bf16 v[140:143], a[252:255], a[216:219], v[140:143]\n"
"v_exp_f32 v124, v124\n"
"v_mfma_f32_16x16x32_bf16 v[172:175], a[252:255], a[28:31], v[172:175]\n"
"ds_read_b128 a[252:255], v243 offset:33600\n"
"v_fma_f32 v126, v126, s92, v156\n"
"v_mfma_f32_16x16x32_bf16 v[172:175], a[248:251], a[192:195], v[172:175]\n"
"s_waitcnt lgkmcnt(4)\n"
"ds_read_b128 a[248:251], v242 offset:33600\n"
"v_add_u32 v243, 0xffff3d60, v243\n"
"v_mfma_f32_16x16x32_bf16 v[100:103], a[224:227], a[0:3], 0\n"
"v_add_u32 v242, 0xffff3d60, v242\n"
"v_fma_f32 v127, v127, s92, v156\n"
"v_mfma_f32_16x16x32_bf16 v[100:103], a[228:231], a[32:35], v[100:103]\n"
"v_exp_f32 v125, v125\n"
"v_mfma_f32_16x16x32_bf16 v[108:111], a[228:231], a[40:43], 0\n"
"buffer_load_dwordx4 v248, s[48:51], s80 offen offset:0 nt lds\n"
"v_mfma_f32_16x16x32_bf16 v[108:111], a[224:227], a[8:11], v[108:111]\n"
"v_mfma_f32_16x16x32_bf16 v[116:119], a[224:227], a[16:19], 0\n"
"ds_read_b128 a[224:227], v240 offset:0\n"
"v_fma_f32 v132, v132, s92, v157\n"
"v_mfma_f32_16x16x32_bf16 v[116:119], a[228:231], a[48:51], v[116:119]\n"
"ds_read_b128 a[228:231], v241 offset:0\n"
"v_fma_f32 v133, v133, s92, v157\n"
"v_mfma_f32_16x16x32_bf16 v[100:103], a[232:235], a[56:59], v[100:103]\n"
"v_exp_f32 v126, v126\n"
"v_mfma_f32_16x16x32_bf16 v[100:103], a[236:239], a[88:91], v[100:103]\n"
"v_exp_f32 v127, v127\n"
"v_mfma_f32_16x16x32_bf16 v[108:111], a[236:239], a[96:99], v[108:111]\n"
"v_exp_f32 v132, v132\n"
"v_mfma_f32_16x16x32_bf16 v[108:111], a[232:235], a[64:67], v[108:111]\n"
"v_exp_f32 v133, v133\n"
"v_mfma_f32_16x16x32_bf16 v[116:119], a[232:235], a[72:75], v[116:119]\n"
"ds_read_b128 a[232:235], v240 offset:128\n"
"v_fma_f32 v134, v134, s92, v157\n"
"v_mfma_f32_16x16x32_bf16 v[116:119], a[236:239], a[104:107], v[116:119]\n"
"s_waitcnt lgkmcnt(3)\n"
"ds_read_b128 a[236:239], v241 offset:128\n"
"v_fma_f32 v135, v135, s92, v157\n"
"v_mfma_f32_16x16x32_bf16 v[100:103], a[240:243], a[112:115], v[100:103]\n"
"v_exp_f32 v134, v134\n"
"v_mfma_f32_16x16x32_bf16 v[100:103], a[244:247], a[144:147], v[100:103]\n"
"v_exp_f32 v135, v135\n"
"v_mfma_f32_16x16x32_bf16 v[108:111], a[244:247], a[152:155], v[108:111]\n"
"v_fma_f32 v140, v140, s92, v158\n"
"v_fma_f32 v141, v141, s92, v158\n"
"v_mfma_f32_16x16x32_bf16 v[108:111], a[240:243], a[120:123], v[108:111]\n"
"v_exp_f32 v140, v140\n"
"v_mfma_f32_16x16x32_bf16 v[116:119], a[240:243], a[128:131], v[116:119]\n"
"ds_read_b128 a[240:243], v240 offset:256\n"
"v_fma_f32 v142, v142, s92, v158\n"
"v_mfma_f32_16x16x32_bf16 v[116:119], a[244:247], a[160:163], v[116:119]\n"
"ds_read_b128 a[244:247], v241 offset:256\n"
"v_fma_f32 v143, v143, s92, v158\n"
"v_mfma_f32_16x16x32_bf16 v[100:103], a[248:251], a[168:171], v[100:103]\n"
"v_exp_f32 v141, v141\n"
"v_mfma_f32_16x16x32_bf16 v[100:103], a[252:255], a[200:203], v[100:103]\n"
"buffer_load_dwordx4 v248, s[48:51], s80 offen offset:1024 nt lds\n"
"v_mfma_f32_16x16x32_bf16 v[108:111], a[252:255], a[208:211], v[108:111]\n"
"s_addk_i32 m0, 0x20\n"
"v_mfma_f32_16x16x32_bf16 v[108:111], a[248:251], a[176:179], v[108:111]\n"
"v_exp_f32 v142, v142\n"
"v_mfma_f32_16x16x32_bf16 v[116:119], a[248:251], a[184:187], v[116:119]\n"
"ds_read_b128 a[248:251], v240 offset:384\n"
"v_add_f32 v164, v164, v124\n"
"v_mfma_f32_16x16x32_bf16 v[116:119], a[252:255], a[216:219], v[116:119]\n"
"s_waitcnt lgkmcnt(3)\n"
"ds_read_b128 a[252:255], v241 offset:384\n"
"v_add_u32 v240, 0x10380, v240\n"
"v_mfma_f32_16x16x32_bf16 v[180:183], a[224:227], a[0:3], 0\n"
"v_add_u32 v241, 0x10380, v241\n"
"v_fma_f32 v100, v100, s92, v156\n"
"v_mfma_f32_16x16x32_bf16 v[180:183], a[228:231], a[32:35], v[180:183]\n"
"v_exp_f32 v143, v143\n"
"v_mfma_f32_16x16x32_bf16 v[188:191], a[228:231], a[40:43], 0\n"
"v_exp_f32 v100, v100\n"
"v_mfma_f32_16x16x32_bf16 v[188:191], a[224:227], a[8:11], v[188:191]\n"
"v_fma_f32 v101, v101, s92, v156\n"
"v_fma_f32 v102, v102, s92, v156\n"
"v_mfma_f32_16x16x32_bf16 v[196:199], a[224:227], a[16:19], 0\n"
"v_exp_f32 v101, v101\n"
"v_mfma_f32_16x16x32_bf16 v[196:199], a[228:231], a[48:51], v[196:199]\n"
"v_exp_f32 v102, v102\n"
"v_mfma_f32_16x16x32_bf16 v[148:151], a[228:231], a[4:7], 0\n"
"ds_read_b128 a[228:231], v243 offset:0\n"
"v_fma_f32 v103, v103, s92, v156\n"
"v_mfma_f32_16x16x32_bf16 v[148:151], a[224:227], a[24:27], v[148:151]\n"
"ds_read_b128 a[224:227], v242 offset:0\n"
"v_fma_f32 v108, v108, s92, v157\n"
"v_mfma_f32_16x16x32_bf16 v[180:183], a[232:235], a[56:59], v[180:183]\n"
"v_exp_f32 v103, v103\n"
"v_mfma_f32_16x16x32_bf16 v[180:183], a[236:239], a[88:91], v[180:183]\n"
"v_exp_f32 v108, v108\n"
"v_mfma_f32_16x16x32_bf16 v[188:191], a[236:239], a[96:99], v[188:191]\n"
"v_fma_f32 v109, v109, s92, v157\n"
"v_fma_f32 v110, v110, s92, v157\n"
"v_mfma_f32_16x16x32_bf16 v[188:191], a[232:235], a[64:67], v[188:191]\n"
"v_exp_f32 v109, v109\n"
"v_mfma_f32_16x16x32_bf16 v[196:199], a[232:235], a[72:75], v[196:199]\n"
"buffer_load_dwordx4 v248, s[48:51], s80 offen offset:2048 nt lds\n"
"v_mfma_f32_16x16x32_bf16 v[196:199], a[236:239], a[104:107], v[196:199]\n"
"v_mfma_f32_16x16x32_bf16 v[148:151], a[236:239], a[12:15], v[148:151]\n"
"ds_read_b128 a[236:239], v243 offset:128\n"
"v_fma_f32 v111, v111, s92, v157\n"
"v_mfma_f32_16x16x32_bf16 v[148:151], a[232:235], a[80:83], v[148:151]\n"
"s_waitcnt lgkmcnt(3)\n"
"ds_read_b128 a[232:235], v242 offset:128\n"
"v_add_f32 v164, v164, v125\n"
"v_mfma_f32_16x16x32_bf16 v[180:183], a[240:243], a[112:115], v[180:183]\n"
"v_exp_f32 v110, v110\n"
"v_mfma_f32_16x16x32_bf16 v[180:183], a[244:247], a[144:147], v[180:183]\n"
"v_exp_f32 v111, v111\n"
"v_mfma_f32_16x16x32_bf16 v[188:191], a[244:247], a[152:155], v[188:191]\n"
"v_fma_f32 v116, v116, s92, v158\n"
"v_add_f32 v164, v164, v126\n"
"v_mfma_f32_16x16x32_bf16 v[188:191], a[240:243], a[120:123], v[188:191]\n"
"v_add_f32 v164, v164, v127\n"
"v_fma_f32 v117, v117, s92, v158\n"
"v_mfma_f32_16x16x32_bf16 v[196:199], a[240:243], a[128:131], v[196:199]\n"
"v_exp_f32 v116, v116\n"
"v_mfma_f32_16x16x32_bf16 v[196:199], a[244:247], a[160:163], v[196:199]\n"
"v_add_f32 v164, v164, v100\n"
"v_add_f32 v165, v165, v132\n"
"v_mfma_f32_16x16x32_bf16 v[148:151], a[244:247], a[20:23], v[148:151]\n"
"ds_read_b128 a[244:247], v243 offset:256\n"
"v_add_f32 v164, v164, v101\n"
"v_mfma_f32_16x16x32_bf16 v[148:151], a[240:243], a[136:139], v[148:151]\n"
"ds_read_b128 a[240:243], v242 offset:256\n"
"v_add_f32 v165, v165, v133\n"
"v_mfma_f32_16x16x32_bf16 v[180:183], a[248:251], a[168:171], v[180:183]\n"
"v_exp_f32 v117, v117\n"
"v_mfma_f32_16x16x32_bf16 v[180:183], a[252:255], a[200:203], v[180:183]\n"
"v_add_f32 v164, v164, v102\n"
"v_add_f32 v165, v165, v134\n"
"v_mfma_f32_16x16x32_bf16 v[188:191], a[252:255], a[208:211], v[188:191]\n"
"v_cvt_pk_bf16_f32 v124, v124, v125\n"
"v_add_f32 v164, v164, v103\n"
"v_mfma_f32_16x16x32_bf16 v[188:191], a[248:251], a[176:179], v[188:191]\n"
"v_cvt_pk_bf16_f32 v125, v126, v127\n"
"v_add_f32 v165, v165, v135\n"
"v_mfma_f32_16x16x32_bf16 v[196:199], a[248:251], a[184:187], v[196:199]\n"
"v_cvt_pk_bf16_f32 v126, v100, v101\n"
"v_cvt_pk_bf16_f32 v127, v102, v103\n"
"v_mfma_f32_16x16x32_bf16 v[196:199], a[252:255], a[216:219], v[196:199]\n"
"buffer_load_dwordx4 v248, s[48:51], s80 offen offset:3072 nt lds\n"
"v_mfma_f32_16x16x32_bf16 v[148:151], a[252:255], a[28:31], v[148:151]\n"
"s_add_u32 m0, s79, 0x40e0\n"
"s_add_u32 s80, s9, s24\n"
"ds_read_b128 a[252:255], v243 offset:384\n"
"v_mfma_f32_16x16x32_bf16 v[148:151], a[248:251], a[192:195], v[148:151]\n"
"s_waitcnt lgkmcnt(3)\n"
"s_addk_i32 s9, 0x1000\n"
"ds_read_b128 a[248:251], v242 offset:384\n"
"v_mfma_f32_16x16x32_bf16 v[60:63], a[224:227], a[0:3], 0\n"
"s_bitset0_b32 s9, 15\n"
"v_add_u32 v242, 0x10380, v242\n"
"v_add_u32 v243, 0x10380, v243\n"
"v_mfma_f32_16x16x32_bf16 v[60:63], a[228:231], a[32:35], v[60:63]\n"
"v_add_f32 v165, v165, v108\n"
"v_add_f32 v166, v166, v140\n"
"v_mfma_f32_16x16x32_bf16 v[68:71], a[228:231], a[40:43], 0\n"
"v_add_f32 v165, v165, v109\n"
"v_add_f32 v166, v166, v141\n"
"v_mfma_f32_16x16x32_bf16 v[68:71], a[224:227], a[8:11], v[68:71]\n"
"v_fma_f32 v118, v118, s92, v158\n"
"v_add_f32 v165, v165, v110\n"
"v_mfma_f32_16x16x32_bf16 v[76:79], a[224:227], a[16:19], 0\n"
"s_waitcnt vmcnt(12) lgkmcnt(0)\n"
"s_barrier\n"
"v_mfma_f32_16x16x32_bf16 v[76:79], a[228:231], a[48:51], v[76:79]\n"
"ds_read_b64_tr_b16 a[224:225], v244 offset:0\n"
"v_add_f32 v166, v166, v142\n"
"v_mfma_f32_16x16x32_bf16 v[60:63], a[232:235], a[56:59], v[60:63]\n"
"ds_read_b64_tr_b16 a[226:227], v246 offset:0\n"
"v_cvt_pk_bf16_f32 v132, v132, v133\n"
"v_mfma_f32_16x16x32_bf16 v[60:63], a[236:239], a[88:91], v[60:63]\n"
"ds_read_b64_tr_b16 a[228:229], v224 offset:16608\n"
"v_cvt_pk_bf16_f32 v133, v134, v135\n"
"v_mfma_f32_16x16x32_bf16 v[68:71], a[236:239], a[96:99], v[68:71]\n"
"ds_read_b64_tr_b16 a[230:231], v226 offset:16608\n"
"v_add_f32 v165, v165, v111\n"
"v_mfma_f32_16x16x32_bf16 v[68:71], a[232:235], a[64:67], v[68:71]\n"
"ds_read_b64 v[30:31], v254 offset:0\n"
"v_cvt_pk_bf16_f32 v134, v108, v109\n"
"v_mfma_f32_16x16x32_bf16 v[76:79], a[232:235], a[72:75], v[76:79]\n"
"ds_read_b64 v[94:95], v254 offset:512\n"
"v_cvt_pk_bf16_f32 v135, v110, v111\n"
"v_mfma_f32_16x16x32_bf16 v[76:79], a[236:239], a[104:107], v[76:79]\n"
"ds_read_b64_tr_b16 a[232:233], v244 offset:32\n"
"v_add_f32 v166, v166, v143\n"
"v_mfma_f32_16x16x32_bf16 v[60:63], a[240:243], a[112:115], v[60:63]\n"
"ds_read_b64_tr_b16 a[234:235], v246 offset:32\n"
"v_fma_f32 v119, v119, s92, v158\n"
"v_mfma_f32_16x16x32_bf16 v[60:63], a[244:247], a[144:147], v[60:63]\n"
"ds_read_b64_tr_b16 a[236:237], v224 offset:16640\n"
"v_add_f32 v166, v166, v116\n"
"v_mfma_f32_16x16x32_bf16 v[68:71], a[244:247], a[152:155], v[68:71]\n"
"buffer_load_dwordx4 v248, s[48:51], s81 offen offset:0 nt lds\n"
"v_mfma_f32_16x16x32_bf16 v[68:71], a[240:243], a[120:123], v[68:71]\n"
"ds_read_b64_tr_b16 a[238:239], v226 offset:16640\n"
"v_mfma_f32_16x16x32_bf16 v[76:79], a[240:243], a[128:131], v[76:79]\n"
"ds_read_b64_tr_b16 a[240:241], v245 offset:0\n"
"v_add_f32 v166, v166, v117\n"
"v_mfma_f32_16x16x32_bf16 v[76:79], a[244:247], a[160:163], v[76:79]\n"
"ds_read_b64_tr_b16 a[242:243], v247 offset:0\n"
"v_cvt_pk_bf16_f32 v140, v140, v141\n"
"v_mfma_f32_16x16x32_bf16 v[60:63], a[248:251], a[168:171], v[60:63]\n"
"ds_read_b64_tr_b16 a[244:245], v225 offset:16608\n"
"v_cvt_pk_bf16_f32 v141, v142, v143\n"
"v_mfma_f32_16x16x32_bf16 v[60:63], a[252:255], a[200:203], v[60:63]\n"
"ds_read_b64_tr_b16 a[246:247], v227 offset:16608\n"
"v_cvt_pk_bf16_f32 v142, v116, v117\n"
"v_mfma_f32_16x16x32_bf16 v[68:71], a[252:255], a[208:211], v[68:71]\n"
"v_exp_f32 v118, v118\n"
"v_mfma_f32_16x16x32_bf16 v[68:71], a[248:251], a[176:179], v[68:71]\n"
"v_exp_f32 v119, v119\n"
"v_mfma_f32_16x16x32_bf16 v[76:79], a[248:251], a[184:187], v[76:79]\n"
"ds_read_b64_tr_b16 a[248:249], v245 offset:32\n"
"v_add_f32 v166, v166, v118\n"
"v_mfma_f32_16x16x32_bf16 v[76:79], a[252:255], a[216:219], v[76:79]\n"
"s_waitcnt lgkmcnt(8)\n"
"ds_read_b64_tr_b16 a[252:253], v225 offset:16640\n"
"v_add_f32 v166, v166, v119\n"
"v_mfma_f32_16x16x32_bf16 v[0:3], a[224:227], v[4:7], v[0:3]\n"
"ds_read_b64_tr_b16 a[254:255], v227 offset:16640\n"
"v_cvt_pk_bf16_f32 v143, v118, v119\n"
"v_mfma_f32_16x16x32_bf16 v[0:3], a[228:231], v[36:39], v[0:3]\n"
"ds_read_b64_tr_b16 a[250:251], v247 offset:32\n"
"v_fma_f32 v172, v172, s92, v159\n"
"v_mfma_f32_16x16x32_bf16 v[8:11], a[228:231], v[44:47], v[8:11]\n"
"v_exp_f32 v172, v172\n"
"v_mfma_f32_16x16x32_bf16 v[8:11], a[224:227], v[12:15], v[8:11]\n"
"v_fma_f32 v173, v173, s92, v159\n"
"v_fma_f32 v174, v174, s92, v159\n"
"v_mfma_f32_16x16x32_bf16 v[16:19], a[224:227], v[20:23], v[16:19]\n"
"v_exp_f32 v173, v173\n"
"v_mfma_f32_16x16x32_bf16 v[16:19], a[228:231], v[52:55], v[16:19]\n"
"v_exp_f32 v174, v174\n"
"v_mfma_f32_16x16x32_bf16 v[24:27], a[228:231], v[92:95], v[24:27]\n"
"ds_read_b64_tr_b16 a[228:229], v224 offset:16736\n"
"v_fma_f32 v175, v175, s92, v159\n"
"v_mfma_f32_16x16x32_bf16 v[24:27], a[224:227], v[28:31], v[24:27]\n"
"s_waitcnt lgkmcnt(5)\n"
"buffer_load_dwordx4 v248, s[48:51], s81 offen offset:1024 nt lds\n"
"v_mfma_f32_16x16x32_bf16 v[220:223], a[232:235], v[28:31], v[220:223]\n"
"s_addk_i32 m0, 0x20\n"
"ds_read_b64_tr_b16 a[224:225], v244 offset:128\n"
"v_mfma_f32_16x16x32_bf16 v[220:223], a[236:239], v[92:95], v[220:223]\n"
"ds_read_b64_tr_b16 a[226:227], v246 offset:128\n"
"v_add_f32 v167, v167, v172\n"
"v_mfma_f32_16x16x32_bf16 v[212:215], a[236:239], v[52:55], v[212:215]\n"
"ds_read_b64_tr_b16 a[230:231], v226 offset:16736\n"
"v_add_f32 v167, v167, v173\n"
"v_mfma_f32_16x16x32_bf16 v[212:215], a[232:235], v[20:23], v[212:215]\n"
"v_exp_f32 v175, v175\n"
"v_mfma_f32_16x16x32_bf16 v[204:207], a[232:235], v[12:15], v[204:207]\n"
"v_cvt_pk_bf16_f32 v172, v172, v173\n"
"v_fma_f32 v180, v180, s92, v156\n"
"v_mfma_f32_16x16x32_bf16 v[204:207], a[236:239], v[44:47], v[204:207]\n"
"v_cvt_pk_bf16_f32 v173, v174, v175\n"
"v_fma_f32 v181, v181, s92, v156\n"
"v_mfma_f32_16x16x32_bf16 v[84:87], a[236:239], v[36:39], v[84:87]\n"
"ds_read_b64_tr_b16 a[236:237], v224 offset:16768\n"
"v_fma_f32 v182, v182, s92, v156\n"
"v_mfma_f32_16x16x32_bf16 v[84:87], a[232:235], v[4:7], v[84:87]\n"
"ds_read_b64_tr_b16 a[232:233], v244 offset:160\n"
"v_fma_f32 v183, v183, s92, v156\n"
"v_mfma_f32_16x16x32_bf16 v[32:35], a[240:243], v[4:7], v[32:35]\n"
"ds_read_b64_tr_b16 a[234:235], v246 offset:160\n"
"v_fma_f32 v188, v188, s92, v157\n"
"v_mfma_f32_16x16x32_bf16 v[32:35], a[244:247], v[36:39], v[32:35]\n"
"ds_read_b64_tr_b16 a[238:239], v226 offset:16768\n"
"v_fma_f32 v189, v189, s92, v157\n"
"v_mfma_f32_16x16x32_bf16 v[40:43], a[244:247], v[44:47], v[40:43]\n"
"ds_write_b64 v253, v[172:173] offset:1024\n"
"v_fma_f32 v190, v190, s92, v157\n"
"v_mfma_f32_16x16x32_bf16 v[40:43], a[240:243], v[12:15], v[40:43]\n"
"v_exp_f32 v180, v180\n"
"v_mfma_f32_16x16x32_bf16 v[48:51], a[240:243], v[20:23], v[48:51]\n"
"ds_read_b64_tr_b16 a[240:241], v245 offset:128\n"
"v_fma_f32 v191, v191, s92, v157\n"
"v_mfma_f32_16x16x32_bf16 v[48:51], a[244:247], v[52:55], v[48:51]\n"
"s_waitcnt lgkmcnt(7)\n"
"ds_read_b64_tr_b16 a[242:243], v247 offset:128\n"
"v_fma_f32 v196, v196, s92, v158\n"
"v_mfma_f32_16x16x32_bf16 a[52:55], a[252:255], v[52:55], a[52:55]\n"
"ds_read_b64_tr_b16 a[244:245], v225 offset:16736\n"
"v_fma_f32 v197, v197, s92, v158\n"
"v_mfma_f32_16x16x32_bf16 a[52:55], a[248:251], v[20:23], a[52:55]\n"
"ds_read_b64_tr_b16 a[246:247], v227 offset:16736\n"
"v_fma_f32 v198, v198, s92, v158\n"
"v_mfma_f32_16x16x32_bf16 a[44:47], a[248:251], v[12:15], a[44:47]\n"
"buffer_load_dwordx4 v248, s[48:51], s81 offen offset:2048 nt lds\n"
"v_mfma_f32_16x16x32_bf16 a[44:47], a[252:255], v[44:47], a[44:47]\n"
"v_mfma_f32_16x16x32_bf16 a[36:39], a[252:255], v[36:39], a[36:39]\n"
"ds_read_b64_tr_b16 a[252:253], v225 offset:16768\n"
"v_fma_f32 v199, v199, s92, v158\n"
"v_mfma_f32_16x16x32_bf16 a[36:39], a[248:251], v[4:7], a[36:39]\n"
"ds_read_b64_tr_b16 a[254:255], v227 offset:16768\n"
"v_fma_f32 v148, v148, s92, v159\n"
"v_mfma_f32_16x16x32_bf16 v[56:59], a[224:227], v[4:7], v[56:59]\n"
"s_waitcnt lgkmcnt(8)\n"
"ds_read_b64_tr_b16 a[248:249], v245 offset:160\n"
"v_fma_f32 v149, v149, s92, v159\n"
"v_mfma_f32_16x16x32_bf16 v[56:59], a[228:231], v[36:39], v[56:59]\n"
"ds_read_b64_tr_b16 a[250:251], v247 offset:160\n"
"v_fma_f32 v150, v150, s92, v159\n"
"v_mfma_f32_16x16x32_bf16 v[64:67], a[228:231], v[44:47], v[64:67]\n"
"v_exp_f32 v181, v181\n"
"v_mfma_f32_16x16x32_bf16 v[64:67], a[224:227], v[12:15], v[64:67]\n"
"v_exp_f32 v182, v182\n"
"v_mfma_f32_16x16x32_bf16 v[72:75], a[224:227], v[20:23], v[72:75]\n"
"v_exp_f32 v183, v183\n"
"v_mfma_f32_16x16x32_bf16 v[72:75], a[228:231], v[52:55], v[72:75]\n"
"v_exp_f32 v188, v188\n"
"v_mfma_f32_16x16x32_bf16 v[80:83], a[228:231], v[92:95], v[80:83]\n"
"ds_read_b64_tr_b16 a[228:229], v224 offset:16864\n"
"v_fma_f32 v151, v151, s92, v159\n"
"v_mfma_f32_16x16x32_bf16 v[80:83], a[224:227], v[28:31], v[80:83]\n"
"ds_read_b64_tr_b16 a[224:225], v244 offset:256\n"
"v_fma_f32 v60, v60, s92, v156\n"
"v_mfma_f32_16x16x32_bf16 a[84:87], a[232:235], v[28:31], a[84:87]\n"
"s_waitcnt lgkmcnt(6)\n"
"ds_read_b64_tr_b16 a[226:227], v246 offset:256\n"
"v_fma_f32 v61, v61, s92, v156\n"
"v_mfma_f32_16x16x32_bf16 a[84:87], a[236:239], v[92:95], a[84:87]\n"
"ds_read_b64_tr_b16 a[230:231], v226 offset:16864\n"
"v_fma_f32 v62, v62, s92, v156\n"
"v_mfma_f32_16x16x32_bf16 a[76:79], a[236:239], v[52:55], a[76:79]\n"
"v_exp_f32 v189, v189\n"
"v_mfma_f32_16x16x32_bf16 a[76:79], a[232:235], v[20:23], a[76:79]\n"
"v_exp_f32 v190, v190\n"
"v_mfma_f32_16x16x32_bf16 a[68:71], a[232:235], v[12:15], a[68:71]\n"
"v_exp_f32 v191, v191\n"
"v_mfma_f32_16x16x32_bf16 a[68:71], a[236:239], v[44:47], a[68:71]\n"
"buffer_load_dwordx4 v248, s[48:51], s81 offen offset:3072 nt lds\n"
"v_mfma_f32_16x16x32_bf16 a[60:63], a[236:239], v[36:39], a[60:63]\n"
"s_add_u32 m0, s79, 0x20700\n"
"s_add_u32 s81, s9, s24\n"
"ds_read_b64_tr_b16 a[236:237], v224 offset:16896\n"
"v_mfma_f32_16x16x32_bf16 a[60:63], a[232:235], v[4:7], a[60:63]\n"
"ds_read_b64_tr_b16 a[232:233], v244 offset:288\n"
"v_fma_f32 v63, v63, s92, v156\n"
"v_mfma_f32_16x16x32_bf16 v[88:91], a[240:243], v[4:7], v[88:91]\n"
"ds_read_b64_tr_b16 a[234:235], v246 offset:288\n"
"v_fma_f32 v68, v68, s92, v157\n"
"v_mfma_f32_16x16x32_bf16 v[88:91], a[244:247], v[36:39], v[88:91]\n"
"ds_read_b64_tr_b16 a[238:239], v226 offset:16896\n"
"v_fma_f32 v69, v69, s92, v157\n"
"v_mfma_f32_16x16x32_bf16 v[96:99], a[244:247], v[44:47], v[96:99]\n"
"v_exp_f32 v196, v196\n"
"v_mfma_f32_16x16x32_bf16 v[96:99], a[240:243], v[12:15], v[96:99]\n"
"v_exp_f32 v197, v197\n"
"v_mfma_f32_16x16x32_bf16 v[104:107], a[240:243], v[20:23], v[104:107]\n"
"ds_read_b64_tr_b16 a[240:241], v245 offset:256\n"
"v_fma_f32 v70, v70, s92, v157\n"
"v_mfma_f32_16x16x32_bf16 v[104:107], a[244:247], v[52:55], v[104:107]\n"
"s_waitcnt lgkmcnt(5)\n"
"ds_read_b64_tr_b16 a[242:243], v247 offset:256\n"
"v_fma_f32 v71, v71, s92, v157\n"
"v_mfma_f32_16x16x32_bf16 a[108:111], a[252:255], v[52:55], a[108:111]\n"
"ds_read_b64_tr_b16 a[244:245], v225 offset:16864\n"
"v_add_f32 v164, v164, v180\n"
"v_mfma_f32_16x16x32_bf16 a[108:111], a[248:251], v[20:23], a[108:111]\n"
"ds_read_b64_tr_b16 a[246:247], v227 offset:16864\n"
"v_add_f32 v164, v164, v181\n"
"v_mfma_f32_16x16x32_bf16 a[100:103], a[248:251], v[12:15], a[100:103]\n"
"v_exp_f32 v198, v198\n"
"v_mfma_f32_16x16x32_bf16 a[100:103], a[252:255], v[44:47], a[100:103]\n"
"v_exp_f32 v199, v199\n"
"v_mfma_f32_16x16x32_bf16 a[92:95], a[252:255], v[36:39], a[92:95]\n"
"ds_read_b64_tr_b16 a[252:253], v225 offset:16896\n"
"v_fma_f32 v76, v76, s92, v158\n"
"v_mfma_f32_16x16x32_bf16 a[92:95], a[248:251], v[4:7], a[92:95]\n"
"ds_read_b64_tr_b16 a[254:255], v227 offset:16896\n"
"v_add_f32 v164, v164, v182\n"
"v_mfma_f32_16x16x32_bf16 v[112:115], a[224:227], v[4:7], v[112:115]\n"
"ds_read_b64_tr_b16 a[248:249], v245 offset:288\n"
"v_add_f32 v164, v164, v183\n"
"v_mfma_f32_16x16x32_bf16 v[112:115], a[228:231], v[36:39], v[112:115]\n"
"ds_read_b64_tr_b16 a[250:251], v247 offset:288\n"
"v_fma_f32 v77, v77, s92, v158\n"
"v_mfma_f32_16x16x32_bf16 v[120:123], a[228:231], v[44:47], v[120:123]\n"
"buffer_load_dwordx4 v248, s[44:47], s80 offen offset:0 nt lds\n"
"v_mfma_f32_16x16x32_bf16 v[120:123], a[224:227], v[12:15], v[120:123]\n"
"v_mfma_f32_16x16x32_bf16 v[128:131], a[224:227], v[20:23], v[128:131]\n"
"v_exp_f32 v148, v148\n"
"v_mfma_f32_16x16x32_bf16 v[128:131], a[228:231], v[52:55], v[128:131]\n"
"v_exp_f32 v149, v149\n"
"v_mfma_f32_16x16x32_bf16 v[136:139], a[228:231], v[92:95], v[136:139]\n"
"ds_read_b64_tr_b16 a[228:229], v224 offset:16992\n"
"v_add_f32 v165, v165, v188\n"
"v_mfma_f32_16x16x32_bf16 v[136:139], a[224:227], v[28:31], v[136:139]\n"
"s_waitcnt lgkmcnt(5)\n"
"ds_read_b64_tr_b16 a[224:225], v244 offset:384\n"
"v_add_f32 v165, v165, v189\n"
"v_mfma_f32_16x16x32_bf16 a[140:143], a[232:235], v[28:31], a[140:143]\n"
"ds_read_b64_tr_b16 a[226:227], v246 offset:384\n"
"v_add_f32 v165, v165, v190\n"
"v_mfma_f32_16x16x32_bf16 a[140:143], a[236:239], v[92:95], a[140:143]\n"
"ds_read_b64_tr_b16 a[230:231], v226 offset:16992\n"
"v_cvt_pk_bf16_f32 v180, v180, v181\n"
"v_mfma_f32_16x16x32_bf16 a[132:135], a[236:239], v[52:55], a[132:135]\n"
"v_exp_f32 v150, v150\n"
"v_mfma_f32_16x16x32_bf16 a[132:135], a[232:235], v[20:23], a[132:135]\n"
"v_exp_f32 v151, v151\n"
"v_mfma_f32_16x16x32_bf16 a[124:127], a[232:235], v[12:15], a[124:127]\n"
"v_exp_f32 v60, v60\n"
"v_mfma_f32_16x16x32_bf16 a[124:127], a[236:239], v[44:47], a[124:127]\n"
"v_exp_f32 v61, v61\n"
"v_mfma_f32_16x16x32_bf16 a[116:119], a[236:239], v[36:39], a[116:119]\n"
"ds_read_b64_tr_b16 a[236:237], v224 offset:17024\n"
"v_add_f32 v164, v164, v60\n"
"v_mfma_f32_16x16x32_bf16 a[116:119], a[232:235], v[4:7], a[116:119]\n"
"ds_read_b64_tr_b16 a[232:233], v244 offset:416\n"
"v_add_f32 v164, v164, v61\n"
"v_mfma_f32_16x16x32_bf16 v[144:147], a[240:243], v[4:7], v[144:147]\n"
"ds_read_b64_tr_b16 a[234:235], v246 offset:416\n"
"v_cvt_pk_bf16_f32 v181, v182, v183\n"
"v_mfma_f32_16x16x32_bf16 v[144:147], a[244:247], v[36:39], v[144:147]\n"
"ds_read_b64_tr_b16 a[238:239], v226 offset:17024\n"
"v_cvt_pk_bf16_f32 v182, v60, v61\n"
"v_mfma_f32_16x16x32_bf16 v[152:155], a[244:247], v[44:47], v[152:155]\n"
"buffer_load_dwordx4 v248, s[44:47], s80 offen offset:1024 nt lds\n"
"v_mfma_f32_16x16x32_bf16 v[152:155], a[240:243], v[12:15], v[152:155]\n"
"s_addk_i32 m0, 0x20\n"
"v_mfma_f32_16x16x32_bf16 v[160:163], a[240:243], v[20:23], v[160:163]\n"
"ds_read_b64_tr_b16 a[240:241], v245 offset:384\n"
"v_add_f32 v165, v165, v191\n"
"v_mfma_f32_16x16x32_bf16 v[160:163], a[244:247], v[52:55], v[160:163]\n"
"s_waitcnt lgkmcnt(5)\n"
"ds_read_b64_tr_b16 a[242:243], v247 offset:384\n"
"v_add_f32 v166, v166, v196\n"
"v_mfma_f32_16x16x32_bf16 a[164:167], a[252:255], v[52:55], a[164:167]\n"
"ds_read_b64_tr_b16 a[244:245], v225 offset:16992\n"
"v_add_f32 v166, v166, v197\n"
"v_mfma_f32_16x16x32_bf16 a[164:167], a[248:251], v[20:23], a[164:167]\n"
"ds_read_b64_tr_b16 a[246:247], v227 offset:16992\n"
"v_fma_f32 v78, v78, s92, v158\n"
"v_mfma_f32_16x16x32_bf16 a[156:159], a[248:251], v[12:15], a[156:159]\n"
"v_exp_f32 v62, v62\n"
"v_mfma_f32_16x16x32_bf16 a[156:159], a[252:255], v[44:47], a[156:159]\n"
"v_exp_f32 v63, v63\n"
"v_mfma_f32_16x16x32_bf16 a[148:151], a[252:255], v[36:39], a[148:151]\n"
"ds_read_b64_tr_b16 a[252:253], v225 offset:17024\n"
"v_add_f32 v164, v164, v62\n"
"v_mfma_f32_16x16x32_bf16 a[148:151], a[248:251], v[4:7], a[148:151]\n"
"ds_read_b64_tr_b16 a[254:255], v227 offset:17024\n"
"v_add_f32 v164, v164, v63\n"
"v_mfma_f32_16x16x32_bf16 v[168:171], a[224:227], v[4:7], v[168:171]\n"
"ds_read_b64_tr_b16 a[248:249], v245 offset:416\n"
"v_cvt_pk_bf16_f32 v183, v62, v63\n"
"v_mfma_f32_16x16x32_bf16 v[168:171], a[228:231], v[36:39], v[168:171]\n"
"ds_read_b64_tr_b16 a[250:251], v247 offset:416\n"
"v_add_f32 v166, v166, v198\n"
"v_mfma_f32_16x16x32_bf16 v[176:179], a[228:231], v[44:47], v[176:179]\n"
"v_exp_f32 v68, v68\n"
"v_mfma_f32_16x16x32_bf16 v[176:179], a[224:227], v[12:15], v[176:179]\n"
"v_exp_f32 v69, v69\n"
"v_mfma_f32_16x16x32_bf16 v[184:187], a[224:227], v[20:23], v[184:187]\n"
"v_exp_f32 v70, v70\n"
"v_mfma_f32_16x16x32_bf16 v[184:187], a[228:231], v[52:55], v[184:187]\n"
"v_exp_f32 v71, v71\n"
"v_mfma_f32_16x16x32_bf16 v[192:195], a[228:231], v[92:95], v[192:195]\n"
"s_waitcnt vmcnt(10) lgkmcnt(0)\n"
"s_barrier\n"
"v_mfma_f32_16x16x32_bf16 v[192:195], a[224:227], v[28:31], v[192:195]\n"
"ds_read_b128 a[224:227], v240 offset:0\n"
"v_add_f32 v165, v165, v68\n"
"v_mfma_f32_16x16x32_bf16 a[196:199], a[232:235], v[28:31], a[196:199]\n"
"ds_read_b128 a[228:231], v241 offset:0\n"
"v_add_f32 v165, v165, v69\n"
"v_mfma_f32_16x16x32_bf16 a[196:199], a[236:239], v[92:95], a[196:199]\n"
"buffer_load_dwordx4 v248, s[44:47], s80 offen offset:2048 nt lds\n"
"v_mfma_f32_16x16x32_bf16 a[188:191], a[236:239], v[52:55], a[188:191]\n"
"v_mfma_f32_16x16x32_bf16 a[188:191], a[232:235], v[20:23], a[188:191]\n"
"v_exp_f32 v76, v76\n"
"v_mfma_f32_16x16x32_bf16 a[180:183], a[232:235], v[12:15], a[180:183]\n"
"v_exp_f32 v77, v77\n"
"v_mfma_f32_16x16x32_bf16 a[180:183], a[236:239], v[44:47], a[180:183]\n"
"v_add_f32 v165, v165, v70\n"
"v_cvt_pk_bf16_f32 v188, v188, v189\n"
"v_mfma_f32_16x16x32_bf16 a[172:175], a[236:239], v[36:39], a[172:175]\n"
"ds_read_b128 a[236:239], v241 offset:128\n"
"v_cvt_pk_bf16_f32 v189, v190, v191\n"
"v_mfma_f32_16x16x32_bf16 a[172:175], a[232:235], v[4:7], a[172:175]\n"
"ds_read_b128 a[232:235], v240 offset:128\n"
"v_add_f32 v165, v165, v71\n"
"v_mfma_f32_16x16x32_bf16 v[200:203], a[240:243], v[4:7], v[200:203]\n"
"v_cvt_pk_bf16_f32 v190, v68, v69\n"
"v_cvt_pk_bf16_f32 v191, v70, v71\n"
"v_mfma_f32_16x16x32_bf16 v[200:203], a[244:247], v[36:39], v[200:203]\n"
"v_add_f32 v166, v166, v199\n"
"v_fma_f32 v79, v79, s92, v158\n"
"v_mfma_f32_16x16x32_bf16 v[208:211], a[244:247], v[44:47], v[208:211]\n"
"v_exp_f32 v78, v78\n"
"v_mfma_f32_16x16x32_bf16 v[208:211], a[240:243], v[12:15], v[208:211]\n"
"v_add_f32 v166, v166, v76\n"
"v_add_f32 v167, v167, v174\n"
"v_mfma_f32_16x16x32_bf16 v[216:219], a[240:243], v[20:23], v[216:219]\n"
"ds_read_b128 a[240:243], v240 offset:256\n"
"v_add_f32 v166, v166, v77\n"
"v_mfma_f32_16x16x32_bf16 v[216:219], a[244:247], v[52:55], v[216:219]\n"
"ds_read_b128 a[244:247], v241 offset:256\n"
"v_add_f32 v167, v167, v175\n"
"v_mfma_f32_16x16x32_bf16 v[236:239], a[252:255], v[52:55], v[236:239]\n"
"v_exp_f32 v79, v79\n"
"v_mfma_f32_16x16x32_bf16 v[236:239], a[248:251], v[20:23], v[236:239]\n"
"v_add_f32 v166, v166, v78\n"
"v_add_f32 v167, v167, v148\n"
"v_mfma_f32_16x16x32_bf16 v[232:235], a[248:251], v[12:15], v[232:235]\n"
"buffer_load_dwordx4 v248, s[44:47], s80 offen offset:3072 nt lds\n"
"v_mfma_f32_16x16x32_bf16 v[232:235], a[252:255], v[44:47], v[232:235]\n"
"s_add_u32 m0, s79, 0x0\n"
"s_add_u32 s80, s10, s24\n"
"v_mfma_f32_16x16x32_bf16 v[228:231], a[252:255], v[36:39], v[228:231]\n"
"s_addk_i32 s10, 0x1000\n"
"ds_read_b128 a[252:255], v241 offset:384\n"
"v_cvt_pk_bf16_f32 v196, v196, v197\n"
"v_mfma_f32_16x16x32_bf16 v[228:231], a[248:251], v[4:7], v[228:231]\n"
"s_waitcnt lgkmcnt(3)\n"
"buffer_load_dword a204, off, s[64:67], s18\n"
"v_mfma_f32_16x16x32_bf16 v[4:7], a[224:227], a[0:3], 0\n"
"s_bitset0_b32 s10, 15\n"
"ds_read_b128 a[248:251], v240 offset:384\n"
"v_mfma_f32_16x16x32_bf16 v[4:7], a[228:231], a[32:35], v[4:7]\n"
"v_add_u32 v240, 0xffffbf20, v240\n"
"v_add_u32 v241, 0xffffbf20, v241\n"
"v_mfma_f32_16x16x32_bf16 v[12:15], a[228:231], a[40:43], 0\n"
"v_cvt_pk_bf16_f32 v197, v198, v199\n"
"v_add_f32 v166, v166, v79\n"
"v_mfma_f32_16x16x32_bf16 v[12:15], a[224:227], a[8:11], v[12:15]\n"
"v_cvt_pk_bf16_f32 v198, v76, v77\n"
"v_cvt_pk_bf16_f32 v199, v78, v79\n"
"v_mfma_f32_16x16x32_bf16 v[20:23], a[224:227], a[16:19], 0\n"
"PG10_%=:\n"
"s_cbranch_execz PGB2_%=\n"
"v_add_f32 v167, v167, v149\n"
"v_cvt_pk_bf16_f32 v148, v148, v149\n"
"v_mfma_f32_16x16x32_bf16 v[20:23], a[228:231], a[48:51], v[20:23]\n"
"v_cvt_pk_bf16_f32 v149, v150, v151\n"
"v_add_u32 v244, 0xffffbf20, v244\n"
"v_mfma_f32_16x16x32_bf16 v[28:31], a[228:231], a[4:7], 0\n"
"ds_read_b128 a[228:231], v243 offset:0\n"
"v_add_u32 v246, 0xffffbf20, v246\n"
"v_mfma_f32_16x16x32_bf16 v[28:31], a[224:227], a[24:27], v[28:31]\n"
"ds_read_b128 a[224:227], v242 offset:0\n"
"v_add_f32 v167, v167, v150\n"
"v_mfma_f32_16x16x32_bf16 v[4:7], a[232:235], a[56:59], v[4:7]\n"
"ds_write_b64 v253, v[148:149] offset:1536\n"
"v_add_f32 v167, v167, v151\n"
"v_mfma_f32_16x16x32_bf16 v[4:7], a[236:239], a[88:91], v[4:7]\n"
"v_add_u32 v245, 0xffffbf20, v245\n"
"v_add_u32 v247, 0xffffbf20, v247\n"
"v_mfma_f32_16x16x32_bf16 v[12:15], a[236:239], a[96:99], v[12:15]\n"
"v_bfe_u32 v251, v248, 4, 6\n"
"v_lshrrev_b32 v249, 3, v251\n"
"v_mfma_f32_16x16x32_bf16 v[12:15], a[232:235], a[64:67], v[12:15]\n"
"v_mul_u32_u24 v249, 14336, v249\n"
"v_and_b32 v250, 7, v251\n"
"v_mfma_f32_16x16x32_bf16 v[20:23], a[232:235], a[72:75], v[20:23]\n"
"v_lshl_add_u32 v249, v250, 4, v249\n"
"v_add_u32 v249, s86, v249\n"
"v_mfma_f32_16x16x32_bf16 v[20:23], a[236:239], a[104:107], v[20:23]\n"
"s_lshr_b32 s86, s72, 16\n"
"buffer_load_dwordx4 v248, s[44:47], s81 offen offset:0 nt lds\n"
"v_mfma_f32_16x16x32_bf16 v[28:31], a[236:239], a[12:15], v[28:31]\n"
"s_cmp_eq_u32 s86, s30\n"
"s_cselect_b32 s68, s68, 16608\n"
"s_cselect_b32 s72, 0x40000000, s72\n"
"ds_read_b128 a[236:239], v243 offset:128\n"
"v_mfma_f32_16x16x32_bf16 v[28:31], a[232:235], a[80:83], v[28:31]\n"
"s_waitcnt lgkmcnt(4)\n"
"ds_read_b128 a[232:235], v242 offset:128\n"
"v_add_u32 v250, 6144, v249\n"
"v_mfma_f32_16x16x32_bf16 v[4:7], a[240:243], a[112:115], v[4:7]\n"
"s_lshr_b32 s86, s73, 16\n"
"v_add_u32 v251, 896, v249\n"
"v_add_u32 v252, 7040, v249\n"
"v_mfma_f32_16x16x32_bf16 v[4:7], a[244:247], a[144:147], v[4:7]\n"
"s_cmp_eq_u32 s86, s30\n"
"s_cselect_b32 s69, s69, 16608\n"
"s_cselect_b32 s73, 0x40000000, s73\n"
"s_sub_u32 s68, s68, s95\n"
"v_mfma_f32_16x16x32_bf16 v[12:15], a[244:247], a[152:155], v[12:15]\n"
"s_lshr_b32 s86, s78, 16\n"
"s_sub_u32 s69, s69, s95\n"
"v_mfma_f32_16x16x32_bf16 v[12:15], a[240:243], a[120:123], v[12:15]\n"
"s_cmp_eq_u32 s86, s30\n"
"s_cselect_b32 s96, s96, 16608\n"
"s_cselect_b32 s78, 0x40000000, s78\n"
"v_mfma_f32_16x16x32_bf16 v[20:23], a[240:243], a[128:131], v[20:23]\n"
"s_lshl_b32 s86, %2, 2\n"
"s_add_u32 s86, s86, 159712\n"
"s_sub_u32 s96, s96, s95\n"
"v_mfma_f32_16x16x32_bf16 v[20:23], a[244:247], a[160:163], v[20:23]\n"
"v_mfma_f32_16x16x32_bf16 v[28:31], a[244:247], a[20:23], v[28:31]\n"
"ds_read_b128 a[244:247], v243 offset:256\n"
"v_mfma_f32_16x16x32_bf16 v[28:31], a[240:243], a[136:139], v[28:31]\n"
"ds_read_b128 a[240:243], v242 offset:256\n"
"v_mfma_f32_16x16x32_bf16 v[4:7], a[248:251], a[168:171], v[4:7]\n"
"buffer_load_dwordx4 v248, s[44:47], s81 offen offset:1024 nt lds\n"
"v_mfma_f32_16x16x32_bf16 v[4:7], a[252:255], a[200:203], v[4:7]\n"
"s_addk_i32 m0, 0x20\n"
"v_mfma_f32_16x16x32_bf16 v[12:15], a[252:255], a[208:211], v[12:15]\n"
"v_mfma_f32_16x16x32_bf16 v[12:15], a[248:251], a[176:179], v[12:15]\n"
"v_mfma_f32_16x16x32_bf16 v[20:23], a[248:251], a[184:187], v[20:23]\n"
"v_mfma_f32_16x16x32_bf16 v[20:23], a[252:255], a[216:219], v[20:23]\n"
"v_mfma_f32_16x16x32_bf16 v[28:31], a[252:255], a[28:31], v[28:31]\n"
"ds_read_b128 a[252:255], v243 offset:384\n"
"v_add_u32 v243, 0xffffbf20, v243\n"
"v_mfma_f32_16x16x32_bf16 v[28:31], a[248:251], a[192:195], v[28:31]\n"
"s_waitcnt lgkmcnt(3)\n"
"ds_read_b128 a[248:251], v242 offset:384\n"
"v_fma_f32 v4, v4, s92, v156\n"
"v_mfma_f32_16x16x32_bf16 v[100:103], a[224:227], a[0:3], 0\n"
"v_add_u32 v242, 0xffffbf20, v242\n"
"v_fma_f32 v5, v5, s92, v156\n"
"v_mfma_f32_16x16x32_bf16 v[100:103], a[228:231], a[32:35], v[100:103]\n"
"v_exp_f32 v4, v4\n"
"v_mfma_f32_16x16x32_bf16 v[108:111], a[228:231], a[40:43], 0\n"
"v_exp_f32 v5, v5\n"
"v_mfma_f32_16x16x32_bf16 v[108:111], a[224:227], a[8:11], v[108:111]\n"
"v_fma_f32 v6, v6, s92, v156\n"
"v_fma_f32 v7, v7, s92, v156\n"
"v_mfma_f32_16x16x32_bf16 v[116:119], a[224:227], a[16:19], 0\n"
"ds_read_b128 a[224:227], v240 offset:0\n"
"v_fma_f32 v12, v12, s92, v157\n"
"v_mfma_f32_16x16x32_bf16 v[116:119], a[228:231], a[48:51], v[116:119]\n"
"ds_read_b128 a[228:231], v241 offset:0\n"
"v_fma_f32 v13, v13, s92, v157\n"
"v_mfma_f32_16x16x32_bf16 v[100:103], a[232:235], a[56:59], v[100:103]\n"
"v_exp_f32 v6, v6\n"
"v_mfma_f32_16x16x32_bf16 v[100:103], a[236:239], a[88:91], v[100:103]\n"
"v_exp_f32 v7, v7\n"
"v_mfma_f32_16x16x32_bf16 v[108:111], a[236:239], a[96:99], v[108:111]\n"
"v_exp_f32 v12, v12\n"
"v_mfma_f32_16x16x32_bf16 v[108:111], a[232:235], a[64:67], v[108:111]\n"
"v_exp_f32 v13, v13\n"
"v_mfma_f32_16x16x32_bf16 v[116:119], a[232:235], a[72:75], v[116:119]\n"
"ds_read_b128 a[232:235], v240 offset:128\n"
"v_fma_f32 v14, v14, s92, v157\n"
"v_mfma_f32_16x16x32_bf16 v[116:119], a[236:239], a[104:107], v[116:119]\n"
"s_waitcnt lgkmcnt(3)\n"
"ds_read_b128 a[236:239], v241 offset:128\n"
"v_fma_f32 v15, v15, s92, v157\n"
"v_mfma_f32_16x16x32_bf16 v[100:103], a[240:243], a[112:115], v[100:103]\n"
"v_exp_f32 v14, v14\n"
"v_mfma_f32_16x16x32_bf16 v[100:103], a[244:247], a[144:147], v[100:103]\n"
"v_exp_f32 v15, v15\n"
"v_mfma_f32_16x16x32_bf16 v[108:111], a[244:247], a[152:155], v[108:111]\n"
"v_fma_f32 v20, v20, s92, v158\n"
"v_fma_f32 v21, v21, s92, v158\n"
"v_mfma_f32_16x16x32_bf16 v[108:111], a[240:243], a[120:123], v[108:111]\n"
"buffer_load_dwordx4 v248, s[44:47], s81 offen offset:2048 nt lds\n"
"v_mfma_f32_16x16x32_bf16 v[116:119], a[240:243], a[128:131], v[116:119]\n"
"ds_read_b128 a[240:243], v240 offset:256\n"
"v_mfma_f32_16x16x32_bf16 v[116:119], a[244:247], a[160:163], v[116:119]\n"
"ds_read_b128 a[244:247], v241 offset:256\n"
"v_fma_f32 v22, v22, s92, v158\n"
"v_mfma_f32_16x16x32_bf16 v[100:103], a[248:251], a[168:171], v[100:103]\n"
"v_exp_f32 v20, v20\n"
"v_mfma_f32_16x16x32_bf16 v[100:103], a[252:255], a[200:203], v[100:103]\n"
"v_exp_f32 v21, v21\n"
"v_mfma_f32_16x16x32_bf16 v[108:111], a[252:255], a[208:211], v[108:111]\n"
"v_fma_f32 v28, v28, s92, v159\n"
"v_fma_f32 v29, v29, s92, v159\n"
"v_mfma_f32_16x16x32_bf16 v[108:111], a[248:251], a[176:179], v[108:111]\n"
"v_exp_f32 v22, v22\n"
"v_mfma_f32_16x16x32_bf16 v[116:119], a[248:251], a[184:187], v[116:119]\n"
"ds_read_b128 a[248:251], v240 offset:384\n"
"v_fma_f32 v30, v30, s92, v159\n"
"v_mfma_f32_16x16x32_bf16 v[116:119], a[252:255], a[216:219], v[116:119]\n"
"s_waitcnt lgkmcnt(3)\n"
"ds_read_b128 a[252:255], v241 offset:384\n"
"v_add_u32 v240, 0x14460, v240\n"
"v_mfma_f32_16x16x32_bf16 v[36:39], a[224:227], a[0:3], 0\n"
"v_add_u32 v241, 0x14460, v241\n"
"v_fma_f32 v31, v31, s92, v159\n"
"v_mfma_f32_16x16x32_bf16 v[36:39], a[228:231], a[32:35], v[36:39]\n"
"v_exp_f32 v28, v28\n"
"v_mfma_f32_16x16x32_bf16 v[44:47], a[228:231], a[40:43], 0\n"
"v_exp_f32 v29, v29\n"
"v_mfma_f32_16x16x32_bf16 v[44:47], a[224:227], a[8:11], v[44:47]\n"
"v_exp_f32 v30, v30\n"
"v_mfma_f32_16x16x32_bf16 v[52:55], a[224:227], a[16:19], 0\n"
"v_exp_f32 v31, v31\n"
"v_mfma_f32_16x16x32_bf16 v[52:55], a[228:231], a[48:51], v[52:55]\n"
"v_fma_f32 v23, v23, s92, v158\n"
"v_add_f32 v167, v167, v28\n"
"v_mfma_f32_16x16x32_bf16 v[92:95], a[228:231], a[4:7], 0\n"
"ds_read_b128 a[228:231], v243 offset:0\n"
"v_add_f32 v167, v167, v29\n"
"v_mfma_f32_16x16x32_bf16 v[92:95], a[224:227], a[24:27], v[92:95]\n"
"ds_read_b128 a[224:227], v242 offset:0\n"
"v_cvt_pk_bf16_f32 v28, v28, v29\n"
"v_mfma_f32_16x16x32_bf16 v[36:39], a[232:235], a[56:59], v[36:39]\n"
"buffer_load_dwordx4 v248, s[44:47], s81 offen offset:3072 nt lds\n"
"v_mfma_f32_16x16x32_bf16 v[36:39], a[236:239], a[88:91], v[36:39]\n"
"s_add_u32 m0, s79, 0x14460\n"
"s_add_u32 s81, s10, s24\n"
"v_mfma_f32_16x16x32_bf16 v[44:47], a[236:239], a[96:99], v[44:47]\n"
"s_addk_i32 s10, 0x1000\n"
"v_cvt_pk_bf16_f32 v29, v30, v31\n"
"v_fma_f32 v100, v100, s92, v156\n"
"v_mfma_f32_16x16x32_bf16 v[44:47], a[232:235], a[64:67], v[44:47]\n"
"s_bitset0_b32 s10, 15\n"
"v_exp_f32 v23, v23\n"
"v_mfma_f32_16x16x32_bf16 v[52:55], a[232:235], a[72:75], v[52:55]\n"
"ds_write_b64 v253, v[28:29] offset:0\n"
"v_fma_f32 v101, v101, s92, v156\n"
"v_mfma_f32_16x16x32_bf16 v[52:55], a[236:239], a[104:107], v[52:55]\n"
"v_exp_f32 v100, v100\n"
"v_mfma_f32_16x16x32_bf16 v[92:95], a[236:239], a[12:15], v[92:95]\n"
"ds_read_b128 a[236:239], v243 offset:128\n"
"v_fma_f32 v102, v102, s92, v156\n"
"v_mfma_f32_16x16x32_bf16 v[92:95], a[232:235], a[80:83], v[92:95]\n"
"s_waitcnt lgkmcnt(4)\n"
"ds_read_b128 a[232:235], v242 offset:128\n"
"v_fma_f32 v103, v103, s92, v156\n"
"v_mfma_f32_16x16x32_bf16 v[36:39], a[240:243], a[112:115], v[36:39]\n"
"v_exp_f32 v101, v101\n"
"v_mfma_f32_16x16x32_bf16 v[36:39], a[244:247], a[144:147], v[36:39]\n"
"v_exp_f32 v102, v102\n"
"v_mfma_f32_16x16x32_bf16 v[44:47], a[244:247], a[152:155], v[44:47]\n"
"v_exp_f32 v103, v103\n"
"v_mfma_f32_16x16x32_bf16 v[44:47], a[240:243], a[120:123], v[44:47]\n"
"v_fma_f32 v108, v108, s92, v157\n"
"v_fma_f32 v109, v109, s92, v157\n"
"v_mfma_f32_16x16x32_bf16 v[52:55], a[240:243], a[128:131], v[52:55]\n"
"v_exp_f32 v108, v108\n"
"v_mfma_f32_16x16x32_bf16 v[52:55], a[244:247], a[160:163], v[52:55]\n"
"v_exp_f32 v109, v109\n"
"v_mfma_f32_16x16x32_bf16 v[92:95], a[244:247], a[20:23], v[92:95]\n"
"ds_read_b128 a[244:247], v243 offset:256\n"
"v_fma_f32 v110, v110, s92, v157\n"
"v_mfma_f32_16x16x32_bf16 v[92:95], a[240:243], a[136:139], v[92:95]\n"
"ds_read_b128 a[240:243], v242 offset:256\n"
"v_fma_f32 v111, v111, s92, v157\n"
"v_mfma_f32_16x16x32_bf16 v[36:39], a[248:251], a[168:171], v[36:39]\n"
"v_exp_f32 v110, v110\n"
"v_mfma_f32_16x16x32_bf16 v[36:39], a[252:255], a[200:203], v[36:39]\n"
"buffer_load_dwordx4 v248, s[48:51], s80 offen offset:0 nt lds\n"
"v_mfma_f32_16x16x32_bf16 v[44:47], a[252:255], a[208:211], v[44:47]\n"
"v_mfma_f32_16x16x32_bf16 v[44:47], a[248:251], a[176:179], v[44:47]\n"
"v_exp_f32 v111, v111\n"
"v_mfma_f32_16x16x32_bf16 v[52:55], a[248:251], a[184:187], v[52:55]\n"
"v_fma_f32 v116, v116, s92, v158\n"
"v_fma_f32 v117, v117, s92, v158\n"
"v_mfma_f32_16x16x32_bf16 v[52:55], a[252:255], a[216:219], v[52:55]\n"
"v_exp_f32 v116, v116\n"
"v_mfma_f32_16x16x32_bf16 v[92:95], a[252:255], a[28:31], v[92:95]\n"
"ds_read_b128 a[252:255], v243 offset:384\n"
"v_fma_f32 v118, v118, s92, v158\n"
"v_mfma_f32_16x16x32_bf16 v[92:95], a[248:251], a[192:195], v[92:95]\n"
"s_waitcnt lgkmcnt(3)\n"
"ds_read_b128 a[248:251], v242 offset:384\n"
"v_fma_f32 v119, v119, s92, v158\n"
"v_mfma_f32_16x16x32_bf16 v[60:63], a[224:227], a[0:3], 0\n"
"v_exp_f32 v117, v117\n"
"v_mfma_f32_16x16x32_bf16 v[60:63], a[228:231], a[32:35], v[60:63]\n"
"v_exp_f32 v118, v118\n"
"v_mfma_f32_16x16x32_bf16 v[68:71], a[228:231], a[40:43], 0\n"
"v_add_u32 v242, 0x14460, v242\n"
"v_add_u32 v243, 0x14460, v243\n"
"v_mfma_f32_16x16x32_bf16 v[68:71], a[224:227], a[8:11], v[68:71]\n"
"v_exp_f32 v119, v119\n"
"v_mfma_f32_16x16x32_bf16 v[76:79], a[224:227], a[16:19], 0\n"
"s_waitcnt vmcnt(10) lgkmcnt(0)\n"
"s_barrier\n"
"v_mfma_f32_16x16x32_bf16 v[76:79], a[228:231], a[48:51], v[76:79]\n"
"ds_read_b64_tr_b16 a[224:225], v244 offset:0\n"
"v_fma_f32 v36, v36, s92, v156\n"
"v_mfma_f32_16x16x32_bf16 v[60:63], a[232:235], a[56:59], v[60:63]\n"
"ds_read_b64_tr_b16 a[226:227], v246 offset:0\n"
"v_fma_f32 v37, v37, s92, v156\n"
"v_mfma_f32_16x16x32_bf16 v[60:63], a[236:239], a[88:91], v[60:63]\n"
"ds_read_b64_tr_b16 a[228:229], v224 offset:0\n"
"v_add_f32 v164, v164, v4\n"
"v_mfma_f32_16x16x32_bf16 v[68:71], a[236:239], a[96:99], v[68:71]\n"
"ds_read_b64_tr_b16 a[230:231], v226 offset:0\n"
"v_add_f32 v164, v164, v5\n"
"v_mfma_f32_16x16x32_bf16 v[68:71], a[232:235], a[64:67], v[68:71]\n"
"ds_read_b64 v[174:175], v254 offset:1024\n"
"v_fma_f32 v38, v38, s92, v156\n"
"v_mfma_f32_16x16x32_bf16 v[76:79], a[232:235], a[72:75], v[76:79]\n"
"buffer_load_dwordx4 v248, s[48:51], s80 offen offset:1024 nt lds\n"
"v_mfma_f32_16x16x32_bf16 v[76:79], a[236:239], a[104:107], v[76:79]\n"
"s_addk_i32 m0, 0x20\n"
"ds_read_b64 v[150:151], v254 offset:1536\n"
"v_mfma_f32_16x16x32_bf16 v[60:63], a[240:243], a[112:115], v[60:63]\n"
"ds_read_b64_tr_b16 a[232:233], v244 offset:32\n"
"v_add_f32 v164, v164, v6\n"
"v_mfma_f32_16x16x32_bf16 v[60:63], a[244:247], a[144:147], v[60:63]\n"
"ds_read_b64_tr_b16 a[234:235], v246 offset:32\n"
"v_add_f32 v164, v164, v7\n"
"v_mfma_f32_16x16x32_bf16 v[68:71], a[244:247], a[152:155], v[68:71]\n"
"ds_read_b64_tr_b16 a[236:237], v224 offset:32\n"
"v_fma_f32 v39, v39, s92, v156\n"
"v_mfma_f32_16x16x32_bf16 v[68:71], a[240:243], a[120:123], v[68:71]\n"
"ds_read_b64_tr_b16 a[238:239], v226 offset:32\n"
"v_add_f32 v164, v164, v100\n"
"v_mfma_f32_16x16x32_bf16 v[76:79], a[240:243], a[128:131], v[76:79]\n"
"ds_read_b64_tr_b16 a[240:241], v245 offset:0\n"
"v_add_f32 v165, v165, v12\n"
"v_mfma_f32_16x16x32_bf16 v[76:79], a[244:247], a[160:163], v[76:79]\n"
"ds_read_b64_tr_b16 a[242:243], v247 offset:0\n"
"v_add_f32 v164, v164, v101\n"
"v_mfma_f32_16x16x32_bf16 v[60:63], a[248:251], a[168:171], v[60:63]\n"
"ds_read_b64_tr_b16 a[244:245], v225 offset:0\n"
"v_add_f32 v165, v165, v13\n"
"v_mfma_f32_16x16x32_bf16 v[60:63], a[252:255], a[200:203], v[60:63]\n"
"ds_read_b64_tr_b16 a[246:247], v227 offset:0\n"
"v_add_f32 v164, v164, v102\n"
"v_mfma_f32_16x16x32_bf16 v[68:71], a[252:255], a[208:211], v[68:71]\n"
"v_exp_f32 v36, v36\n"
"v_mfma_f32_16x16x32_bf16 v[68:71], a[248:251], a[176:179], v[68:71]\n"
"v_exp_f32 v37, v37\n"
"v_mfma_f32_16x16x32_bf16 v[76:79], a[248:251], a[184:187], v[76:79]\n"
"buffer_load_dword a205, off, s[56:59], s19\n"
"v_mfma_f32_16x16x32_bf16 v[76:79], a[252:255], a[216:219], v[76:79]\n"
"s_waitcnt lgkmcnt(8)\n"
"ds_read_b64_tr_b16 a[248:249], v245 offset:32\n"
"v_mfma_f32_16x16x32_bf16 v[0:3], a[224:227], v[124:127], v[0:3]\n"
"ds_read_b64_tr_b16 a[252:253], v225 offset:32\n"
"v_add_f32 v165, v165, v14\n"
"v_mfma_f32_16x16x32_bf16 v[0:3], a[228:231], v[180:183], v[0:3]\n"
"ds_read_b64_tr_b16 a[254:255], v227 offset:32\n"
"v_cvt_pk_bf16_f32 v4, v4, v5\n"
"v_mfma_f32_16x16x32_bf16 v[8:11], a[228:231], v[188:191], v[8:11]\n"
"ds_read_b64_tr_b16 a[250:251], v247 offset:32\n"
"v_cvt_pk_bf16_f32 v5, v6, v7\n"
"v_mfma_f32_16x16x32_bf16 v[8:11], a[224:227], v[132:135], v[8:11]\n"
"buffer_load_dwordx4 v248, s[48:51], s80 offen offset:2048 nt lds\n"
"v_mfma_f32_16x16x32_bf16 v[16:19], a[224:227], v[140:143], v[16:19]\n"
"v_mfma_f32_16x16x32_bf16 v[16:19], a[228:231], v[196:199], v[16:19]\n"
"v_exp_f32 v38, v38\n"
"v_mfma_f32_16x16x32_bf16 v[24:27], a[228:231], v[148:151], v[24:27]\n"
"ds_read_b64_tr_b16 a[228:229], v224 offset:128\n"
"v_add_f32 v164, v164, v103\n"
"v_mfma_f32_16x16x32_bf16 v[24:27], a[224:227], v[172:175], v[24:27]\n"
"s_waitcnt lgkmcnt(5)\n"
"ds_read_b64_tr_b16 a[224:225], v244 offset:128\n"
"v_cvt_pk_bf16_f32 v6, v100, v101\n"
"v_mfma_f32_16x16x32_bf16 v[220:223], a[232:235], v[172:175], v[220:223]\n"
"ds_read_b64_tr_b16 a[226:227], v246 offset:128\n"
"v_cvt_pk_bf16_f32 v7, v102, v103\n"
"v_mfma_f32_16x16x32_bf16 v[220:223], a[236:239], v[148:151], v[220:223]\n"
"ds_read_b64_tr_b16 a[230:231], v226 offset:128\n"
"v_add_f32 v165, v165, v15\n"
"v_mfma_f32_16x16x32_bf16 v[212:215], a[236:239], v[196:199], v[212:215]\n"
"v_exp_f32 v39, v39\n"
"v_mfma_f32_16x16x32_bf16 v[212:215], a[232:235], v[140:143], v[212:215]\n"
"v_add_f32 v165, v165, v108\n"
"v_add_f32 v166, v166, v20\n"
"v_mfma_f32_16x16x32_bf16 v[204:207], a[232:235], v[132:135], v[204:207]\n"
"v_add_f32 v165, v165, v109\n"
"v_add_f32 v166, v166, v21\n"
"v_mfma_f32_16x16x32_bf16 v[204:207], a[236:239], v[188:191], v[204:207]\n"
"v_fma_f32 v44, v44, s92, v157\n"
"v_add_f32 v165, v165, v110\n"
"v_mfma_f32_16x16x32_bf16 v[84:87], a[236:239], v[180:183], v[84:87]\n"
"ds_read_b64_tr_b16 a[236:237], v224 offset:160\n"
"v_add_f32 v166, v166, v22\n"
"v_mfma_f32_16x16x32_bf16 v[84:87], a[232:235], v[124:127], v[84:87]\n"
"ds_read_b64_tr_b16 a[238:239], v226 offset:160\n"
"v_cvt_pk_bf16_f32 v12, v12, v13\n"
"v_mfma_f32_16x16x32_bf16 v[32:35], a[240:243], v[124:127], v[32:35]\n"
"ds_read_b64_tr_b16 a[232:233], v244 offset:160\n"
"v_cvt_pk_bf16_f32 v13, v14, v15\n"
"v_mfma_f32_16x16x32_bf16 v[32:35], a[244:247], v[180:183], v[32:35]\n"
"ds_read_b64_tr_b16 a[234:235], v246 offset:160\n"
"v_add_f32 v165, v165, v111\n"
"v_mfma_f32_16x16x32_bf16 v[40:43], a[244:247], v[188:191], v[40:43]\n"
"v_cvt_pk_bf16_f32 v14, v108, v109\n"
"v_cvt_pk_bf16_f32 v15, v110, v111\n"
"v_mfma_f32_16x16x32_bf16 v[40:43], a[240:243], v[132:135], v[40:43]\n"
"v_add_f32 v166, v166, v23\n"
"v_fma_f32 v45, v45, s92, v157\n"
"v_mfma_f32_16x16x32_bf16 v[48:51], a[240:243], v[140:143], v[48:51]\n"
"buffer_load_dwordx4 v248, s[48:51], s80 offen offset:3072 nt lds\n"
"v_mfma_f32_16x16x32_bf16 v[48:51], a[244:247], v[196:199], v[48:51]\n"
"s_waitcnt lgkmcnt(4)\n"
"s_add_u32 m0, s79, 0x81c0\n"
"ds_read_b64_tr_b16 a[240:241], v245 offset:128\n"
"v_mfma_f32_16x16x32_bf16 a[52:55], a[252:255], v[196:199], a[52:55]\n"
"s_add_u32 s80, s10, s24\n"
"ds_read_b64_tr_b16 a[242:243], v247 offset:128\n"
"v_add_f32 v166, v166, v116\n"
"v_mfma_f32_16x16x32_bf16 a[52:55], a[248:251], v[140:143], a[52:55]\n"
"s_addk_i32 s10, 0x1000\n"
"ds_read_b64_tr_b16 a[244:245], v225 offset:128\n"
"v_add_f32 v166, v166, v117\n"
"v_mfma_f32_16x16x32_bf16 a[44:47], a[248:251], v[132:135], a[44:47]\n"
"s_bitset0_b32 s10, 15\n"
"ds_read_b64_tr_b16 a[246:247], v227 offset:128\n"
"v_fma_f32 v46, v46, s92, v157\n"
"v_mfma_f32_16x16x32_bf16 a[44:47], a[252:255], v[188:191], a[44:47]\n"
"v_exp_f32 v44, v44\n"
"v_mfma_f32_16x16x32_bf16 a[36:39], a[252:255], v[180:183], a[36:39]\n"
"ds_read_b64_tr_b16 a[252:253], v225 offset:160\n"
"v_add_f32 v166, v166, v118\n"
"v_mfma_f32_16x16x32_bf16 a[36:39], a[248:251], v[124:127], a[36:39]\n"
"ds_read_b64_tr_b16 a[254:255], v227 offset:160\n"
"v_cvt_pk_bf16_f32 v20, v20, v21\n"
"v_mfma_f32_16x16x32_bf16 v[56:59], a[224:227], v[124:127], v[56:59]\n"
"ds_read_b64_tr_b16 a[248:249], v245 offset:160\n"
"v_cvt_pk_bf16_f32 v21, v22, v23\n"
"v_mfma_f32_16x16x32_bf16 v[56:59], a[228:231], v[180:183], v[56:59]\n"
"ds_read_b64_tr_b16 a[250:251], v247 offset:160\n"
"v_add_f32 v166, v166, v119\n"
"v_mfma_f32_16x16x32_bf16 v[64:67], a[228:231], v[188:191], v[64:67]\n"
"v_exp_f32 v45, v45\n"
"v_mfma_f32_16x16x32_bf16 v[64:67], a[224:227], v[132:135], v[64:67]\n"
"v_cvt_pk_bf16_f32 v22, v116, v117\n"
"v_cvt_pk_bf16_f32 v23, v118, v119\n"
"v_mfma_f32_16x16x32_bf16 v[72:75], a[224:227], v[140:143], v[72:75]\n"
"v_exp_f32 v46, v46\n"
"v_mfma_f32_16x16x32_bf16 v[72:75], a[228:231], v[196:199], v[72:75]\n"
"v_fma_f32 v47, v47, s92, v157\n"
"v_fma_f32 v52, v52, s92, v158\n"
"v_mfma_f32_16x16x32_bf16 v[80:83], a[228:231], v[148:151], v[80:83]\n"
"ds_read_b64_tr_b16 a[228:229], v224 offset:256\n"
"v_fma_f32 v53, v53, s92, v158\n"
"v_mfma_f32_16x16x32_bf16 v[80:83], a[224:227], v[172:175], v[80:83]\n"
"s_waitcnt lgkmcnt(6)\n"
"ds_read_b64_tr_b16 a[224:225], v244 offset:256\n"
"v_fma_f32 v54, v54, s92, v158\n"
"v_mfma_f32_16x16x32_bf16 a[84:87], a[232:235], v[172:175], a[84:87]\n"
"ds_read_b64_tr_b16 a[226:227], v246 offset:256\n"
"v_fma_f32 v92, v92, s92, v159\n"
"v_mfma_f32_16x16x32_bf16 a[84:87], a[236:239], v[148:151], a[84:87]\n"
"buffer_load_dwordx4 v248, s[48:51], s81 offen offset:0 nt lds\n"
"v_mfma_f32_16x16x32_bf16 a[76:79], a[236:239], v[196:199], a[76:79]\n"
"ds_read_b64_tr_b16 a[230:231], v226 offset:256\n"
"v_mfma_f32_16x16x32_bf16 a[76:79], a[232:235], v[140:143], a[76:79]\n"
"v_exp_f32 v47, v47\n"
"v_mfma_f32_16x16x32_bf16 a[68:71], a[232:235], v[132:135], a[68:71]\n"
"v_exp_f32 v52, v52\n"
"v_mfma_f32_16x16x32_bf16 a[68:71], a[236:239], v[188:191], a[68:71]\n"
"v_exp_f32 v53, v53\n"
"v_mfma_f32_16x16x32_bf16 a[60:63], a[236:239], v[180:183], a[60:63]\n"
"ds_read_b64_tr_b16 a[236:237], v224 offset:288\n"
"v_fma_f32 v93, v93, s92, v159\n"
"v_mfma_f32_16x16x32_bf16 a[60:63], a[232:235], v[124:127], a[60:63]\n"
"ds_read_b64_tr_b16 a[232:233], v244 offset:288\n"
"v_fma_f32 v94, v94, s92, v159\n"
"v_mfma_f32_16x16x32_bf16 v[88:91], a[240:243], v[124:127], v[88:91]\n"
"s_waitcnt lgkmcnt(6)\n"
"ds_read_b64_tr_b16 a[234:235], v246 offset:288\n"
"v_fma_f32 v95, v95, s92, v159\n"
"v_mfma_f32_16x16x32_bf16 v[88:91], a[244:247], v[180:183], v[88:91]\n"
"ds_read_b64_tr_b16 a[238:239], v226 offset:288\n"
"v_add_f32 v167, v167, v30\n"
"v_mfma_f32_16x16x32_bf16 v[96:99], a[244:247], v[188:191], v[96:99]\n"
"v_exp_f32 v54, v54\n"
"v_mfma_f32_16x16x32_bf16 v[96:99], a[240:243], v[132:135], v[96:99]\n"
"v_exp_f32 v92, v92\n"
"v_mfma_f32_16x16x32_bf16 v[104:107], a[240:243], v[140:143], v[104:107]\n"
"ds_read_b64_tr_b16 a[240:241], v245 offset:256\n"
"v_add_f32 v167, v167, v31\n"
"v_mfma_f32_16x16x32_bf16 v[104:107], a[244:247], v[196:199], v[104:107]\n"
"ds_read_b64_tr_b16 a[242:243], v247 offset:256\n"
"v_fma_f32 v55, v55, s92, v158\n"
"v_mfma_f32_16x16x32_bf16 a[108:111], a[252:255], v[196:199], a[108:111]\n"
"ds_read_b64_tr_b16 a[244:245], v225 offset:256\n"
"v_add_f32 v167, v167, v92\n"
"v_mfma_f32_16x16x32_bf16 a[108:111], a[248:251], v[140:143], a[108:111]\n"
"ds_read_b64_tr_b16 a[246:247], v227 offset:256\n"
"v_fma_f32 v60, v60, s92, v156\n"
"v_mfma_f32_16x16x32_bf16 a[100:103], a[248:251], v[132:135], a[100:103]\n"
"v_exp_f32 v93, v93\n"
"v_mfma_f32_16x16x32_bf16 a[100:103], a[252:255], v[188:191], a[100:103]\n"
"v_exp_f32 v94, v94\n"
"v_mfma_f32_16x16x32_bf16 a[92:95], a[252:255], v[180:183], a[92:95]\n"
"buffer_load_dwordx4 v248, s[48:51], s81 offen offset:1024 nt lds\n"
"v_mfma_f32_16x16x32_bf16 a[92:95], a[248:251], v[124:127], a[92:95]\n"
"s_waitcnt lgkmcnt(6)\n"
"s_addk_i32 m0, 0x20\n"
"ds_read_b64_tr_b16 a[252:253], v225 offset:288\n"
"v_mfma_f32_16x16x32_bf16 v[112:115], a[224:227], v[124:127], v[112:115]\n"
"ds_read_b64_tr_b16 a[254:255], v227 offset:288\n"
"v_add_f32 v167, v167, v93\n"
"v_mfma_f32_16x16x32_bf16 v[112:115], a[228:231], v[180:183], v[112:115]\n"
"ds_read_b64_tr_b16 a[248:249], v245 offset:288\n"
"v_cvt_pk_bf16_f32 v92, v92, v93\n"
"v_mfma_f32_16x16x32_bf16 v[120:123], a[228:231], v[188:191], v[120:123]\n"
"ds_read_b64_tr_b16 a[250:251], v247 offset:288\n"
"v_fma_f32 v61, v61, s92, v156\n"
"v_mfma_f32_16x16x32_bf16 v[120:123], a[224:227], v[132:135], v[120:123]\n"
"v_exp_f32 v95, v95\n"
"v_mfma_f32_16x16x32_bf16 v[128:131], a[224:227], v[140:143], v[128:131]\n"
"v_cvt_pk_bf16_f32 v93, v94, v95\n"
"v_fma_f32 v62, v62, s92, v156\n"
"v_mfma_f32_16x16x32_bf16 v[128:131], a[228:231], v[196:199], v[128:131]\n"
"v_exp_f32 v55, v55\n"
"v_mfma_f32_16x16x32_bf16 v[136:139], a[228:231], v[148:151], v[136:139]\n"
"ds_read_b64_tr_b16 a[228:229], v224 offset:384\n"
"v_fma_f32 v63, v63, s92, v156\n"
"v_mfma_f32_16x16x32_bf16 v[136:139], a[224:227], v[172:175], v[136:139]\n"
"s_waitcnt lgkmcnt(5)\n"
"ds_read_b64_tr_b16 a[224:225], v244 offset:384\n"
"v_fma_f32 v68, v68, s92, v157\n"
"v_mfma_f32_16x16x32_bf16 a[140:143], a[232:235], v[172:175], a[140:143]\n"
"ds_read_b64_tr_b16 a[226:227], v246 offset:384\n"
"v_fma_f32 v69, v69, s92, v157\n"
"v_mfma_f32_16x16x32_bf16 a[140:143], a[236:239], v[148:151], a[140:143]\n"
"ds_read_b64_tr_b16 a[230:231], v226 offset:384\n"
"v_fma_f32 v70, v70, s92, v157\n"
"v_mfma_f32_16x16x32_bf16 a[132:135], a[236:239], v[196:199], a[132:135]\n"
"ds_write_b64 v253, v[92:93] offset:512\n"
"v_fma_f32 v71, v71, s92, v157\n"
"v_mfma_f32_16x16x32_bf16 a[132:135], a[232:235], v[140:143], a[132:135]\n"
"v_exp_f32 v60, v60\n"
"v_mfma_f32_16x16x32_bf16 a[124:127], a[232:235], v[132:135], a[124:127]\n"
"v_exp_f32 v61, v61\n"
"v_mfma_f32_16x16x32_bf16 a[124:127], a[236:239], v[188:191], a[124:127]\n"
"v_exp_f32 v62, v62\n"
"v_mfma_f32_16x16x32_bf16 a[116:119], a[236:239], v[180:183], a[116:119]\n"
"ds_read_b64_tr_b16 a[236:237], v224 offset:416\n"
"v_fma_f32 v76, v76, s92, v158\n"
"v_mfma_f32_16x16x32_bf16 a[116:119], a[232:235], v[124:127], a[116:119]\n"
"buffer_load_dwordx4 v248, s[48:51], s81 offen offset:2048 nt lds\n"
"v_mfma_f32_16x16x32_bf16 v[144:147], a[240:243], v[124:127], v[144:147]\n"
"ds_read_b64_tr_b16 a[232:233], v244 offset:416\n"
"v_mfma_f32_16x16x32_bf16 v[144:147], a[244:247], v[180:183], v[144:147]\n"
"ds_read_b64_tr_b16 a[234:235], v246 offset:416\n"
"v_fma_f32 v77, v77, s92, v158\n"
"v_mfma_f32_16x16x32_bf16 v[152:155], a[244:247], v[188:191], v[152:155]\n"
"ds_read_b64_tr_b16 a[238:239], v226 offset:416\n"
"v_fma_f32 v78, v78, s92, v158\n"
"v_mfma_f32_16x16x32_bf16 v[152:155], a[240:243], v[132:135], v[152:155]\n"
"v_exp_f32 v63, v63\n"
"v_mfma_f32_16x16x32_bf16 v[160:163], a[240:243], v[140:143], v[160:163]\n"
"ds_read_b64_tr_b16 a[240:241], v245 offset:384\n"
"v_fma_f32 v79, v79, s92, v158\n"
"v_mfma_f32_16x16x32_bf16 v[160:163], a[244:247], v[196:199], v[160:163]\n"
"s_waitcnt lgkmcnt(6)\n"
"ds_read_b64_tr_b16 a[242:243], v247 offset:384\n"
"v_add_f32 v164, v164, v36\n"
"v_mfma_f32_16x16x32_bf16 a[164:167], a[252:255], v[196:199], a[164:167]\n"
"ds_read_b64_tr_b16 a[244:245], v225 offset:384\n"
"v_add_f32 v164, v164, v37\n"
"v_mfma_f32_16x16x32_bf16 a[164:167], a[248:251], v[140:143], a[164:167]\n"
"ds_read_b64_tr_b16 a[246:247], v227 offset:384\n"
"v_add_f32 v164, v164, v38\n"
"v_mfma_f32_16x16x32_bf16 a[156:159], a[248:251], v[132:135], a[156:159]\n"
"v_exp_f32 v68, v68\n"
"v_mfma_f32_16x16x32_bf16 a[156:159], a[252:255], v[188:191], a[156:159]\n"
"v_exp_f32 v69, v69\n"
"v_mfma_f32_16x16x32_bf16 a[148:151], a[252:255], v[180:183], a[148:151]\n"
"ds_read_b64_tr_b16 a[252:253], v225 offset:416\n"
"v_add_f32 v164, v164, v39\n"
"v_mfma_f32_16x16x32_bf16 a[148:151], a[248:251], v[124:127], a[148:151]\n"
"ds_read_b64_tr_b16 a[254:255], v227 offset:416\n"
"v_add_f32 v164, v164, v60\n"
"v_mfma_f32_16x16x32_bf16 v[168:171], a[224:227], v[124:127], v[168:171]\n"
"ds_read_b64_tr_b16 a[248:249], v245 offset:416\n"
"v_add_f32 v165, v165, v44\n"
"v_mfma_f32_16x16x32_bf16 v[168:171], a[228:231], v[180:183], v[168:171]\n"
"ds_read_b64_tr_b16 a[250:251], v247 offset:416\n"
"v_add_f32 v164, v164, v61\n"
"v_mfma_f32_16x16x32_bf16 v[176:179], a[228:231], v[188:191], v[176:179]\n"
"v_exp_f32 v70, v70\n"
"v_mfma_f32_16x16x32_bf16 v[176:179], a[224:227], v[132:135], v[176:179]\n"
"buffer_load_dwordx4 v248, s[48:51], s81 offen offset:3072 nt lds\n"
"v_mfma_f32_16x16x32_bf16 v[184:187], a[224:227], v[140:143], v[184:187]\n"
"s_add_u32 m0, s79, 0x1c620\n"
"s_add_u32 s81, s10, s24\n"
"v_mfma_f32_16x16x32_bf16 v[184:187], a[228:231], v[196:199], v[184:187]\n"
"v_exp_f32 v71, v71\n"
"v_mfma_f32_16x16x32_bf16 v[192:195], a[228:231], v[148:151], v[192:195]\n"
"s_waitcnt vmcnt(9) lgkmcnt(0)\n"
"s_barrier\n"
"v_mfma_f32_16x16x32_bf16 v[192:195], a[224:227], v[172:175], v[192:195]\n"
"ds_read_b128 a[224:227], v240 offset:0\n"
"v_add_f32 v165, v165, v45\n"
"v_mfma_f32_16x16x32_bf16 a[196:199], a[232:235], v[172:175], a[196:199]\n"
"ds_read_b128 a[228:231], v241 offset:0\n"
"v_add_f32 v164, v164, v62\n"
"v_mfma_f32_16x16x32_bf16 a[196:199], a[236:239], v[148:151], a[196:199]\n"
"v_exp_f32 v76, v76\n"
"v_mfma_f32_16x16x32_bf16 a[188:191], a[236:239], v[196:199], a[188:191]\n"
"v_exp_f32 v77, v77\n"
"v_mfma_f32_16x16x32_bf16 a[188:191], a[232:235], v[140:143], a[188:191]\n"
"v_exp_f32 v78, v78\n"
"v_mfma_f32_16x16x32_bf16 a[180:183], a[232:235], v[132:135], a[180:183]\n"
"v_exp_f32 v79, v79\n"
"v_mfma_f32_16x16x32_bf16 a[180:183], a[236:239], v[188:191], a[180:183]\n"
"v_add_f32 v165, v165, v46\n"
"v_cvt_pk_bf16_f32 v36, v36, v37\n"
"v_mfma_f32_16x16x32_bf16 a[172:175], a[236:239], v[180:183], a[172:175]\n"
"ds_read_b128 a[236:239], v241 offset:128\n"
"v_cvt_pk_bf16_f32 v37, v38, v39\n"
"v_mfma_f32_16x16x32_bf16 a[172:175], a[232:235], v[124:127], a[172:175]\n"
"ds_read_b128 a[232:235], v240 offset:128\n"
"v_add_f32 v164, v164, v63\n"
"v_mfma_f32_16x16x32_bf16 v[200:203], a[240:243], v[124:127], v[200:203]\n"
"v_cvt_pk_bf16_f32 v38, v60, v61\n"
"v_cvt_pk_bf16_f32 v39, v62, v63\n"
"v_mfma_f32_16x16x32_bf16 v[200:203], a[244:247], v[180:183], v[200:203]\n"
"v_add_f32 v165, v165, v47\n"
"v_add_f32 v166, v166, v52\n"
"v_mfma_f32_16x16x32_bf16 v[208:211], a[244:247], v[188:191], v[208:211]\n"
"v_add_f32 v165, v165, v68\n"
"v_add_f32 v166, v166, v53\n"
"v_mfma_f32_16x16x32_bf16 v[208:211], a[240:243], v[132:135], v[208:211]\n"
"v_add_f32 v165, v165, v69\n"
"v_add_f32 v166, v166, v54\n"
"v_mfma_f32_16x16x32_bf16 v[216:219], a[240:243], v[140:143], v[216:219]\n"
"buffer_load_dwordx4 v248, s[48:51], s80 offen offset:0 nt lds\n"
"v_mfma_f32_16x16x32_bf16 v[216:219], a[244:247], v[196:199], v[216:219]\n"
"ds_read_b128 a[240:243], v240 offset:256\n"
"v_mfma_f32_16x16x32_bf16 v[236:239], a[252:255], v[196:199], v[236:239]\n"
"ds_read_b128 a[244:247], v241 offset:256\n"
"v_add_f32 v165, v165, v70\n"
"v_mfma_f32_16x16x32_bf16 v[236:239], a[248:251], v[140:143], v[236:239]\n"
"v_cvt_pk_bf16_f32 v44, v44, v45\n"
"v_add_f32 v165, v165, v71\n"
"v_mfma_f32_16x16x32_bf16 v[232:235], a[248:251], v[132:135], v[232:235]\n"
"v_cvt_pk_bf16_f32 v45, v46, v47\n"
"v_add_f32 v166, v166, v55\n"
"v_mfma_f32_16x16x32_bf16 v[232:235], a[252:255], v[188:191], v[232:235]\n"
"PG11_%=:\n"
"s_cbranch_execz PGB2_%=\n"
"v_cvt_pk_bf16_f32 v46, v68, v69\n"
"v_cvt_pk_bf16_f32 v47, v70, v71\n"
"v_mfma_f32_16x16x32_bf16 v[228:231], a[252:255], v[180:183], v[228:231]\n"
"ds_read_b128 a[252:255], v241 offset:384\n"
"v_add_f32 v166, v166, v76\n"
"v_mfma_f32_16x16x32_bf16 v[228:231], a[248:251], v[124:127], v[228:231]\n"
"s_waitcnt lgkmcnt(3)\n"
"ds_read_b128 a[248:251], v240 offset:384\n"
"v_add_u32 v241, 0xfffdf900, v241\n"
"v_mfma_f32_16x16x32_bf16 v[124:127], a[224:227], a[0:3], 0\n"
"v_add_u32 v240, 0xfffdf900, v240\n"
"v_add_f32 v166, v166, v77\n"
"v_mfma_f32_16x16x32_bf16 v[124:127], a[228:231], a[32:35], v[124:127]\n"
"v_add_f32 v166, v166, v78\n"
"v_cvt_pk_bf16_f32 v52, v52, v53\n"
"v_mfma_f32_16x16x32_bf16 v[132:135], a[228:231], a[40:43], 0\n"
"v_cvt_pk_bf16_f32 v53, v54, v55\n"
"v_add_f32 v166, v166, v79\n"
"v_mfma_f32_16x16x32_bf16 v[132:135], a[224:227], a[8:11], v[132:135]\n"
"v_cvt_pk_bf16_f32 v54, v76, v77\n"
"v_cvt_pk_bf16_f32 v55, v78, v79\n"
"v_mfma_f32_16x16x32_bf16 v[140:143], a[224:227], a[16:19], 0\n"
"v_add_f32 v167, v167, v94\n"
"v_add_u32 v244, 0xffffbf20, v244\n"
"v_mfma_f32_16x16x32_bf16 v[140:143], a[228:231], a[48:51], v[140:143]\n"
"v_add_f32 v167, v167, v95\n"
"v_add_u32 v246, 0xffffbf20, v246\n"
"v_mfma_f32_16x16x32_bf16 v[172:175], a[228:231], a[4:7], 0\n"
"ds_read_b128 a[228:231], v243 offset:0\n"
"v_add_u32 v245, 0xffffbf20, v245\n"
"v_mfma_f32_16x16x32_bf16 v[172:175], a[224:227], a[24:27], v[172:175]\n"
"ds_read_b128 a[224:227], v242 offset:0\n"
"v_add_u32 v247, 0xffffbf20, v247\n"
"v_mfma_f32_16x16x32_bf16 v[124:127], a[232:235], a[56:59], v[124:127]\n"
"v_mfma_f32_16x16x32_bf16 v[124:127], a[236:239], a[88:91], v[124:127]\n"
"buffer_load_dwordx4 v248, s[48:51], s80 offen offset:1024 nt lds\n"
"v_mfma_f32_16x16x32_bf16 v[132:135], a[236:239], a[96:99], v[132:135]\n"
"s_addk_i32 m0, 0x20\n"
"v_mfma_f32_16x16x32_bf16 v[132:135], a[232:235], a[64:67], v[132:135]\n"
"v_mfma_f32_16x16x32_bf16 v[140:143], a[232:235], a[72:75], v[140:143]\n"
"v_mfma_f32_16x16x32_bf16 v[140:143], a[236:239], a[104:107], v[140:143]\n"
"v_mfma_f32_16x16x32_bf16 v[172:175], a[236:239], a[12:15], v[172:175]\n"
"ds_read_b128 a[236:239], v243 offset:128\n"
"v_mfma_f32_16x16x32_bf16 v[172:175], a[232:235], a[80:83], v[172:175]\n"
"s_waitcnt lgkmcnt(3)\n"
"ds_read_b128 a[232:235], v242 offset:128\n"
"v_mfma_f32_16x16x32_bf16 v[124:127], a[240:243], a[112:115], v[124:127]\n"
"v_mfma_f32_16x16x32_bf16 v[124:127], a[244:247], a[144:147], v[124:127]\n"
"v_mfma_f32_16x16x32_bf16 v[132:135], a[244:247], a[152:155], v[132:135]\n"
"v_mfma_f32_16x16x32_bf16 v[132:135], a[240:243], a[120:123], v[132:135]\n"
"v_mfma_f32_16x16x32_bf16 v[140:143], a[240:243], a[128:131], v[140:143]\n"
"buffer_load_dwordx4 v248, s[48:51], s80 offen offset:2048 nt lds\n"
"v_mfma_f32_16x16x32_bf16 v[140:143], a[244:247], a[160:163], v[140:143]\n"
"v_mfma_f32_16x16x32_bf16 v[172:175], a[244:247], a[20:23], v[172:175]\n"
"ds_read_b128 a[244:247], v243 offset:256\n"
"v_mfma_f32_16x16x32_bf16 v[172:175], a[240:243], a[136:139], v[172:175]\n"
"ds_read_b128 a[240:243], v242 offset:256\n"
"v_mfma_f32_16x16x32_bf16 v[124:127], a[248:251], a[168:171], v[124:127]\n"
"v_mfma_f32_16x16x32_bf16 v[124:127], a[252:255], a[200:203], v[124:127]\n"
"v_mfma_f32_16x16x32_bf16 v[132:135], a[252:255], a[208:211], v[132:135]\n"
"v_mfma_f32_16x16x32_bf16 v[132:135], a[248:251], a[176:179], v[132:135]\n"
"v_mfma_f32_16x16x32_bf16 v[140:143], a[248:251], a[184:187], v[140:143]\n"
"v_mfma_f32_16x16x32_bf16 v[140:143], a[252:255], a[216:219], v[140:143]\n"
"v_mfma_f32_16x16x32_bf16 v[172:175], a[252:255], a[28:31], v[172:175]\n"
"ds_read_b128 a[252:255], v243 offset:384\n"
"v_add_u32 v243, 0xfffdf900, v243\n"
"v_mfma_f32_16x16x32_bf16 v[172:175], a[248:251], a[192:195], v[172:175]\n"
"s_waitcnt lgkmcnt(3)\n"
"ds_read_b128 a[248:251], v242 offset:384\n"
"v_fma_f32 v124, v124, s92, v156\n"
"v_mfma_f32_16x16x32_bf16 v[100:103], a[224:227], a[0:3], 0\n"
"v_exp_f32 v124, v124\n"
"v_mfma_f32_16x16x32_bf16 v[100:103], a[228:231], a[32:35], v[100:103]\n"
"v_fma_f32 v125, v125, s92, v156\n"
"v_fma_f32 v126, v126, s92, v156\n"
"v_mfma_f32_16x16x32_bf16 v[108:111], a[228:231], a[40:43], 0\n"
"v_exp_f32 v125, v125\n"
"v_mfma_f32_16x16x32_bf16 v[108:111], a[224:227], a[8:11], v[108:111]\n"
"v_exp_f32 v126, v126\n"
"v_mfma_f32_16x16x32_bf16 v[116:119], a[224:227], a[16:19], 0\n"
"ds_read_b128 a[224:227], v240 offset:0\n"
"v_fma_f32 v127, v127, s92, v156\n"
"v_mfma_f32_16x16x32_bf16 v[116:119], a[228:231], a[48:51], v[116:119]\n"
"ds_read_b128 a[228:231], v241 offset:0\n"
"v_fma_f32 v132, v132, s92, v157\n"
"v_mfma_f32_16x16x32_bf16 v[100:103], a[232:235], a[56:59], v[100:103]\n"
"v_exp_f32 v127, v127\n"
"v_mfma_f32_16x16x32_bf16 v[100:103], a[236:239], a[88:91], v[100:103]\n"
"v_exp_f32 v132, v132\n"
"v_mfma_f32_16x16x32_bf16 v[108:111], a[236:239], a[96:99], v[108:111]\n"
"v_fma_f32 v133, v133, s92, v157\n"
"v_fma_f32 v134, v134, s92, v157\n"
"v_mfma_f32_16x16x32_bf16 v[108:111], a[232:235], a[64:67], v[108:111]\n"
"v_exp_f32 v133, v133\n"
"v_mfma_f32_16x16x32_bf16 v[116:119], a[232:235], a[72:75], v[116:119]\n"
"ds_read_b128 a[232:235], v240 offset:128\n"
"v_fma_f32 v135, v135, s92, v157\n"
"v_mfma_f32_16x16x32_bf16 v[116:119], a[236:239], a[104:107], v[116:119]\n"
"s_waitcnt lgkmcnt(3)\n"
"buffer_load_dwordx4 v248, s[48:51], s80 offen offset:3072 nt lds\n"
"v_mfma_f32_16x16x32_bf16 v[100:103], a[240:243], a[112:115], v[100:103]\n"
"s_add_u32 m0, s79, 0xc2a0\n"
"s_add_u32 s80, s97, 0x0\n"
"ds_read_b128 a[236:239], v241 offset:128\n"
"v_mfma_f32_16x16x32_bf16 v[100:103], a[244:247], a[144:147], v[100:103]\n"
"v_exp_f32 v134, v134\n"
"v_mfma_f32_16x16x32_bf16 v[108:111], a[244:247], a[152:155], v[108:111]\n"
"v_add_u32 v242, 0xfffdf900, v242\n"
"v_fma_f32 v140, v140, s92, v158\n"
"v_mfma_f32_16x16x32_bf16 v[108:111], a[240:243], a[120:123], v[108:111]\n"
"v_exp_f32 v135, v135\n"
"v_mfma_f32_16x16x32_bf16 v[116:119], a[240:243], a[128:131], v[116:119]\n"
"ds_read_b128 a[240:243], v240 offset:256\n"
"v_fma_f32 v141, v141, s92, v158\n"
"v_mfma_f32_16x16x32_bf16 v[116:119], a[244:247], a[160:163], v[116:119]\n"
"ds_read_b128 a[244:247], v241 offset:256\n"
"v_fma_f32 v142, v142, s92, v158\n"
"v_mfma_f32_16x16x32_bf16 v[100:103], a[248:251], a[168:171], v[100:103]\n"
"v_exp_f32 v140, v140\n"
"v_mfma_f32_16x16x32_bf16 v[100:103], a[252:255], a[200:203], v[100:103]\n"
"v_exp_f32 v141, v141\n"
"v_mfma_f32_16x16x32_bf16 v[108:111], a[252:255], a[208:211], v[108:111]\n"
"v_exp_f32 v142, v142\n"
"v_mfma_f32_16x16x32_bf16 v[108:111], a[248:251], a[176:179], v[108:111]\n"
"v_fma_f32 v143, v143, s92, v158\n"
"v_fma_f32 v172, v172, s92, v159\n"
"v_mfma_f32_16x16x32_bf16 v[116:119], a[248:251], a[184:187], v[116:119]\n"
"ds_read_b128 a[248:251], v240 offset:384\n"
"v_fma_f32 v173, v173, s92, v159\n"
"v_mfma_f32_16x16x32_bf16 v[116:119], a[252:255], a[216:219], v[116:119]\n"
"s_waitcnt lgkmcnt(4)\n"
"ds_read_b128 a[252:255], v241 offset:384\n"
"v_fma_f32 v174, v174, s92, v159\n"
"v_mfma_f32_16x16x32_bf16 v[180:183], a[224:227], a[0:3], 0\n"
"v_exp_f32 v143, v143\n"
"v_mfma_f32_16x16x32_bf16 v[180:183], a[228:231], a[32:35], v[180:183]\n"
"v_exp_f32 v172, v172\n"
"v_mfma_f32_16x16x32_bf16 v[188:191], a[228:231], a[40:43], 0\n"
"v_exp_f32 v173, v173\n"
"v_mfma_f32_16x16x32_bf16 v[188:191], a[224:227], a[8:11], v[188:191]\n"
"v_exp_f32 v174, v174\n"
"v_mfma_f32_16x16x32_bf16 v[196:199], a[224:227], a[16:19], 0\n"
"buffer_load_dwordx4 v248, s[48:51], s81 offen offset:0 nt lds\n"
"v_mfma_f32_16x16x32_bf16 v[196:199], a[228:231], a[48:51], v[196:199]\n"
"v_fma_f32 v175, v175, s92, v159\n"
"v_add_f32 v167, v167, v172\n"
"v_mfma_f32_16x16x32_bf16 v[148:151], a[228:231], a[4:7], 0\n"
"ds_read_b128 a[228:231], v243 offset:0\n"
"v_add_f32 v167, v167, v173\n"
"v_mfma_f32_16x16x32_bf16 v[148:151], a[224:227], a[24:27], v[148:151]\n"
"ds_read_b128 a[224:227], v242 offset:0\n"
"v_cvt_pk_bf16_f32 v172, v172, v173\n"
"v_mfma_f32_16x16x32_bf16 v[180:183], a[232:235], a[56:59], v[180:183]\n"
"s_waitcnt lgkmcnt(4)\n"
"v_exp_f32 v175, v175\n"
"v_mfma_f32_16x16x32_bf16 v[180:183], a[236:239], a[88:91], v[180:183]\n"
"v_cvt_pk_bf16_f32 v173, v174, v175\n"
"v_fma_f32 v100, v100, s92, v156\n"
"v_mfma_f32_16x16x32_bf16 v[188:191], a[236:239], a[96:99], v[188:191]\n"
"ds_write_b64 v253, v[172:173] offset:1024\n"
"v_fma_f32 v101, v101, s92, v156\n"
"v_mfma_f32_16x16x32_bf16 v[188:191], a[232:235], a[64:67], v[188:191]\n"
"v_exp_f32 v100, v100\n"
"v_mfma_f32_16x16x32_bf16 v[196:199], a[232:235], a[72:75], v[196:199]\n"
"v_exp_f32 v101, v101\n"
"v_mfma_f32_16x16x32_bf16 v[196:199], a[236:239], a[104:107], v[196:199]\n"
"v_fma_f32 v102, v102, s92, v156\n"
"v_fma_f32 v103, v103, s92, v156\n"
"v_mfma_f32_16x16x32_bf16 v[148:151], a[236:239], a[12:15], v[148:151]\n"
"ds_read_b128 a[236:239], v243 offset:128\n"
"v_fma_f32 v108, v108, s92, v157\n"
"v_mfma_f32_16x16x32_bf16 v[148:151], a[232:235], a[80:83], v[148:151]\n"
"ds_read_b128 a[232:235], v242 offset:128\n"
"v_fma_f32 v109, v109, s92, v157\n"
"v_mfma_f32_16x16x32_bf16 v[180:183], a[240:243], a[112:115], v[180:183]\n"
"v_exp_f32 v102, v102\n"
"v_mfma_f32_16x16x32_bf16 v[180:183], a[244:247], a[144:147], v[180:183]\n"
"v_exp_f32 v103, v103\n"
"v_mfma_f32_16x16x32_bf16 v[188:191], a[244:247], a[152:155], v[188:191]\n"
"v_exp_f32 v108, v108\n"
"v_mfma_f32_16x16x32_bf16 v[188:191], a[240:243], a[120:123], v[188:191]\n"
"v_exp_f32 v109, v109\n"
"v_mfma_f32_16x16x32_bf16 v[196:199], a[240:243], a[128:131], v[196:199]\n"
"v_fma_f32 v110, v110, s92, v157\n"
"v_fma_f32 v111, v111, s92, v157\n"
"v_mfma_f32_16x16x32_bf16 v[196:199], a[244:247], a[160:163], v[196:199]\n"
"v_exp_f32 v110, v110\n"
"v_mfma_f32_16x16x32_bf16 v[148:151], a[244:247], a[20:23], v[148:151]\n"
"ds_read_b128 a[244:247], v243 offset:256\n"
"v_fma_f32 v116, v116, s92, v158\n"
"v_mfma_f32_16x16x32_bf16 v[148:151], a[240:243], a[136:139], v[148:151]\n"
"s_waitcnt lgkmcnt(3)\n"
"buffer_load_dwordx4 v248, s[48:51], s81 offen offset:1024 nt lds\n"
"v_mfma_f32_16x16x32_bf16 v[180:183], a[248:251], a[168:171], v[180:183]\n"
"s_addk_i32 m0, 0x20\n"
"ds_read_b128 a[240:243], v242 offset:256\n"
"v_fma_f32 v117, v117, s92, v158\n"
"v_mfma_f32_16x16x32_bf16 v[180:183], a[252:255], a[200:203], v[180:183]\n"
"v_exp_f32 v111, v111\n"
"v_mfma_f32_16x16x32_bf16 v[188:191], a[252:255], a[208:211], v[188:191]\n"
"v_exp_f32 v116, v116\n"
"v_mfma_f32_16x16x32_bf16 v[188:191], a[248:251], a[176:179], v[188:191]\n"
"v_exp_f32 v117, v117\n"
"v_mfma_f32_16x16x32_bf16 v[196:199], a[248:251], a[184:187], v[196:199]\n"
"v_fma_f32 v118, v118, s92, v158\n"
"v_fma_f32 v119, v119, s92, v158\n"
"v_mfma_f32_16x16x32_bf16 v[196:199], a[252:255], a[216:219], v[196:199]\n"
"v_exp_f32 v118, v118\n"
"v_mfma_f32_16x16x32_bf16 v[148:151], a[252:255], a[28:31], v[148:151]\n"
"ds_read_b128 a[252:255], v243 offset:384\n"
"v_fma_f32 v180, v180, s92, v156\n"
"v_mfma_f32_16x16x32_bf16 v[148:151], a[248:251], a[192:195], v[148:151]\n"
"ds_read_b128 a[248:251], v242 offset:384\n"
"v_fma_f32 v181, v181, s92, v156\n"
"v_mfma_f32_16x16x32_bf16 v[60:63], a[224:227], a[0:3], 0\n"
"v_exp_f32 v119, v119\n"
"v_mfma_f32_16x16x32_bf16 v[60:63], a[228:231], a[32:35], v[60:63]\n"
"v_exp_f32 v180, v180\n"
"v_mfma_f32_16x16x32_bf16 v[68:71], a[228:231], a[40:43], 0\n"
"v_exp_f32 v181, v181\n"
"v_mfma_f32_16x16x32_bf16 v[68:71], a[224:227], a[8:11], v[68:71]\n"
"v_fma_f32 v182, v182, s92, v156\n"
"v_fma_f32 v183, v183, s92, v156\n"
"v_mfma_f32_16x16x32_bf16 v[76:79], a[224:227], a[16:19], 0\n"
"s_waitcnt vmcnt(6) lgkmcnt(0)\n"
"s_barrier\n"
"v_mfma_f32_16x16x32_bf16 v[76:79], a[228:231], a[48:51], v[76:79]\n"
"buffer_load_dwordx4 v248, s[48:51], s81 offen offset:2048 nt lds\n"
"v_mfma_f32_16x16x32_bf16 v[60:63], a[232:235], a[56:59], v[60:63]\n"
"ds_read_b64_tr_b16 a[224:225], v244 offset:0\n"
"v_fma_f32 v188, v188, s92, v157\n"
"v_mfma_f32_16x16x32_bf16 v[60:63], a[236:239], a[88:91], v[60:63]\n"
"ds_read_b64_tr_b16 a[226:227], v246 offset:0\n"
"v_fma_f32 v189, v189, s92, v157\n"
"v_mfma_f32_16x16x32_bf16 v[68:71], a[236:239], a[96:99], v[68:71]\n"
"ds_read_b64_tr_b16 a[228:229], v224 offset:16608\n"
"v_fma_f32 v190, v190, s92, v157\n"
"v_mfma_f32_16x16x32_bf16 v[68:71], a[232:235], a[64:67], v[68:71]\n"
"ds_read_b64_tr_b16 a[230:231], v226 offset:16608\n"
"v_fma_f32 v191, v191, s92, v157\n"
"v_mfma_f32_16x16x32_bf16 v[76:79], a[232:235], a[72:75], v[76:79]\n"
"ds_read_b64 v[30:31], v254 offset:0\n"
"v_fma_f32 v196, v196, s92, v158\n"
"v_mfma_f32_16x16x32_bf16 v[76:79], a[236:239], a[104:107], v[76:79]\n"
"ds_read_b64 v[94:95], v254 offset:512\n"
"v_fma_f32 v197, v197, s92, v158\n"
"v_mfma_f32_16x16x32_bf16 v[60:63], a[240:243], a[112:115], v[60:63]\n"
"ds_read_b64_tr_b16 a[232:233], v244 offset:32\n"
"v_fma_f32 v198, v198, s92, v158\n"
"v_mfma_f32_16x16x32_bf16 v[60:63], a[244:247], a[144:147], v[60:63]\n"
"ds_read_b64_tr_b16 a[234:235], v246 offset:32\n"
"v_fma_f32 v199, v199, s92, v158\n"
"v_mfma_f32_16x16x32_bf16 v[68:71], a[244:247], a[152:155], v[68:71]\n"
"ds_read_b64_tr_b16 a[236:237], v224 offset:16640\n"
"v_fma_f32 v148, v148, s92, v159\n"
"v_mfma_f32_16x16x32_bf16 v[68:71], a[240:243], a[120:123], v[68:71]\n"
"ds_read_b64_tr_b16 a[238:239], v226 offset:16640\n"
"v_fma_f32 v149, v149, s92, v159\n"
"v_mfma_f32_16x16x32_bf16 v[76:79], a[240:243], a[128:131], v[76:79]\n"
"ds_read_b64_tr_b16 a[240:241], v245 offset:0\n"
"v_fma_f32 v150, v150, s92, v159\n"
"v_mfma_f32_16x16x32_bf16 v[76:79], a[244:247], a[160:163], v[76:79]\n"
"ds_read_b64_tr_b16 a[242:243], v247 offset:0\n"
"v_fma_f32 v151, v151, s92, v159\n"
"v_mfma_f32_16x16x32_bf16 v[60:63], a[248:251], a[168:171], v[60:63]\n"
"ds_read_b64_tr_b16 a[244:245], v225 offset:16608\n"
"v_add_f32 v167, v167, v174\n"
"v_mfma_f32_16x16x32_bf16 v[60:63], a[252:255], a[200:203], v[60:63]\n"
"ds_read_b64_tr_b16 a[246:247], v227 offset:16608\n"
"v_add_f32 v167, v167, v175\n"
"v_mfma_f32_16x16x32_bf16 v[68:71], a[252:255], a[208:211], v[68:71]\n"
"v_exp_f32 v182, v182\n"
"v_mfma_f32_16x16x32_bf16 v[68:71], a[248:251], a[176:179], v[68:71]\n"
"v_exp_f32 v183, v183\n"
"v_mfma_f32_16x16x32_bf16 v[76:79], a[248:251], a[184:187], v[76:79]\n"
"ds_read_b64_tr_b16 a[248:249], v245 offset:32\n"
"v_add_f32 v164, v164, v124\n"
"v_mfma_f32_16x16x32_bf16 v[76:79], a[252:255], a[216:219], v[76:79]\n"
"s_waitcnt lgkmcnt(9)\n"
"ds_read_b64_tr_b16 a[252:253], v225 offset:16640\n"
"v_fma_f32 v60, v60, s92, v156\n"
"v_mfma_f32_16x16x32_bf16 v[0:3], a[224:227], v[4:7], v[0:3]\n"
"ds_read_b64_tr_b16 a[254:255], v227 offset:16640\n"
"v_fma_f32 v61, v61, s92, v156\n"
"v_mfma_f32_16x16x32_bf16 v[0:3], a[228:231], v[36:39], v[0:3]\n"
"ds_read_b64_tr_b16 a[250:251], v247 offset:32\n"
"v_fma_f32 v62, v62, s92, v156\n"
"v_mfma_f32_16x16x32_bf16 v[8:11], a[228:231], v[44:47], v[8:11]\n"
"v_exp_f32 v188, v188\n"
"v_mfma_f32_16x16x32_bf16 v[8:11], a[224:227], v[12:15], v[8:11]\n"
"v_exp_f32 v189, v189\n"
"v_mfma_f32_16x16x32_bf16 v[16:19], a[224:227], v[20:23], v[16:19]\n"
"v_exp_f32 v190, v190\n"
"v_mfma_f32_16x16x32_bf16 v[16:19], a[228:231], v[52:55], v[16:19]\n"
"buffer_load_dwordx4 v248, s[48:51], s81 offen offset:3072 nt lds\n"
"v_mfma_f32_16x16x32_bf16 v[24:27], a[228:231], v[92:95], v[24:27]\n"
"s_add_u32 m0, s79, 0x10380\n"
"ds_read_b64_tr_b16 a[228:229], v224 offset:16736\n"
"v_fma_f32 v63, v63, s92, v156\n"
"v_mfma_f32_16x16x32_bf16 v[24:27], a[224:227], v[28:31], v[24:27]\n"
"s_waitcnt lgkmcnt(5)\n"
"ds_read_b64_tr_b16 a[230:231], v226 offset:16736\n"
"v_fma_f32 v68, v68, s92, v157\n"
"v_mfma_f32_16x16x32_bf16 v[220:223], a[232:235], v[28:31], v[220:223]\n"
"s_add_u32 s81, s97, 0x1c000\n"
"ds_read_b64_tr_b16 a[224:225], v244 offset:128\n"
"v_fma_f32 v69, v69, s92, v157\n"
"v_mfma_f32_16x16x32_bf16 v[220:223], a[236:239], v[92:95], v[220:223]\n"
"s_add_u32 s97, s97, 128\n"
"s_cmp_eq_u32 s97, 896\n"
"s_cselect_b32 s97, 0, s97\n"
"ds_read_b64_tr_b16 a[226:227], v246 offset:128\n"
"v_fma_f32 v70, v70, s92, v157\n"
"v_mfma_f32_16x16x32_bf16 v[212:215], a[236:239], v[52:55], v[212:215]\n"
"v_exp_f32 v191, v191\n"
"v_mfma_f32_16x16x32_bf16 v[212:215], a[232:235], v[20:23], v[212:215]\n"
"v_exp_f32 v196, v196\n"
"v_mfma_f32_16x16x32_bf16 v[204:207], a[232:235], v[12:15], v[204:207]\n"
"v_exp_f32 v197, v197\n"
"v_mfma_f32_16x16x32_bf16 v[204:207], a[236:239], v[44:47], v[204:207]\n"
"v_exp_f32 v198, v198\n"
"v_mfma_f32_16x16x32_bf16 v[84:87], a[236:239], v[36:39], v[84:87]\n"
"ds_read_b64_tr_b16 a[236:237], v224 offset:16768\n"
"v_fma_f32 v71, v71, s92, v157\n"
"v_mfma_f32_16x16x32_bf16 v[84:87], a[232:235], v[4:7], v[84:87]\n"
"ds_read_b64_tr_b16 a[232:233], v244 offset:160\n"
"v_fma_f32 v76, v76, s92, v158\n"
"v_mfma_f32_16x16x32_bf16 v[32:35], a[240:243], v[4:7], v[32:35]\n"
"ds_read_b64_tr_b16 a[234:235], v246 offset:160\n"
"v_fma_f32 v77, v77, s92, v158\n"
"v_mfma_f32_16x16x32_bf16 v[32:35], a[244:247], v[36:39], v[32:35]\n"
"ds_read_b64_tr_b16 a[238:239], v226 offset:16768\n"
"v_fma_f32 v78, v78, s92, v158\n"
"v_mfma_f32_16x16x32_bf16 v[40:43], a[244:247], v[44:47], v[40:43]\n"
"v_exp_f32 v199, v199\n"
"v_mfma_f32_16x16x32_bf16 v[40:43], a[240:243], v[12:15], v[40:43]\n"
"v_exp_f32 v148, v148\n"
"v_mfma_f32_16x16x32_bf16 v[48:51], a[240:243], v[20:23], v[48:51]\n"
"ds_read_b64_tr_b16 a[240:241], v245 offset:128\n"
"v_add_f32 v167, v167, v148\n"
"v_mfma_f32_16x16x32_bf16 v[48:51], a[244:247], v[52:55], v[48:51]\n"
"s_waitcnt lgkmcnt(5)\n"
"ds_read_b64_tr_b16 a[242:243], v247 offset:128\n"
"v_fma_f32 v79, v79, s92, v158\n"
"v_mfma_f32_16x16x32_bf16 a[52:55], a[252:255], v[52:55], a[52:55]\n"
"ds_read_b64_tr_b16 a[244:245], v225 offset:16736\n"
"v_add_f32 v164, v164, v125\n"
"v_mfma_f32_16x16x32_bf16 a[52:55], a[248:251], v[20:23], a[52:55]\n"
"ds_read_b64_tr_b16 a[246:247], v227 offset:16736\n"
"v_add_f32 v164, v164, v126\n"
"v_mfma_f32_16x16x32_bf16 a[44:47], a[248:251], v[12:15], a[44:47]\n"
"buffer_load_dwordx4 v249, s[52:55], s80 offen offset:0 lds\n"
"v_mfma_f32_16x16x32_bf16 a[44:47], a[252:255], v[44:47], a[44:47]\n"
"v_exp_f32 v149, v149\n"
"v_mfma_f32_16x16x32_bf16 a[36:39], a[252:255], v[36:39], a[36:39]\n"
"ds_read_b64_tr_b16 a[252:253], v225 offset:16768\n"
"v_add_f32 v167, v167, v149\n"
"v_mfma_f32_16x16x32_bf16 a[36:39], a[248:251], v[4:7], a[36:39]\n"
"ds_read_b64_tr_b16 a[254:255], v227 offset:16768\n"
"v_cvt_pk_bf16_f32 v148, v148, v149\n"
"v_mfma_f32_16x16x32_bf16 v[56:59], a[224:227], v[4:7], v[56:59]\n"
"ds_read_b64_tr_b16 a[248:249], v245 offset:160\n"
"v_add_f32 v165, v165, v132\n"
"v_mfma_f32_16x16x32_bf16 v[56:59], a[228:231], v[36:39], v[56:59]\n"
"buffer_load_dwordx4 v250, s[52:55], s80 offen offset:1024 lds\n"
"v_mfma_f32_16x16x32_bf16 v[64:67], a[228:231], v[44:47], v[64:67]\n"
"s_addk_i32 m0, 0x820\n"
"ds_read_b64_tr_b16 a[250:251], v247 offset:160\n"
"v_add_f32 v164, v164, v127\n"
"v_mfma_f32_16x16x32_bf16 v[64:67], a[224:227], v[12:15], v[64:67]\n"
"v_exp_f32 v150, v150\n"
"v_mfma_f32_16x16x32_bf16 v[72:75], a[224:227], v[20:23], v[72:75]\n"
"v_exp_f32 v151, v151\n"
"v_mfma_f32_16x16x32_bf16 v[72:75], a[228:231], v[52:55], v[72:75]\n"
"v_cvt_pk_bf16_f32 v149, v150, v151\n"
"v_add_f32 v165, v165, v133\n"
"v_mfma_f32_16x16x32_bf16 v[80:83], a[228:231], v[92:95], v[80:83]\n"
"buffer_load_dwordx4 v251, s[52:55], s80 offen offset:0 lds\n"
"v_mfma_f32_16x16x32_bf16 v[80:83], a[224:227], v[28:31], v[80:83]\n"
"s_waitcnt lgkmcnt(4)\n"
"ds_read_b64_tr_b16 a[228:229], v224 offset:16864\n"
"v_add_f32 v164, v164, v100\n"
"v_mfma_f32_16x16x32_bf16 a[84:87], a[232:235], v[28:31], a[84:87]\n"
"ds_read_b64_tr_b16 a[224:225], v244 offset:256\n"
"v_add_f32 v165, v165, v134\n"
"v_mfma_f32_16x16x32_bf16 a[84:87], a[236:239], v[92:95], a[84:87]\n"
"ds_read_b64_tr_b16 a[226:227], v246 offset:256\n"
"v_add_f32 v164, v164, v101\n"
"v_mfma_f32_16x16x32_bf16 a[76:79], a[236:239], v[52:55], a[76:79]\n"
"ds_read_b64_tr_b16 a[230:231], v226 offset:16864\n"
"v_add_f32 v165, v165, v135\n"
"v_mfma_f32_16x16x32_bf16 a[76:79], a[232:235], v[20:23], a[76:79]\n"
"buffer_load_dwordx4 v252, s[52:55], s80 offen offset:1024 lds\n"
"v_mfma_f32_16x16x32_bf16 a[68:71], a[232:235], v[12:15], a[68:71]\n"
"s_add_u32 m0, s79, 0x18540\n"
"ds_write_b64 v253, v[148:149] offset:1536\n"
"v_add_f32 v164, v164, v102\n"
"v_mfma_f32_16x16x32_bf16 a[68:71], a[236:239], v[44:47], a[68:71]\n"
"s_add_u32 s80, s97, 0x0\n"
"v_exp_f32 v60, v60\n"
"v_mfma_f32_16x16x32_bf16 a[60:63], a[236:239], v[36:39], a[60:63]\n"
"ds_read_b64_tr_b16 a[236:237], v224 offset:16896\n"
"v_add_f32 v165, v165, v108\n"
"v_mfma_f32_16x16x32_bf16 a[60:63], a[232:235], v[4:7], a[60:63]\n"
"ds_read_b64_tr_b16 a[232:233], v244 offset:288\n"
"v_add_f32 v164, v164, v103\n"
"v_mfma_f32_16x16x32_bf16 v[88:91], a[240:243], v[4:7], v[88:91]\n"
"ds_read_b64_tr_b16 a[234:235], v246 offset:288\n"
"v_add_f32 v165, v165, v109\n"
"v_mfma_f32_16x16x32_bf16 v[88:91], a[244:247], v[36:39], v[88:91]\n"
"ds_read_b64_tr_b16 a[238:239], v226 offset:16896\n"
"v_add_f32 v167, v167, v150\n"
"v_mfma_f32_16x16x32_bf16 v[96:99], a[244:247], v[44:47], v[96:99]\n"
"v_exp_f32 v61, v61\n"
"v_mfma_f32_16x16x32_bf16 v[96:99], a[240:243], v[12:15], v[96:99]\n"
"v_exp_f32 v62, v62\n"
"v_mfma_f32_16x16x32_bf16 v[104:107], a[240:243], v[20:23], v[104:107]\n"
"ds_read_b64_tr_b16 a[240:241], v245 offset:256\n"
"v_add_f32 v165, v165, v110\n"
"v_mfma_f32_16x16x32_bf16 v[104:107], a[244:247], v[52:55], v[104:107]\n"
"s_waitcnt lgkmcnt(7)\n"
"ds_read_b64_tr_b16 a[242:243], v247 offset:256\n"
"v_add_f32 v166, v166, v140\n"
"v_mfma_f32_16x16x32_bf16 a[108:111], a[252:255], v[52:55], a[108:111]\n"
"ds_read_b64_tr_b16 a[244:245], v225 offset:16864\n"
"v_add_f32 v164, v164, v180\n"
"v_mfma_f32_16x16x32_bf16 a[108:111], a[248:251], v[20:23], a[108:111]\n"
"ds_read_b64_tr_b16 a[246:247], v227 offset:16864\n"
"v_add_f32 v167, v167, v151\n"
"v_mfma_f32_16x16x32_bf16 a[100:103], a[248:251], v[12:15], a[100:103]\n"
"v_exp_f32 v63, v63\n"
"v_mfma_f32_16x16x32_bf16 a[100:103], a[252:255], v[44:47], a[100:103]\n"
"v_exp_f32 v68, v68\n"
"v_mfma_f32_16x16x32_bf16 a[92:95], a[252:255], v[36:39], a[92:95]\n"
"ds_read_b64_tr_b16 a[252:253], v225 offset:16896\n"
"v_cvt_pk_bf16_f32 v124, v124, v125\n"
"v_mfma_f32_16x16x32_bf16 a[92:95], a[248:251], v[4:7], a[92:95]\n"
"ds_read_b64_tr_b16 a[254:255], v227 offset:16896\n"
"v_cvt_pk_bf16_f32 v125, v126, v127\n"
"v_mfma_f32_16x16x32_bf16 v[112:115], a[224:227], v[4:7], v[112:115]\n"
"s_waitcnt lgkmcnt(7)\n"
"ds_read_b64_tr_b16 a[248:249], v245 offset:288\n"
"v_add_f32 v165, v165, v111\n"
"v_mfma_f32_16x16x32_bf16 v[112:115], a[228:231], v[36:39], v[112:115]\n"
"ds_read_b64_tr_b16 a[250:251], v247 offset:288\n"
"v_add_f32 v166, v166, v141\n"
"v_mfma_f32_16x16x32_bf16 v[120:123], a[228:231], v[44:47], v[120:123]\n"
"v_exp_f32 v69, v69\n"
"v_mfma_f32_16x16x32_bf16 v[120:123], a[224:227], v[12:15], v[120:123]\n"
"v_exp_f32 v70, v70\n"
"v_mfma_f32_16x16x32_bf16 v[128:131], a[224:227], v[20:23], v[128:131]\n"
"v_exp_f32 v71, v71\n"
"v_mfma_f32_16x16x32_bf16 v[128:131], a[228:231], v[52:55], v[128:131]\n"
"v_exp_f32 v76, v76\n"
"v_mfma_f32_16x16x32_bf16 v[136:139], a[228:231], v[92:95], v[136:139]\n"
"ds_read_b64_tr_b16 a[228:229], v224 offset:16992\n"
"v_add_f32 v164, v164, v181\n"
"v_mfma_f32_16x16x32_bf16 v[136:139], a[224:227], v[28:31], v[136:139]\n"
"ds_read_b64_tr_b16 a[224:225], v244 offset:384\n"
"v_cvt_pk_bf16_f32 v126, v100, v101\n"
"v_mfma_f32_16x16x32_bf16 a[140:143], a[232:235], v[28:31], a[140:143]\n"
"s_waitcnt lgkmcnt(6)\n"
"ds_read_b64_tr_b16 a[226:227], v246 offset:384\n"
"v_cvt_pk_bf16_f32 v127, v102, v103\n"
"v_mfma_f32_16x16x32_bf16 a[140:143], a[236:239], v[92:95], a[140:143]\n"
"ds_read_b64_tr_b16 a[230:231], v226 offset:16992\n"
"v_add_f32 v166, v166, v142\n"
"v_mfma_f32_16x16x32_bf16 a[132:135], a[236:239], v[52:55], a[132:135]\n"
"v_exp_f32 v77, v77\n"
"v_mfma_f32_16x16x32_bf16 a[132:135], a[232:235], v[20:23], a[132:135]\n"
"v_exp_f32 v78, v78\n"
"v_mfma_f32_16x16x32_bf16 a[124:127], a[232:235], v[12:15], a[124:127]\n"
"v_exp_f32 v79, v79\n"
"v_mfma_f32_16x16x32_bf16 a[124:127], a[236:239], v[44:47], a[124:127]\n"
"v_add_f32 v164, v164, v182\n"
"v_add_f32 v165, v165, v188\n"
"v_mfma_f32_16x16x32_bf16 a[116:119], a[236:239], v[36:39], a[116:119]\n"
"ds_read_b64_tr_b16 a[236:237], v224 offset:17024\n"
"v_cvt_pk_bf16_f32 v180, v180, v181\n"
"v_mfma_f32_16x16x32_bf16 a[116:119], a[232:235], v[4:7], a[116:119]\n"
"ds_read_b64_tr_b16 a[232:233], v244 offset:416\n"
"v_add_f32 v166, v166, v143\n"
"v_mfma_f32_16x16x32_bf16 v[144:147], a[240:243], v[4:7], v[144:147]\n"
"ds_read_b64_tr_b16 a[234:235], v246 offset:416\n"
"v_add_f32 v164, v164, v183\n"
"v_mfma_f32_16x16x32_bf16 v[144:147], a[244:247], v[36:39], v[144:147]\n"
"ds_read_b64_tr_b16 a[238:239], v226 offset:17024\n"
"v_cvt_pk_bf16_f32 v181, v182, v183\n"
"v_mfma_f32_16x16x32_bf16 v[152:155], a[244:247], v[44:47], v[152:155]\n"
"v_add_f32 v165, v165, v189\n"
"v_cvt_pk_bf16_f32 v182, v60, v61\n"
"v_mfma_f32_16x16x32_bf16 v[152:155], a[240:243], v[12:15], v[152:155]\n"
"v_cvt_pk_bf16_f32 v183, v62, v63\n"
"v_add_f32 v166, v166, v116\n"
"v_mfma_f32_16x16x32_bf16 v[160:163], a[240:243], v[20:23], v[160:163]\n"
"ds_read_b64_tr_b16 a[240:241], v245 offset:384\n"
"v_add_f32 v165, v165, v190\n"
"v_mfma_f32_16x16x32_bf16 v[160:163], a[244:247], v[52:55], v[160:163]\n"
"s_waitcnt lgkmcnt(5)\n"
"ds_read_b64_tr_b16 a[242:243], v247 offset:384\n"
"v_cvt_pk_bf16_f32 v188, v188, v189\n"
"v_mfma_f32_16x16x32_bf16 a[164:167], a[252:255], v[52:55], a[164:167]\n"
"ds_read_b64_tr_b16 a[244:245], v225 offset:16992\n"
"v_add_f32 v166, v166, v117\n"
"v_mfma_f32_16x16x32_bf16 a[164:167], a[248:251], v[20:23], a[164:167]\n"
"ds_read_b64_tr_b16 a[246:247], v227 offset:16992\n"
"v_add_f32 v165, v165, v191\n"
"v_mfma_f32_16x16x32_bf16 a[156:159], a[248:251], v[12:15], a[156:159]\n"
"v_cvt_pk_bf16_f32 v189, v190, v191\n"
"v_add_f32 v166, v166, v118\n"
"v_mfma_f32_16x16x32_bf16 a[156:159], a[252:255], v[44:47], a[156:159]\n"
"v_cvt_pk_bf16_f32 v190, v68, v69\n"
"v_cvt_pk_bf16_f32 v191, v70, v71\n"
"v_mfma_f32_16x16x32_bf16 a[148:151], a[252:255], v[36:39], a[148:151]\n"
"ds_read_b64_tr_b16 a[252:253], v225 offset:17024\n"
"v_cvt_pk_bf16_f32 v132, v132, v133\n"
"v_mfma_f32_16x16x32_bf16 a[148:151], a[248:251], v[4:7], a[148:151]\n"
"ds_read_b64_tr_b16 a[254:255], v227 offset:17024\n"
"v_cvt_pk_bf16_f32 v133, v134, v135\n"
"v_mfma_f32_16x16x32_bf16 v[168:171], a[224:227], v[4:7], v[168:171]\n"
"ds_read_b64_tr_b16 a[248:249], v245 offset:416\n"
"v_add_f32 v166, v166, v119\n"
"v_mfma_f32_16x16x32_bf16 v[168:171], a[228:231], v[36:39], v[168:171]\n"
"ds_read_b64_tr_b16 a[250:251], v247 offset:416\n"
"v_cvt_pk_bf16_f32 v134, v108, v109\n"
"v_mfma_f32_16x16x32_bf16 v[176:179], a[228:231], v[44:47], v[176:179]\n"
"v_cvt_pk_bf16_f32 v135, v110, v111\n"
"v_add_f32 v166, v166, v196\n"
"v_mfma_f32_16x16x32_bf16 v[176:179], a[224:227], v[12:15], v[176:179]\n"
"v_cvt_pk_bf16_f32 v140, v140, v141\n"
"v_add_f32 v166, v166, v197\n"
"v_mfma_f32_16x16x32_bf16 v[184:187], a[224:227], v[20:23], v[184:187]\n"
"v_cvt_pk_bf16_f32 v141, v142, v143\n"
"v_add_f32 v166, v166, v198\n"
"v_mfma_f32_16x16x32_bf16 v[184:187], a[228:231], v[52:55], v[184:187]\n"
"v_cvt_pk_bf16_f32 v142, v116, v117\n"
"v_cvt_pk_bf16_f32 v143, v118, v119\n"
"v_mfma_f32_16x16x32_bf16 v[192:195], a[228:231], v[92:95], v[192:195]\n"
"s_waitcnt vmcnt(4) lgkmcnt(0)\n"
"s_barrier\n"
"v_mfma_f32_16x16x32_bf16 v[192:195], a[224:227], v[28:31], v[192:195]\n"
"ds_read_b64_tr_b16 a[228:229], v224 offset:33216\n"
"v_cvt_pk_bf16_f32 v196, v196, v197\n"
"v_mfma_f32_16x16x32_bf16 a[196:199], a[232:235], v[28:31], a[196:199]\n"
"ds_read_b64_tr_b16 a[224:225], v244 offset:33216\n"
"v_add_f32 v166, v166, v199\n"
"v_mfma_f32_16x16x32_bf16 a[196:199], a[236:239], v[92:95], a[196:199]\n"
"ds_read_b64_tr_b16 a[226:227], v246 offset:33216\n"
"v_cvt_pk_bf16_f32 v197, v198, v199\n"
"v_mfma_f32_16x16x32_bf16 a[188:191], a[236:239], v[52:55], a[188:191]\n"
"ds_read_b64_tr_b16 a[230:231], v226 offset:33216\n"
"v_cvt_pk_bf16_f32 v198, v76, v77\n"
"v_mfma_f32_16x16x32_bf16 a[188:191], a[232:235], v[20:23], a[188:191]\n"
"ds_read_b64 v[174:175], v254 offset:1024\n"
"v_cvt_pk_bf16_f32 v199, v78, v79\n"
"v_mfma_f32_16x16x32_bf16 a[180:183], a[232:235], v[12:15], a[180:183]\n"
"ds_read_b64 v[150:151], v254 offset:1536\n"
"v_add_f32 v164, v164, v60\n"
"v_mfma_f32_16x16x32_bf16 a[180:183], a[236:239], v[44:47], a[180:183]\n"
"v_add_f32 v164, v164, v61\n"
"v_add_f32 v165, v165, v68\n"
"v_mfma_f32_16x16x32_bf16 a[172:175], a[236:239], v[36:39], a[172:175]\n"
"ds_read_b64_tr_b16 a[236:237], v224 offset:33248\n"
"v_add_f32 v164, v164, v62\n"
"v_mfma_f32_16x16x32_bf16 a[172:175], a[232:235], v[4:7], a[172:175]\n"
"buffer_load_dwordx4 v249, s[52:55], s81 offen offset:0 lds\n"
"v_mfma_f32_16x16x32_bf16 v[200:203], a[240:243], v[4:7], v[200:203]\n"
"ds_read_b64_tr_b16 a[232:233], v244 offset:33248\n"
"v_add_f32 v164, v164, v63\n"
"v_mfma_f32_16x16x32_bf16 v[200:203], a[244:247], v[36:39], v[200:203]\n"
"ds_read_b64_tr_b16 a[234:235], v246 offset:33248\n"
"v_add_f32 v165, v165, v69\n"
"v_mfma_f32_16x16x32_bf16 v[208:211], a[244:247], v[44:47], v[208:211]\n"
"ds_read_b64_tr_b16 a[238:239], v226 offset:33248\n"
"v_add_f32 v165, v165, v70\n"
"v_mfma_f32_16x16x32_bf16 v[208:211], a[240:243], v[12:15], v[208:211]\n"
"v_add_f32 v166, v166, v76\n"
"v_add_f32 v165, v165, v71\n"
"v_mfma_f32_16x16x32_bf16 v[216:219], a[240:243], v[20:23], v[216:219]\n"
"buffer_load_dwordx4 v250, s[52:55], s81 offen offset:1024 lds\n"
"v_mfma_f32_16x16x32_bf16 v[216:219], a[244:247], v[52:55], v[216:219]\n"
"s_addk_i32 m0, 0x820\n"
"ds_read_b64_tr_b16 a[240:241], v244 offset:33344\n"
"v_add_f32 v166, v166, v77\n"
"v_mfma_f32_16x16x32_bf16 v[236:239], a[252:255], v[52:55], v[236:239]\n"
"PG12_%=:\n"
"s_cbranch_execz PGB2_%=\n"
"ds_read_b64_tr_b16 a[242:243], v246 offset:33344\n"
"v_add_f32 v166, v166, v78\n"
"v_mfma_f32_16x16x32_bf16 v[236:239], a[248:251], v[20:23], v[236:239]\n"
"ds_read_b64_tr_b16 a[244:245], v224 offset:33344\n"
"v_add_f32 v166, v166, v79\n"
"v_mfma_f32_16x16x32_bf16 v[232:235], a[248:251], v[12:15], v[232:235]\n"
"ds_read_b64_tr_b16 a[246:247], v226 offset:33344\n"
"v_bfe_u32 v12, v248, 4, 6\n"
"v_mfma_f32_16x16x32_bf16 v[232:235], a[252:255], v[44:47], v[232:235]\n"
"buffer_load_dwordx4 v251, s[52:55], s81 offen offset:0 lds\n"
"v_mfma_f32_16x16x32_bf16 v[228:231], a[252:255], v[36:39], v[228:231]\n"
"ds_read_b64_tr_b16 a[252:253], v224 offset:33376\n"
"v_lshl_add_u32 v12, v12, 4, s86\n"
"v_mfma_f32_16x16x32_bf16 v[228:231], a[248:251], v[4:7], v[228:231]\n"
"s_waitcnt lgkmcnt(8)\n"
"ds_read_b64_tr_b16 a[248:249], v244 offset:33376\n"
"v_mov_b32 v4, v164\n"
"v_mfma_f32_16x16x32_bf16 v[0:3], a[224:227], v[124:127], v[0:3]\n"
"s_xor_b32 s86, s31, 1\n"
"s_lshl_b32 s86, s86, 13\n"
"ds_read_b64_tr_b16 a[250:251], v246 offset:33376\n"
"v_mov_b32 v5, v165\n"
"v_mfma_f32_16x16x32_bf16 v[0:3], a[228:231], v[180:183], v[0:3]\n"
"ds_read_b64_tr_b16 a[254:255], v226 offset:33376\n"
"v_mov_b32 v6, v166\n"
"v_mfma_f32_16x16x32_bf16 v[8:11], a[228:231], v[188:191], v[8:11]\n"
"buffer_load_dwordx4 v252, s[52:55], s81 offen offset:1024 lds\n"
"v_mfma_f32_16x16x32_bf16 v[8:11], a[224:227], v[132:135], v[8:11]\n"
"s_add_u32 m0, s79, 0x0\n"
"v_permlane16_swap_b32 v164, v4\n"
"v_mfma_f32_16x16x32_bf16 v[16:19], a[224:227], v[140:143], v[16:19]\n"
"s_add_u32 s81, s97, 0x1c000\n"
"v_permlane16_swap_b32 v165, v5\n"
"v_mfma_f32_16x16x32_bf16 v[16:19], a[228:231], v[196:199], v[16:19]\n"
"s_add_u32 s97, s97, 128\n"
"s_cmp_eq_u32 s97, 896\n"
"s_cselect_b32 s97, 0, s97\n"
"v_permlane16_swap_b32 v166, v6\n"
"v_mfma_f32_16x16x32_bf16 v[24:27], a[228:231], v[148:151], v[24:27]\n"
"ds_read_b64_tr_b16 a[228:229], v225 offset:33216\n"
"v_mov_b32 v7, v167\n"
"v_mfma_f32_16x16x32_bf16 v[24:27], a[224:227], v[172:175], v[24:27]\n"
"s_waitcnt lgkmcnt(6)\n"
"ds_read_b64_tr_b16 a[224:225], v245 offset:33216\n"
"v_add_f32 v164, v164, v4\n"
"v_mfma_f32_16x16x32_bf16 v[220:223], a[232:235], v[172:175], v[220:223]\n"
"ds_read_b64_tr_b16 a[226:227], v247 offset:33216\n"
"v_mov_b32 v4, v164\n"
"v_mfma_f32_16x16x32_bf16 v[220:223], a[236:239], v[148:151], v[220:223]\n"
"ds_read_b64_tr_b16 a[230:231], v227 offset:33216\n"
"v_add_f32 v165, v165, v5\n"
"v_mfma_f32_16x16x32_bf16 v[212:215], a[236:239], v[196:199], v[212:215]\n"
"v_mov_b32 v5, v165\n"
"v_add_f32 v166, v166, v6\n"
"v_mfma_f32_16x16x32_bf16 v[212:215], a[232:235], v[140:143], v[212:215]\n"
"v_permlane16_swap_b32 v167, v7\n"
"v_mfma_f32_16x16x32_bf16 v[204:207], a[232:235], v[132:135], v[204:207]\n"
"v_permlane32_swap_b32 v164, v4\n"
"v_mfma_f32_16x16x32_bf16 v[204:207], a[236:239], v[188:191], v[204:207]\n"
"v_permlane32_swap_b32 v165, v5\n"
"v_mfma_f32_16x16x32_bf16 v[84:87], a[236:239], v[180:183], v[84:87]\n"
"ds_read_b64_tr_b16 a[236:237], v225 offset:33248\n"
"v_mov_b32 v6, v166\n"
"v_mfma_f32_16x16x32_bf16 v[84:87], a[232:235], v[124:127], v[84:87]\n"
"ds_read_b64_tr_b16 a[238:239], v227 offset:33248\n"
"v_add_f32 v167, v167, v7\n"
"v_mfma_f32_16x16x32_bf16 v[56:59], a[240:243], v[124:127], v[56:59]\n"
"s_waitcnt lgkmcnt(6)\n"
"ds_read_b64_tr_b16 a[232:233], v245 offset:33248\n"
"v_mov_b32 v7, v167\n"
"v_mfma_f32_16x16x32_bf16 v[56:59], a[244:247], v[180:183], v[56:59]\n"
"ds_read_b64_tr_b16 a[234:235], v247 offset:33248\n"
"v_add_f32 v164, v164, v4\n"
"v_mfma_f32_16x16x32_bf16 v[64:67], a[244:247], v[188:191], v[64:67]\n"
"v_permlane32_swap_b32 v166, v6\n"
"v_mfma_f32_16x16x32_bf16 v[64:67], a[240:243], v[132:135], v[64:67]\n"
"v_permlane32_swap_b32 v167, v7\n"
"v_mfma_f32_16x16x32_bf16 v[72:75], a[240:243], v[140:143], v[72:75]\n"
"v_add_f32 v165, v165, v5\n"
"v_add_f32 v166, v166, v6\n"
"v_mfma_f32_16x16x32_bf16 v[72:75], a[244:247], v[196:199], v[72:75]\n"
"v_add_f32 v167, v167, v7\n"
"v_max3_f32 v4, v164, v165, v166\n"
"v_mfma_f32_16x16x32_bf16 v[80:83], a[244:247], v[148:151], v[80:83]\n"
"ds_read_b64_tr_b16 a[244:245], v225 offset:33344\n"
"v_max_f32 v4, v4, v167\n"
"v_mfma_f32_16x16x32_bf16 v[80:83], a[240:243], v[172:175], v[80:83]\n"
"ds_read_b64_tr_b16 a[240:241], v245 offset:33344\n"
"v_cvt_pk_bf16_f32 v24, v24, v25\n"
"v_mfma_f32_16x16x32_bf16 a[84:87], a[248:251], v[172:175], a[84:87]\n"
"ds_read_b64_tr_b16 a[242:243], v247 offset:33344\n"
"v_cvt_pk_bf16_f32 v25, v26, v27\n"
"v_mfma_f32_16x16x32_bf16 a[84:87], a[252:255], v[148:151], a[84:87]\n"
"ds_read_b64_tr_b16 a[246:247], v227 offset:33344\n"
"v_add_u32 v14, 0x40e0, v248\n"
"v_mfma_f32_16x16x32_bf16 a[76:79], a[252:255], v[196:199], a[76:79]\n"
"ds_write_b32 v253, v167 offset:2048\n"
"v_cvt_pk_bf16_f32 v26, v220, v221\n"
"v_mfma_f32_16x16x32_bf16 a[76:79], a[248:251], v[140:143], a[76:79]\n"
"ds_write_b32 v12, v4\n"
"v_cvt_pk_bf16_f32 v27, v222, v223\n"
"v_mfma_f32_16x16x32_bf16 a[68:71], a[248:251], v[132:135], a[68:71]\n"
"ds_write_b128 v14, v[24:27] offset:0\n"
"v_cvt_pk_bf16_f32 v80, v80, v81\n"
"v_mfma_f32_16x16x32_bf16 a[68:71], a[252:255], v[188:191], a[68:71]\n"
"v_cvt_pk_bf16_f32 v81, v82, v83\n"
"v_accvgpr_read_b32 v20, a87\n"
"v_mfma_f32_16x16x32_bf16 a[60:63], a[252:255], v[180:183], a[60:63]\n"
"ds_read_b64_tr_b16 a[252:253], v225 offset:33376\n"
"v_accvgpr_read_b32 v82, a84\n"
"v_mfma_f32_16x16x32_bf16 a[60:63], a[248:251], v[124:127], a[60:63]\n"
"s_waitcnt lgkmcnt(8)\n"
"ds_read_b64_tr_b16 a[254:255], v227 offset:33376\n"
"v_accvgpr_read_b32 v83, a85\n"
"v_mfma_f32_16x16x32_bf16 v[32:35], a[224:227], v[124:127], v[32:35]\n"
"ds_read_b64_tr_b16 a[248:249], v245 offset:33376\n"
"v_cvt_pk_bf16_f32 v82, v82, v83\n"
"v_mfma_f32_16x16x32_bf16 v[32:35], a[228:231], v[180:183], v[32:35]\n"
"ds_read_b64_tr_b16 a[250:251], v247 offset:33376\n"
"v_accvgpr_read_b32 v83, a86\n"
"v_mfma_f32_16x16x32_bf16 v[40:43], a[228:231], v[188:191], v[40:43]\n"
"v_cvt_pk_bf16_f32 v83, v83, v20\n"
"v_add_u32 v240, s93, v244\n"
"v_mfma_f32_16x16x32_bf16 v[40:43], a[224:227], v[132:135], v[40:43]\n"
"ds_write_b128 v14, v[80:83] offset:1024\n"
"v_add_u32 v240, 0xffffbf20, v240\n"
"v_mfma_f32_16x16x32_bf16 v[48:51], a[224:227], v[140:143], v[48:51]\n"
"ds_read_b64_tr_b16 a[224:225], v244 offset:33472\n"
"v_cvt_pk_bf16_f32 v56, v56, v57\n"
"v_mfma_f32_16x16x32_bf16 v[48:51], a[228:231], v[196:199], v[48:51]\n"
"ds_read_b64_tr_b16 a[226:227], v246 offset:33472\n"
"v_cvt_pk_bf16_f32 v0, v0, v1\n"
"v_mfma_f32_16x16x32_bf16 a[52:55], a[236:239], v[196:199], a[52:55]\n"
"ds_read_b64_tr_b16 a[228:229], v224 offset:33472\n"
"v_cvt_pk_bf16_f32 v57, v58, v59\n"
"v_mfma_f32_16x16x32_bf16 a[52:55], a[232:235], v[140:143], a[52:55]\n"
"ds_read_b64_tr_b16 a[230:231], v226 offset:33472\n"
"v_cvt_pk_bf16_f32 v1, v2, v3\n"
"v_mfma_f32_16x16x32_bf16 a[44:47], a[232:235], v[132:135], a[44:47]\n"
"v_accvgpr_read_b32 v58, a60\n"
"v_accvgpr_read_b32 v59, a61\n"
"v_mfma_f32_16x16x32_bf16 a[44:47], a[236:239], v[188:191], a[44:47]\n"
"v_cvt_pk_bf16_f32 v2, v84, v85\n"
"v_cvt_pk_bf16_f32 v3, v86, v87\n"
"v_mfma_f32_16x16x32_bf16 a[36:39], a[236:239], v[180:183], a[36:39]\n"
"ds_read_b64_tr_b16 a[236:237], v224 offset:33504\n"
"v_cvt_pk_bf16_f32 v58, v58, v59\n"
"v_mfma_f32_16x16x32_bf16 a[36:39], a[232:235], v[124:127], a[36:39]\n"
"s_waitcnt lgkmcnt(8)\n"
"ds_read_b64_tr_b16 a[232:233], v244 offset:33504\n"
"v_accvgpr_read_b32 v59, a62\n"
"v_mfma_f32_16x16x32_bf16 v[88:91], a[240:243], v[124:127], v[88:91]\n"
"ds_read_b64_tr_b16 a[234:235], v246 offset:33504\n"
"v_accvgpr_read_b32 v21, a63\n"
"v_mfma_f32_16x16x32_bf16 v[88:91], a[244:247], v[180:183], v[88:91]\n"
"ds_read_b64_tr_b16 a[238:239], v226 offset:33504\n"
"v_cvt_pk_bf16_f32 v64, v64, v65\n"
"v_mfma_f32_16x16x32_bf16 v[96:99], a[244:247], v[188:191], v[96:99]\n"
"v_cvt_pk_bf16_f32 v65, v66, v67\n"
"v_cvt_pk_bf16_f32 v59, v59, v21\n"
"v_mfma_f32_16x16x32_bf16 v[96:99], a[240:243], v[132:135], v[96:99]\n"
"v_accvgpr_read_b32 v66, a68\n"
"v_accvgpr_read_b32 v67, a69\n"
"v_mfma_f32_16x16x32_bf16 v[104:107], a[240:243], v[140:143], v[104:107]\n"
"ds_read_b64_tr_b16 a[240:241], v244 offset:33600\n"
"v_cvt_pk_bf16_f32 v66, v66, v67\n"
"v_mfma_f32_16x16x32_bf16 v[104:107], a[244:247], v[196:199], v[104:107]\n"
"ds_read_b64_tr_b16 a[242:243], v246 offset:33600\n"
"v_accvgpr_read_b32 v67, a70\n"
"v_mfma_f32_16x16x32_bf16 a[108:111], a[252:255], v[196:199], a[108:111]\n"
"s_waitcnt lgkmcnt(7)\n"
"ds_read_b64_tr_b16 a[244:245], v224 offset:33600\n"
"v_accvgpr_read_b32 v22, a71\n"
"v_mfma_f32_16x16x32_bf16 a[108:111], a[248:251], v[140:143], a[108:111]\n"
"ds_read_b64_tr_b16 a[246:247], v226 offset:33600\n"
"v_cvt_pk_bf16_f32 v67, v67, v22\n"
"v_mfma_f32_16x16x32_bf16 a[100:103], a[248:251], v[132:135], a[100:103]\n"
"v_cvt_pk_bf16_f32 v8, v8, v9\n"
"v_cvt_pk_bf16_f32 v72, v72, v73\n"
"v_mfma_f32_16x16x32_bf16 a[100:103], a[252:255], v[188:191], a[100:103]\n"
"v_cvt_pk_bf16_f32 v9, v10, v11\n"
"v_cvt_pk_bf16_f32 v16, v16, v17\n"
"v_mfma_f32_16x16x32_bf16 a[92:95], a[252:255], v[180:183], a[92:95]\n"
"ds_read_b64_tr_b16 a[252:253], v224 offset:33632\n"
"v_cvt_pk_bf16_f32 v10, v204, v205\n"
"v_mfma_f32_16x16x32_bf16 a[92:95], a[248:251], v[124:127], a[92:95]\n"
"ds_read_b64_tr_b16 a[248:249], v244 offset:33632\n"
"v_cvt_pk_bf16_f32 v11, v206, v207\n"
"v_mfma_f32_16x16x32_bf16 v[112:115], a[224:227], v[124:127], v[112:115]\n"
"s_waitcnt lgkmcnt(7)\n"
"ds_read_b64_tr_b16 a[250:251], v246 offset:33632\n"
"v_cvt_pk_bf16_f32 v73, v74, v75\n"
"v_mfma_f32_16x16x32_bf16 v[112:115], a[228:231], v[180:183], v[112:115]\n"
"ds_read_b64_tr_b16 a[254:255], v226 offset:33632\n"
"v_cvt_pk_bf16_f32 v17, v18, v19\n"
"v_mfma_f32_16x16x32_bf16 v[120:123], a[228:231], v[188:191], v[120:123]\n"
"v_accvgpr_read_b32 v74, a76\n"
"v_accvgpr_read_b32 v75, a77\n"
"v_mfma_f32_16x16x32_bf16 v[120:123], a[224:227], v[132:135], v[120:123]\n"
"v_cvt_pk_bf16_f32 v18, v212, v213\n"
"v_cvt_pk_bf16_f32 v19, v214, v215\n"
"v_mfma_f32_16x16x32_bf16 v[128:131], a[224:227], v[140:143], v[128:131]\n"
"v_cvt_pk_bf16_f32 v74, v74, v75\n"
"v_accvgpr_read_b32 v23, a79\n"
"v_mfma_f32_16x16x32_bf16 v[128:131], a[228:231], v[196:199], v[128:131]\n"
"v_accvgpr_read_b32 v75, a78\n"
"v_add_u32 v241, s93, v245\n"
"v_mfma_f32_16x16x32_bf16 v[136:139], a[228:231], v[148:151], v[136:139]\n"
"ds_read_b64_tr_b16 a[228:229], v225 offset:33472\n"
"v_cvt_pk_bf16_f32 v75, v75, v23\n"
"v_mfma_f32_16x16x32_bf16 v[136:139], a[224:227], v[172:175], v[136:139]\n"
"buffer_load_dwordx4 v249, s[52:55], s80 offen offset:0 lds\n"
"v_mfma_f32_16x16x32_bf16 a[140:143], a[232:235], v[172:175], a[140:143]\n"
"s_waitcnt lgkmcnt(5)\n"
"ds_read_b64_tr_b16 a[230:231], v227 offset:33472\n"
"v_add_u32 v241, 0xffffbf20, v241\n"
"v_mfma_f32_16x16x32_bf16 a[140:143], a[236:239], v[148:151], a[140:143]\n"
"ds_read_b64_tr_b16 a[224:225], v245 offset:33472\n"
"v_add_u32 v242, s94, v244\n"
"v_mfma_f32_16x16x32_bf16 a[132:135], a[236:239], v[196:199], a[132:135]\n"
"ds_read_b64_tr_b16 a[226:227], v247 offset:33472\n"
"v_cvt_pk_bf16_f32 v136, v136, v137\n"
"v_mfma_f32_16x16x32_bf16 a[132:135], a[232:235], v[140:143], a[132:135]\n"
"v_cvt_pk_bf16_f32 v137, v138, v139\n"
"v_add_u32 v242, 0xffffbf20, v242\n"
"v_mfma_f32_16x16x32_bf16 a[124:127], a[232:235], v[132:135], a[124:127]\n"
"buffer_load_dwordx4 v250, s[52:55], s80 offen offset:1024 lds\n"
"v_mfma_f32_16x16x32_bf16 a[124:127], a[236:239], v[188:191], a[124:127]\n"
"s_addk_i32 m0, 0x820\n"
"v_accvgpr_read_b32 v138, a140\n"
"v_accvgpr_read_b32 v139, a141\n"
"v_mfma_f32_16x16x32_bf16 a[116:119], a[236:239], v[180:183], a[116:119]\n"
"ds_read_b64_tr_b16 a[236:237], v225 offset:33504\n"
"v_cvt_pk_bf16_f32 v138, v138, v139\n"
"v_mfma_f32_16x16x32_bf16 a[116:119], a[232:235], v[124:127], a[116:119]\n"
"ds_read_b64_tr_b16 a[238:239], v227 offset:33504\n"
"v_accvgpr_read_b32 v139, a142\n"
"v_mfma_f32_16x16x32_bf16 v[168:171], a[240:243], v[124:127], v[168:171]\n"
"ds_read_b64_tr_b16 a[232:233], v245 offset:33504\n"
"v_accvgpr_read_b32 v38, a143\n"
"v_mfma_f32_16x16x32_bf16 v[168:171], a[244:247], v[180:183], v[168:171]\n"
"buffer_load_dwordx4 v251, s[52:55], s80 offen offset:0 lds\n"
"v_mfma_f32_16x16x32_bf16 v[176:179], a[244:247], v[188:191], v[176:179]\n"
"ds_read_b64_tr_b16 a[234:235], v247 offset:33504\n"
"v_cvt_pk_bf16_f32 v139, v139, v38\n"
"v_mfma_f32_16x16x32_bf16 v[176:179], a[240:243], v[132:135], v[176:179]\n"
"ds_write_b128 v14, v[136:139] offset:2048\n"
"v_xor_b32 v15, 0x1000, v248\n"
"v_mfma_f32_16x16x32_bf16 v[184:187], a[240:243], v[140:143], v[184:187]\n"
"v_add_u32 v15, 0x40e0, v15\n"
"v_cvt_pk_bf16_f32 v32, v32, v33\n"
"v_mfma_f32_16x16x32_bf16 v[184:187], a[244:247], v[196:199], v[184:187]\n"
"v_cvt_pk_bf16_f32 v33, v34, v35\n"
"v_accvgpr_read_b32 v28, a39\n"
"v_mfma_f32_16x16x32_bf16 v[192:195], a[244:247], v[148:151], v[192:195]\n"
"buffer_load_dwordx4 v252, s[52:55], s80 offen offset:1024 lds\n"
"v_mfma_f32_16x16x32_bf16 v[192:195], a[240:243], v[172:175], v[192:195]\n"
"s_waitcnt lgkmcnt(5)\n"
"s_add_u32 m0, s79, 0x20700\n"
"ds_read_b64_tr_b16 a[244:245], v225 offset:33600\n"
"v_mfma_f32_16x16x32_bf16 a[196:199], a[248:251], v[172:175], a[196:199]\n"
"s_add_u32 s80, s97, 0x0\n"
"ds_read_b64_tr_b16 a[246:247], v227 offset:33600\n"
"v_accvgpr_read_b32 v34, a36\n"
"v_mfma_f32_16x16x32_bf16 a[196:199], a[252:255], v[148:151], a[196:199]\n"
"ds_read_b64_tr_b16 a[240:241], v245 offset:33600\n"
"v_cvt_pk_bf16_f32 v192, v192, v193\n"
"v_mfma_f32_16x16x32_bf16 a[188:191], a[252:255], v[196:199], a[188:191]\n"
"ds_read_b64_tr_b16 a[242:243], v247 offset:33600\n"
"v_cvt_pk_bf16_f32 v193, v194, v195\n"
"v_mfma_f32_16x16x32_bf16 a[188:191], a[248:251], v[140:143], a[188:191]\n"
"v_accvgpr_read_b32 v35, a37\n"
"v_cvt_pk_bf16_f32 v88, v88, v89\n"
"v_mfma_f32_16x16x32_bf16 a[180:183], a[248:251], v[132:135], a[180:183]\n"
"v_accvgpr_read_b32 v194, a196\n"
"v_accvgpr_read_b32 v195, a197\n"
"v_mfma_f32_16x16x32_bf16 a[180:183], a[252:255], v[188:191], a[180:183]\n"
"buffer_load_dwordx4 v249, s[52:55], s81 offen offset:0 lds\n"
"v_mfma_f32_16x16x32_bf16 a[172:175], a[252:255], v[180:183], a[172:175]\n"
"ds_read_b64_tr_b16 a[252:253], v225 offset:33632\n"
"v_cvt_pk_bf16_f32 v194, v194, v195\n"
"v_mfma_f32_16x16x32_bf16 a[172:175], a[248:251], v[124:127], a[172:175]\n"
"ds_read_b64_tr_b16 a[254:255], v227 offset:33632\n"
"v_accvgpr_read_b32 v195, a198\n"
"v_mfma_f32_16x16x32_bf16 v[144:147], a[224:227], v[124:127], v[144:147]\n"
"ds_read_b64_tr_b16 a[248:249], v245 offset:33632\n"
"v_accvgpr_read_b32 v46, a199\n"
"v_mfma_f32_16x16x32_bf16 v[144:147], a[228:231], v[180:183], v[144:147]\n"
"ds_read_b64_tr_b16 a[250:251], v247 offset:33632\n"
"v_cvt_pk_bf16_f32 v195, v195, v46\n"
"v_mfma_f32_16x16x32_bf16 v[152:155], a[228:231], v[188:191], v[152:155]\n"
"buffer_load_dwordx4 v250, s[52:55], s81 offen offset:1024 lds\n"
"v_mfma_f32_16x16x32_bf16 v[152:155], a[224:227], v[132:135], v[152:155]\n"
"s_addk_i32 m0, 0x820\n"
"ds_write_b128 v14, v[192:195] offset:3072\n"
"v_cvt_pk_bf16_f32 v34, v34, v35\n"
"v_mfma_f32_16x16x32_bf16 v[160:163], a[224:227], v[140:143], v[160:163]\n"
"v_accvgpr_read_b32 v35, a38\n"
"v_cvt_pk_bf16_f32 v89, v90, v91\n"
"v_mfma_f32_16x16x32_bf16 v[160:163], a[228:231], v[196:199], v[160:163]\n"
"s_waitcnt lgkmcnt(6)\n"
"v_accvgpr_read_b32 v90, a92\n"
"v_accvgpr_read_b32 v91, a93\n"
"v_mfma_f32_16x16x32_bf16 a[164:167], a[236:239], v[196:199], a[164:167]\n"
"s_waitcnt vmcnt(10) lgkmcnt(1)\n"
"s_barrier\n"
"v_mfma_f32_16x16x32_bf16 a[164:167], a[232:235], v[140:143], a[164:167]\n"
"buffer_load_dwordx4 v251, s[52:55], s81 offen offset:0 lds\n"
"v_mfma_f32_16x16x32_bf16 a[156:159], a[232:235], v[132:135], a[156:159]\n"
"ds_read_b64_tr_b16 a[224:225], v240 offset:0\n"
"v_cvt_pk_bf16_f32 v35, v35, v28\n"
"v_mfma_f32_16x16x32_bf16 a[156:159], a[236:239], v[188:191], a[156:159]\n"
"ds_read_b64_tr_b16 a[226:227], v240 offset:128\n"
"v_cvt_pk_bf16_f32 v90, v90, v91\n"
"v_mfma_f32_16x16x32_bf16 a[148:151], a[236:239], v[180:183], a[148:151]\n"
"ds_read_b64_tr_b16 a[228:229], v240 offset:512\n"
"v_accvgpr_read_b32 v91, a94\n"
"v_mfma_f32_16x16x32_bf16 a[148:151], a[232:235], v[124:127], a[148:151]\n"
"ds_read_b64_tr_b16 a[230:231], v240 offset:640\n"
"v_accvgpr_read_b32 v31, a95\n"
"v_mfma_f32_16x16x32_bf16 v[200:203], a[240:243], v[124:127], v[200:203]\n"
"buffer_load_dwordx4 v252, s[52:55], s81 offen offset:1024 lds\n"
"v_mfma_f32_16x16x32_bf16 v[200:203], a[244:247], v[180:183], v[200:203]\n"
"s_add_u32 m0, s79, 0x81c0\n"
"s_add_u32 s81, s97, 0x1c000\n"
"ds_read_b64_tr_b16 a[236:237], v240 offset:544\n"
"v_mfma_f32_16x16x32_bf16 v[208:211], a[244:247], v[188:191], v[208:211]\n"
"s_add_u32 s97, s97, 128\n"
"s_cmp_eq_u32 s97, 896\n"
"s_cselect_b32 s97, 0, s97\n"
"ds_read_b64_tr_b16 a[232:233], v240 offset:32\n"
"v_cvt_pk_bf16_f32 v96, v96, v97\n"
"v_mfma_f32_16x16x32_bf16 v[208:211], a[240:243], v[132:135], v[208:211]\n"
"ds_read_b64_tr_b16 a[234:235], v240 offset:160\n"
"v_cvt_pk_bf16_f32 v97, v98, v99\n"
"v_mfma_f32_16x16x32_bf16 v[216:219], a[240:243], v[140:143], v[216:219]\n"
"ds_read_b64_tr_b16 a[238:239], v240 offset:672\n"
"v_accvgpr_read_b32 v98, a100\n"
"v_mfma_f32_16x16x32_bf16 v[216:219], a[244:247], v[196:199], v[216:219]\n"
"ds_read_b64_tr_b16 a[240:241], v241 offset:0\n"
"v_accvgpr_read_b32 v99, a101\n"
"v_mfma_f32_16x16x32_bf16 v[236:239], a[252:255], v[196:199], v[236:239]\n"
"ds_read_b64_tr_b16 a[242:243], v241 offset:128\n"
"v_cvt_pk_bf16_f32 v91, v91, v31\n"
"v_mfma_f32_16x16x32_bf16 v[236:239], a[248:251], v[140:143], v[236:239]\n"
"ds_read_b64_tr_b16 a[244:245], v241 offset:512\n"
"v_cvt_pk_bf16_f32 v98, v98, v99\n"
"v_mfma_f32_16x16x32_bf16 v[232:235], a[248:251], v[132:135], v[232:235]\n"
"buffer_load_dwordx4 v249, s[52:55], s80 offen offset:0 lds\n"
"v_mfma_f32_16x16x32_bf16 v[232:235], a[252:255], v[188:191], v[232:235]\n"
"ds_read_b64_tr_b16 a[246:247], v241 offset:640\n"
"v_accvgpr_read_b32 v99, a102\n"
"v_mfma_f32_16x16x32_bf16 v[228:231], a[252:255], v[180:183], v[228:231]\n"
"ds_read_b64_tr_b16 a[252:253], v241 offset:544\n"
"v_accvgpr_read_b32 v36, a103\n"
"v_mfma_f32_16x16x32_bf16 v[228:231], a[248:251], v[124:127], v[228:231]\n"
"s_waitcnt lgkmcnt(9)\n"
"ds_read_b64_tr_b16 a[248:249], v241 offset:32\n"
"v_cvt_pk_bf16_f32 v40, v40, v41\n"
"v_mfma_f32_16x16x32_bf16 v[124:127], a[224:227], v[0:3], 0\n"
"ds_read_b64_tr_b16 a[250:251], v241 offset:160\n"
"v_cvt_pk_bf16_f32 v41, v42, v43\n"
"v_mfma_f32_16x16x32_bf16 v[124:127], a[228:231], v[56:59], v[124:127]\n"
"buffer_load_dwordx4 v250, s[52:55], s80 offen offset:1024 lds\n"
"v_mfma_f32_16x16x32_bf16 v[180:183], a[228:231], v[64:67], 0\n"
"s_addk_i32 m0, 0x820\n"
"ds_read_b64_tr_b16 a[254:255], v241 offset:672\n"
"v_accvgpr_read_b32 v42, a44\n"
"v_mfma_f32_16x16x32_bf16 v[180:183], a[224:227], v[8:11], v[180:183]\n"
"ds_read_b128 a[4:7], v15 offset:0\n"
"v_accvgpr_read_b32 v43, a45\n"
"v_mfma_f32_16x16x32_bf16 v[60:63], a[224:227], v[16:19], 0\n"
"ds_read_b128 a[12:15], v15 offset:1024\n"
"v_cvt_pk_bf16_f32 v99, v99, v36\n"
"v_mfma_f32_16x16x32_bf16 v[60:63], a[228:231], v[72:75], v[60:63]\n"
"v_cvt_pk_bf16_f32 v42, v42, v43\n"
"v_accvgpr_read_b32 v29, a47\n"
"v_mfma_f32_16x16x32_bf16 v[108:111], a[228:231], v[80:83], 0\n"
"buffer_load_dwordx4 v251, s[52:55], s80 offen offset:0 lds\n"
"v_mfma_f32_16x16x32_bf16 v[108:111], a[224:227], v[24:27], v[108:111]\n"
"s_waitcnt lgkmcnt(7)\n"
"ds_read_b64_tr_b16 a[228:229], v242 offset:512\n"
"v_accvgpr_read_b32 v43, a46\n"
"v_mfma_f32_16x16x32_bf16 v[132:135], a[232:235], v[0:3], 0\n"
"ds_read_b64_tr_b16 a[230:231], v242 offset:640\n"
"v_cvt_pk_bf16_f32 v48, v48, v49\n"
"v_mfma_f32_16x16x32_bf16 v[132:135], a[236:239], v[56:59], v[132:135]\n"
"ds_read_b64_tr_b16 a[224:225], v242 offset:0\n"
"v_cvt_pk_bf16_f32 v49, v50, v51\n"
"v_mfma_f32_16x16x32_bf16 v[188:191], a[236:239], v[64:67], 0\n"
"ds_read_b64_tr_b16 a[226:227], v242 offset:128\n"
"v_accvgpr_read_b32 v50, a52\n"
"v_mfma_f32_16x16x32_bf16 v[188:191], a[232:235], v[8:11], v[188:191]\n"
"buffer_load_dwordx4 v252, s[52:55], s80 offen offset:1024 lds\n"
"v_mfma_f32_16x16x32_bf16 v[68:71], a[232:235], v[16:19], 0\n"
"s_add_u32 m0, s79, 0x14460\n"
"v_accvgpr_read_b32 v51, a53\n"
"v_cvt_pk_bf16_f32 v43, v43, v29\n"
"v_mfma_f32_16x16x32_bf16 v[68:71], a[236:239], v[72:75], v[68:71]\n"
"s_add_u32 s80, s97, 0x0\n"
"v_add_u32 v243, s94, v245\n"
"v_cvt_pk_bf16_f32 v50, v50, v51\n"
"v_mfma_f32_16x16x32_bf16 v[116:119], a[236:239], v[80:83], 0\n"
"ds_read_b64_tr_b16 a[236:237], v242 offset:544\n"
"v_accvgpr_read_b32 v51, a54\n"
"v_mfma_f32_16x16x32_bf16 v[116:119], a[232:235], v[24:27], v[116:119]\n"
"ds_read_b64_tr_b16 a[232:233], v242 offset:32\n"
"v_accvgpr_read_b32 v30, a55\n"
"v_mfma_f32_16x16x32_bf16 v[140:143], a[240:243], v[0:3], 0\n"
"s_waitcnt lgkmcnt(6)\n"
"ds_read_b64_tr_b16 a[234:235], v242 offset:160\n"
"v_add_u32 v243, 0xffffbf20, v243\n"
"v_mfma_f32_16x16x32_bf16 v[140:143], a[244:247], v[56:59], v[140:143]\n"
"ds_read_b64_tr_b16 a[238:239], v242 offset:672\n"
"v_cvt_pk_bf16_f32 v104, v104, v105\n"
"v_mfma_f32_16x16x32_bf16 v[196:199], a[244:247], v[64:67], 0\n"
"buffer_load_dwordx4 v249, s[52:55], s81 offen offset:0 lds\n"
"v_mfma_f32_16x16x32_bf16 v[196:199], a[240:243], v[8:11], v[196:199]\n"
"v_cvt_pk_bf16_f32 v105, v106, v107\n"
"v_cvt_pk_bf16_f32 v51, v51, v30\n"
"v_mfma_f32_16x16x32_bf16 v[76:79], a[240:243], v[16:19], 0\n"
"v_accvgpr_read_b32 v106, a108\n"
"v_accvgpr_read_b32 v107, a109\n"
"v_mfma_f32_16x16x32_bf16 v[76:79], a[244:247], v[72:75], v[76:79]\n"
"v_cvt_pk_bf16_f32 v106, v106, v107\n"
"v_accvgpr_read_b32 v37, a111\n"
"v_mfma_f32_16x16x32_bf16 v[172:175], a[248:251], v[0:3], 0\n"
"ds_read_b64_tr_b16 a[240:241], v243 offset:0\n"
"v_accvgpr_read_b32 v107, a110\n"
"v_mfma_f32_16x16x32_bf16 v[172:175], a[252:255], v[56:59], v[172:175]\n"
"buffer_load_dwordx4 v250, s[52:55], s81 offen offset:1024 lds\n"
"v_mfma_f32_16x16x32_bf16 v[148:151], a[252:255], v[64:67], 0\n"
"s_addk_i32 m0, 0x820\n"
"ds_read_b64_tr_b16 a[242:243], v243 offset:128\n"
"v_cvt_pk_bf16_f32 v107, v107, v37\n"
"v_mfma_f32_16x16x32_bf16 v[148:151], a[248:251], v[8:11], v[148:151]\n"
"ds_read_b64_tr_b16 a[244:245], v243 offset:512\n"
"v_cvt_pk_bf16_f32 v112, v112, v113\n"
"v_mfma_f32_16x16x32_bf16 v[100:103], a[248:251], v[16:19], 0\n"
"ds_read_b64_tr_b16 a[246:247], v243 offset:640\n"
"v_cvt_pk_bf16_f32 v113, v114, v115\n"
"v_mfma_f32_16x16x32_bf16 v[100:103], a[252:255], v[72:75], v[100:103]\n"
"s_waitcnt lgkmcnt(6)\n"
"ds_read_b64_tr_b16 a[248:249], v243 offset:32\n"
"v_accvgpr_read_b32 v114, a116\n"
"v_mfma_f32_16x16x32_bf16 v[124:127], a[224:227], v[32:35], v[124:127]\n"
"buffer_load_dwordx4 v251, s[52:55], s81 offen offset:0 lds\n"
"v_mfma_f32_16x16x32_bf16 v[124:127], a[228:231], v[88:91], v[124:127]\n"
"ds_read_b64_tr_b16 a[250:251], v243 offset:160\n"
"v_accvgpr_read_b32 v115, a117\n"
"v_mfma_f32_16x16x32_bf16 v[180:183], a[228:231], v[96:99], v[180:183]\n"
"ds_read_b64_tr_b16 a[252:253], v243 offset:544\n"
"v_cvt_pk_bf16_f32 v114, v114, v115\n"
"v_mfma_f32_16x16x32_bf16 v[180:183], a[224:227], v[40:43], v[180:183]\n"
"ds_read_b64_tr_b16 a[254:255], v243 offset:672\n"
"v_accvgpr_read_b32 v115, a118\n"
"v_mfma_f32_16x16x32_bf16 v[60:63], a[224:227], v[48:51], v[60:63]\n"
"v_accvgpr_read_b32 v39, a119\n"
"v_cvt_pk_bf16_f32 v168, v168, v169\n"
"v_mfma_f32_16x16x32_bf16 v[60:63], a[228:231], v[104:107], v[60:63]\n"
"buffer_load_dwordx4 v252, s[52:55], s81 offen offset:1024 lds\n"
"v_mfma_f32_16x16x32_bf16 v[108:111], a[228:231], a[12:15], v[108:111]\n"
"s_add_u32 m0, s79, 0xc2a0\n"
"v_cvt_pk_bf16_f32 v169, v170, v171\n"
"v_cvt_pk_bf16_f32 v115, v115, v39\n"
"v_mfma_f32_16x16x32_bf16 v[108:111], a[224:227], a[4:7], v[108:111]\n"
"s_waitcnt lgkmcnt(7)\n"
"v_accvgpr_read_b32 v170, a172\n"
"v_accvgpr_read_b32 v171, a173\n"
"v_mfma_f32_16x16x32_bf16 v[116:119], a[232:235], a[4:7], v[116:119]\n"
"s_waitcnt vmcnt(16) lgkmcnt(0)\n"
"s_barrier\n"
"v_mfma_f32_16x16x32_bf16 v[116:119], a[236:239], a[12:15], v[116:119]\n"
"s_add_u32 s81, s97, 0x1c000\n"
"buffer_load_dwordx4 v249, s[52:55], s80 offen offset:0 lds\n"
"v_mfma_f32_16x16x32_bf16 v[68:71], a[236:239], v[104:107], v[68:71]\n"
"s_add_u32 s97, s97, 128\n"
"s_cmp_eq_u32 s97, 896\n"
"s_cselect_b32 s97, 0, s97\n"
"ds_read_b64_tr_b16 a[228:229], v240 offset:33728\n"
"v_cvt_pk_bf16_f32 v170, v170, v171\n"
"v_mfma_f32_16x16x32_bf16 v[68:71], a[232:235], v[48:51], v[68:71]\n"
"ds_read_b64_tr_b16 a[224:225], v240 offset:33216\n"
"v_accvgpr_read_b32 v171, a174\n"
"v_mfma_f32_16x16x32_bf16 v[188:191], a[232:235], v[40:43], v[188:191]\n"
"ds_read_b64_tr_b16 a[226:227], v240 offset:33344\n"
"v_accvgpr_read_b32 v47, a175\n"
"v_mfma_f32_16x16x32_bf16 v[188:191], a[236:239], v[96:99], v[188:191]\n"
"ds_read_b64_tr_b16 a[230:231], v240 offset:33856\n"
"v_cvt_pk_bf16_f32 v176, v176, v177\n"
"v_mfma_f32_16x16x32_bf16 v[132:135], a[236:239], v[88:91], v[132:135]\n"
"ds_read_b64_tr_b16 a[236:237], v240 offset:33760\n"
"v_cvt_pk_bf16_f32 v177, v178, v179\n"
"v_mfma_f32_16x16x32_bf16 v[132:135], a[232:235], v[32:35], v[132:135]\n"
"buffer_load_dwordx4 v250, s[52:55], s80 offen offset:1024 lds\n"
"v_mfma_f32_16x16x32_bf16 v[140:143], a[240:243], v[32:35], v[140:143]\n"
"s_addk_i32 m0, 0x820\n"
"ds_read_b64_tr_b16 a[232:233], v240 offset:33248\n"
"v_accvgpr_read_b32 v178, a180\n"
"v_mfma_f32_16x16x32_bf16 v[140:143], a[244:247], v[88:91], v[140:143]\n"
"v_accvgpr_read_b32 v179, a181\n"
"v_cvt_pk_bf16_f32 v171, v171, v47\n"
"v_mfma_f32_16x16x32_bf16 v[196:199], a[244:247], v[96:99], v[196:199]\n"
"buffer_load_dwordx4 v251, s[52:55], s80 offen offset:0 lds\n"
"v_mfma_f32_16x16x32_bf16 v[196:199], a[240:243], v[40:43], v[196:199]\n"
"v_cvt_pk_bf16_f32 v178, v178, v179\n"
"v_accvgpr_read_b32 v52, a183\n"
"v_mfma_f32_16x16x32_bf16 v[76:79], a[240:243], v[48:51], v[76:79]\n"
"v_accvgpr_read_b32 v179, a182\n"
"v_cvt_pk_bf16_f32 v120, v120, v121\n"
"v_mfma_f32_16x16x32_bf16 v[76:79], a[244:247], v[104:107], v[76:79]\n"
"buffer_load_dwordx4 v252, s[52:55], s80 offen offset:1024 lds\n"
"v_mfma_f32_16x16x32_bf16 v[100:103], a[252:255], v[104:107], v[100:103]\n"
"s_add_u32 m0, s79, 0x1c620\n"
"v_cvt_pk_bf16_f32 v121, v122, v123\n"
"v_cvt_pk_bf16_f32 v179, v179, v52\n"
"v_mfma_f32_16x16x32_bf16 v[100:103], a[248:251], v[48:51], v[100:103]\n"
"s_add_u32 s80, s97, 0x0\n"
"v_accvgpr_read_b32 v122, a124\n"
"v_accvgpr_read_b32 v123, a125\n"
"v_mfma_f32_16x16x32_bf16 v[148:151], a[248:251], v[40:43], v[148:151]\n"
"v_cvt_pk_bf16_f32 v122, v122, v123\n"
"v_accvgpr_read_b32 v44, a127\n"
"v_mfma_f32_16x16x32_bf16 v[148:151], a[252:255], v[96:99], v[148:151]\n"
"ds_read_b64_tr_b16 a[234:235], v240 offset:33376\n"
"v_accvgpr_read_b32 v123, a126\n"
"v_mfma_f32_16x16x32_bf16 v[172:175], a[252:255], v[88:91], v[172:175]\n"
"v_cvt_pk_bf16_f32 v128, v128, v129\n"
"v_cvt_pk_bf16_f32 v123, v123, v44\n"
"v_mfma_f32_16x16x32_bf16 v[172:175], a[248:251], v[32:35], v[172:175]\n"
"s_waitcnt lgkmcnt(2)\n"
"v_cvt_pk_bf16_f32 v129, v130, v131\n"
"v_accvgpr_read_b32 v45, a135\n"
"v_mfma_f32_16x16x32_bf16 v[124:127], a[224:227], v[112:115], v[124:127]\n"
"v_accvgpr_read_b32 v130, a132\n"
"v_accvgpr_read_b32 v131, a133\n"
"v_mfma_f32_16x16x32_bf16 v[124:127], a[228:231], v[168:171], v[124:127]\n"
"ds_read_b64_tr_b16 a[238:239], v240 offset:33888\n"
"v_cvt_pk_bf16_f32 v130, v130, v131\n"
"v_mfma_f32_16x16x32_bf16 v[180:183], a[228:231], v[176:179], v[180:183]\n"
"v_accvgpr_read_b32 v131, a134\n"
"v_cvt_pk_bf16_f32 v184, v184, v185\n"
"v_cvt_pk_bf16_f32 v185, v186, v187\n"
"v_accvgpr_read_b32 v186, a188\n"
"v_accvgpr_read_b32 v187, a189\n"
"v_mfma_f32_16x16x32_bf16 v[180:183], a[224:227], v[120:123], v[180:183]\n"
"PG13_%=:\n"
"s_cbranch_execz PGB3_%=\n"
"ds_read_b64_tr_b16 a[240:241], v241 offset:33216\n"
"v_cvt_pk_bf16_f32 v131, v131, v45\n"
"v_cvt_pk_bf16_f32 v186, v186, v187\n"
"v_accvgpr_read_b32 v187, a190\n"
"v_mfma_f32_16x16x32_bf16 v[60:63], a[224:227], v[128:131], v[60:63]\n"
"ds_read_b64_tr_b16 a[242:243], v241 offset:33344\n"
"v_accvgpr_read_b32 v53, a191\n"
"v_cvt_pk_bf16_f32 v187, v187, v53\n"
"v_cvt_pk_bf16_f32 v144, v144, v145\n"
"v_cvt_pk_bf16_f32 v145, v146, v147\n"
"v_mfma_f32_16x16x32_bf16 v[60:63], a[228:231], v[184:187], v[60:63]\n"
"ds_read_b64_tr_b16 a[244:245], v241 offset:33728\n"
"v_accvgpr_read_b32 v146, a148\n"
"v_mfma_f32_16x16x32_bf16 v[108:111], a[228:231], v[192:195], v[108:111]\n"
"ds_read_b64_tr_b16 a[246:247], v241 offset:33856\n"
"v_accvgpr_read_b32 v147, a149\n"
"v_mfma_f32_16x16x32_bf16 v[108:111], a[224:227], v[136:139], v[108:111]\n"
"s_waitcnt lgkmcnt(5)\n"
"buffer_load_dwordx4 v249, s[52:55], s81 offen offset:0 lds\n"
"v_mfma_f32_16x16x32_bf16 v[132:135], a[232:235], v[112:115], v[132:135]\n"
"s_waitcnt lgkmcnt(4)\n"
"ds_read_b64_tr_b16 a[252:253], v241 offset:33760\n"
"v_cvt_pk_bf16_f32 v146, v146, v147\n"
"v_mfma_f32_16x16x32_bf16 v[132:135], a[236:239], v[168:171], v[132:135]\n"
"ds_read_b64_tr_b16 a[248:249], v241 offset:33248\n"
"v_accvgpr_read_b32 v147, a150\n"
"v_mfma_f32_16x16x32_bf16 v[188:191], a[236:239], v[176:179], v[188:191]\n"
"buffer_load_dwordx4 v250, s[52:55], s81 offen offset:1024 lds\n"
"v_mfma_f32_16x16x32_bf16 v[188:191], a[232:235], v[120:123], v[188:191]\n"
"s_addk_i32 m0, 0x820\n"
"ds_read_b64_tr_b16 a[250:251], v241 offset:33376\n"
"v_accvgpr_read_b32 v54, a151\n"
"v_mfma_f32_16x16x32_bf16 v[68:71], a[232:235], v[128:131], v[68:71]\n"
"ds_read_b64_tr_b16 a[254:255], v241 offset:33888\n"
"v_cvt_pk_bf16_f32 v147, v147, v54\n"
"v_mfma_f32_16x16x32_bf16 v[68:71], a[236:239], v[184:187], v[68:71]\n"
"buffer_load_dwordx4 v251, s[52:55], s81 offen offset:0 lds\n"
"v_mfma_f32_16x16x32_bf16 v[116:119], a[236:239], v[192:195], v[116:119]\n"
"ds_read_b32 v13, v254 offset:2048\n"
"v_cvt_pk_bf16_f32 v200, v200, v201\n"
"v_mfma_f32_16x16x32_bf16 v[116:119], a[232:235], v[136:139], v[116:119]\n"
"s_waitcnt lgkmcnt(7)\n"
"ds_read_b128 a[20:23], v15 offset:2048\n"
"v_cvt_pk_bf16_f32 v201, v202, v203\n"
"v_mfma_f32_16x16x32_bf16 v[140:143], a[240:243], v[112:115], v[140:143]\n"
"s_waitcnt lgkmcnt(6)\n"
"buffer_load_dwordx4 v252, s[52:55], s81 offen offset:1024 lds\n"
"v_mfma_f32_16x16x32_bf16 v[140:143], a[244:247], v[168:171], v[140:143]\n"
"s_add_u32 m0, s79, 0x10380\n"
"ds_read_b64_tr_b16 a[228:229], v242 offset:33728\n"
"v_cvt_pk_bf16_f32 v202, v228, v229\n"
"v_mfma_f32_16x16x32_bf16 v[196:199], a[244:247], v[176:179], v[196:199]\n"
"s_add_u32 s81, s97, 0x1c000\n"
"ds_read_b64_tr_b16 a[224:225], v242 offset:33216\n"
"v_cvt_pk_bf16_f32 v203, v230, v231\n"
"v_mfma_f32_16x16x32_bf16 v[196:199], a[240:243], v[120:123], v[196:199]\n"
"s_add_u32 s97, s97, 128\n"
"s_cmp_eq_u32 s97, 896\n"
"s_cselect_b32 s97, 0, s97\n"
"ds_read_b64_tr_b16 a[226:227], v242 offset:33344\n"
"v_cvt_pk_bf16_f32 v152, v152, v153\n"
"v_mfma_f32_16x16x32_bf16 v[76:79], a[240:243], v[128:131], v[76:79]\n"
"ds_read_b64_tr_b16 a[230:231], v242 offset:33856\n"
"v_cvt_pk_bf16_f32 v153, v154, v155\n"
"v_mfma_f32_16x16x32_bf16 v[76:79], a[244:247], v[184:187], v[76:79]\n"
"s_waitcnt lgkmcnt(7)\n"
"ds_read_b128 a[28:31], v15 offset:3072\n"
"v_cvt_pk_bf16_f32 v208, v208, v209\n"
"v_mfma_f32_16x16x32_bf16 v[172:175], a[248:251], v[112:115], v[172:175]\n"
"s_waitcnt lgkmcnt(7)\n"
"ds_read_b64_tr_b16 a[236:237], v242 offset:33760\n"
"v_accvgpr_read_b32 v154, a156\n"
"v_mfma_f32_16x16x32_bf16 v[172:175], a[252:255], v[168:171], v[172:175]\n"
"ds_read_b64_tr_b16 a[232:233], v242 offset:33248\n"
"v_accvgpr_read_b32 v155, a157\n"
"v_mfma_f32_16x16x32_bf16 v[148:151], a[252:255], v[176:179], v[148:151]\n"
"ds_read_b64_tr_b16 a[234:235], v242 offset:33376\n"
"v_cvt_pk_bf16_f32 v209, v210, v211\n"
"v_mfma_f32_16x16x32_bf16 v[148:151], a[248:251], v[120:123], v[148:151]\n"
"v_cvt_pk_bf16_f32 v210, v232, v233\n"
"v_cvt_pk_bf16_f32 v211, v234, v235\n"
"v_mfma_f32_16x16x32_bf16 v[100:103], a[248:251], v[128:131], v[100:103]\n"
"v_cvt_pk_bf16_f32 v154, v154, v155\n"
"v_accvgpr_read_b32 v55, a159\n"
"v_mfma_f32_16x16x32_bf16 v[100:103], a[252:255], v[184:187], v[100:103]\n"
"s_waitcnt lgkmcnt(5)\n"
"v_accvgpr_read_b32 v155, a158\n"
"v_cvt_pk_bf16_f32 v160, v160, v161\n"
"v_mfma_f32_16x16x32_bf16 v[124:127], a[224:227], v[144:147], v[124:127]\n"
"s_waitcnt lgkmcnt(4)\n"
"v_cvt_pk_bf16_f32 v161, v162, v163\n"
"v_cvt_pk_bf16_f32 v155, v155, v55\n"
"v_mfma_f32_16x16x32_bf16 v[124:127], a[228:231], v[200:203], v[124:127]\n"
"buffer_load_dwordx4 v249, s[52:55], s80 offen offset:0 lds\n"
"v_accvgpr_read_b32 v162, a164\n"
"v_accvgpr_read_b32 v163, a165\n"
"v_mfma_f32_16x16x32_bf16 v[180:183], a[228:231], v[208:211], v[180:183]\n"
"ds_read_b64_tr_b16 a[238:239], v242 offset:33888\n"
"v_cvt_pk_bf16_f32 v162, v162, v163\n"
"v_accvgpr_read_b32 v163, a166\n"
"v_accvgpr_read_b32 v92, a167\n"
"v_mfma_f32_16x16x32_bf16 v[180:183], a[224:227], v[152:155], v[180:183]\n"
"ds_read_b64_tr_b16 a[240:241], v243 offset:33216\n"
"v_cvt_pk_bf16_f32 v163, v163, v92\n"
"v_cvt_pk_bf16_f32 v216, v216, v217\n"
"v_cvt_pk_bf16_f32 v217, v218, v219\n"
"v_mfma_f32_16x16x32_bf16 v[60:63], a[224:227], v[160:163], v[60:63]\n"
"ds_read_b64_tr_b16 a[242:243], v243 offset:33344\n"
"v_cvt_pk_bf16_f32 v218, v236, v237\n"
"v_cvt_pk_bf16_f32 v219, v238, v239\n"
"v_add_f32 v13, v167, v13\n"
"v_add_u32 v242, 0xfffefc80, v242\n"
"v_mfma_f32_16x16x32_bf16 v[60:63], a[228:231], v[216:219], v[60:63]\n"
"s_waitcnt lgkmcnt(6)\n"
"buffer_load_dwordx4 v250, s[52:55], s80 offen offset:1024 lds\n"
"v_mfma_f32_16x16x32_bf16 v[108:111], a[228:231], a[28:31], v[108:111]\n"
"s_addk_i32 m0, 0x820\n"
"ds_read_b64_tr_b16 a[244:245], v243 offset:33728\n"
"v_add_u32 v240, 0xfffefc80, v240\n"
"v_mfma_f32_16x16x32_bf16 v[108:111], a[224:227], a[20:23], v[108:111]\n"
"s_waitcnt lgkmcnt(4)\n"
"ds_read_b64_tr_b16 a[246:247], v243 offset:33856\n"
"v_and_b32 v224, 0x3f0, v248\n"
"v_mfma_f32_16x16x32_bf16 v[116:119], a[232:235], a[20:23], v[116:119]\n"
"s_waitcnt lgkmcnt(4)\n"
"buffer_load_dwordx4 v251, s[52:55], s80 offen offset:0 lds\n"
"v_mfma_f32_16x16x32_bf16 v[116:119], a[236:239], a[28:31], v[116:119]\n"
"ds_read_b64_tr_b16 a[248:249], v243 offset:33248\n"
"v_add_u32 v224, 159712, v224\n"
"v_mfma_f32_16x16x32_bf16 v[68:71], a[236:239], v[216:219], v[68:71]\n"
"ds_read_b64_tr_b16 a[250:251], v243 offset:33376\n"
"v_add_u32 v241, 0xfffefc80, v241\n"
"v_mfma_f32_16x16x32_bf16 v[68:71], a[232:235], v[160:163], v[68:71]\n"
"buffer_load_dwordx4 v252, s[52:55], s80 offen offset:1024 lds\n"
"v_mfma_f32_16x16x32_bf16 v[188:191], a[232:235], v[152:155], v[188:191]\n"
"s_add_u32 m0, s79, 0x18540\n"
"ds_read_b64_tr_b16 a[252:253], v243 offset:33760\n"
"v_bfe_u32 v253, v248, 4, 6\n"
"v_rcp_f32 v244, v164\n"
"v_mfma_f32_16x16x32_bf16 v[188:191], a[236:239], v[208:211], v[188:191]\n"
"s_add_u32 s80, s97, 0x0\n"
"ds_read_b64_tr_b16 a[254:255], v243 offset:33888\n"
"v_lshrrev_b32 v235, 5, v253\n"
"v_rcp_f32 v246, v166\n"
"v_mfma_f32_16x16x32_bf16 v[132:135], a[236:239], v[200:203], v[132:135]\n"
"v_rcp_f32 v245, v165\n"
"v_mfma_f32_16x16x32_bf16 v[132:135], a[232:235], v[144:147], v[132:135]\n"
"s_waitcnt lgkmcnt(6)\n"
"v_rcp_f32 v247, v13\n"
"v_mfma_f32_16x16x32_bf16 v[140:143], a[240:243], v[144:147], v[140:143]\n"
"s_waitcnt lgkmcnt(4)\n"
"ds_read_b128 v[228:231], v224\n"
"v_add_u32 v243, 0xfffefc80, v243\n"
"v_mfma_f32_16x16x32_bf16 v[140:143], a[244:247], v[200:203], v[140:143]\n"
"v_mul_u32_u24 v235, 15872, v235\n"
"v_mul_f32 v124, v244, v124\n"
"v_mfma_f32_16x16x32_bf16 v[196:199], a[244:247], v[208:211], v[196:199]\n"
"s_waitcnt vmcnt(24) lgkmcnt(1)\n"
"s_barrier\n"
"v_mfma_f32_16x16x32_bf16 v[196:199], a[240:243], v[152:155], v[196:199]\n"
"buffer_load_dwordx4 v249, s[52:55], s81 offen offset:0 lds\n"
"v_mfma_f32_16x16x32_bf16 v[76:79], a[240:243], v[160:163], v[76:79]\n"
"ds_read_b64_tr_b16 a[228:229], v242 offset:0\n"
"v_mul_f32 v125, v244, v125\n"
"v_mfma_f32_16x16x32_bf16 v[76:79], a[244:247], v[216:219], v[76:79]\n"
"ds_read_b64_tr_b16 a[224:225], v240 offset:0\n"
"v_mul_f32 v126, v244, v126\n"
"v_mfma_f32_16x16x32_bf16 v[100:103], a[252:255], v[216:219], v[100:103]\n"
"ds_read_b64_tr_b16 a[226:227], v240 offset:128\n"
"v_mul_f32 v127, v244, v127\n"
"v_mfma_f32_16x16x32_bf16 v[100:103], a[248:251], v[160:163], v[100:103]\n"
"ds_read_b64_tr_b16 a[230:231], v242 offset:128\n"
"v_lshl_add_u32 v253, v253, 4, v235\n"
"v_mfma_f32_16x16x32_bf16 v[148:151], a[248:251], v[152:155], v[148:151]\n"
"ds_read_b64_tr_b16 a[236:237], v242 offset:32\n"
"v_mul_f32 v132, v244, v132\n"
"v_mfma_f32_16x16x32_bf16 v[148:151], a[252:255], v[208:211], v[148:151]\n"
"ds_read_b64_tr_b16 a[238:239], v242 offset:160\n"
"v_mul_f32 v133, v244, v133\n"
"v_mfma_f32_16x16x32_bf16 v[172:175], a[252:255], v[200:203], v[172:175]\n"
"ds_read_b64_tr_b16 a[232:233], v240 offset:32\n"
"v_mul_f32 v134, v244, v134\n"
"v_mfma_f32_16x16x32_bf16 v[172:175], a[248:251], v[144:147], v[172:175]\n"
"s_waitcnt lgkmcnt(4)\n"
"buffer_load_dwordx4 v250, s[52:55], s81 offen offset:1024 lds\n"
"v_mfma_f32_16x16x32_bf16 v[4:7], a[224:227], v[0:3], 0\n"
"s_waitcnt lgkmcnt(3)\n"
"s_addk_i32 m0, 0x820\n"
"ds_read_b64_tr_b16 a[234:235], v240 offset:160\n"
"v_mfma_f32_16x16x32_bf16 v[4:7], a[228:231], v[32:35], v[4:7]\n"
"ds_read_b64_tr_b16 a[240:241], v241 offset:0\n"
"v_mul_f32 v135, v244, v135\n"
"v_mfma_f32_16x16x32_bf16 v[36:39], a[228:231], v[40:43], 0\n"
"buffer_load_dwordx4 v251, s[52:55], s81 offen offset:0 lds\n"
"v_mfma_f32_16x16x32_bf16 v[36:39], a[224:227], v[8:11], v[36:39]\n"
"ds_read_b64_tr_b16 a[242:243], v241 offset:128\n"
"v_cvt_pk_bf16_f32 v124, v124, v125\n"
"v_mfma_f32_16x16x32_bf16 v[84:87], a[224:227], v[16:19], 0\n"
"ds_read_b64_tr_b16 a[244:245], v243 offset:0\n"
"v_cvt_pk_bf16_f32 v125, v126, v127\n"
"v_mfma_f32_16x16x32_bf16 v[84:87], a[228:231], v[48:51], v[84:87]\n"
"buffer_load_dwordx4 v252, s[52:55], s81 offen offset:1024 lds\n"
"v_mfma_f32_16x16x32_bf16 v[156:159], a[228:231], a[4:7], 0\n"
"s_add_u32 m0, s79, 0x0\n"
"ds_read_b64_tr_b16 a[246:247], v243 offset:128\n"
"v_lshl_add_u32 v253, s31, 13, v253\n"
"v_mfma_f32_16x16x32_bf16 v[156:159], a[224:227], v[24:27], v[156:159]\n"
"s_waitcnt lgkmcnt(4)\n"
"ds_read_b64_tr_b16 a[252:253], v243 offset:32\n"
"v_cvt_pk_bf16_f32 v126, v132, v133\n"
"v_cvt_pk_bf16_f32 v127, v134, v135\n"
"v_mfma_f32_16x16x32_bf16 v[164:167], a[232:235], v[24:27], 0\n"
"s_add_u32 s81, s97, 0x1c000\n"
"ds_read_b64_tr_b16 a[254:255], v243 offset:160\n"
"v_mul_f32 v180, v245, v180\n"
"v_mfma_f32_16x16x32_bf16 v[164:167], a[236:239], a[4:7], v[164:167]\n"
"s_add_u32 s97, s97, 128\n"
"s_cmp_eq_u32 s97, 896\n"
"s_cselect_b32 s97, 0, s97\n"
"ds_read_b64_tr_b16 a[248:249], v241 offset:32\n"
"v_mul_f32 v181, v245, v181\n"
"v_mfma_f32_16x16x32_bf16 v[204:207], a[236:239], v[48:51], 0\n"
"ds_read_b64_tr_b16 a[250:251], v241 offset:160\n"
"v_mul_f32 v182, v245, v182\n"
"v_mfma_f32_16x16x32_bf16 v[204:207], a[232:235], v[16:19], v[204:207]\n"
"ds_read_b64_tr_b16 a[228:229], v242 offset:512\n"
"v_mul_f32 v183, v245, v183\n"
"v_mfma_f32_16x16x32_bf16 v[44:47], a[232:235], v[8:11], 0\n"
"ds_read_b64_tr_b16 a[230:231], v242 offset:640\n"
"v_mul_f32 v188, v245, v188\n"
"v_mfma_f32_16x16x32_bf16 v[44:47], a[236:239], v[40:43], v[44:47]\n"
"ds_read_b64_tr_b16 a[224:225], v240 offset:512\n"
"v_mul_f32 v189, v245, v189\n"
"v_mfma_f32_16x16x32_bf16 v[12:15], a[236:239], v[32:35], 0\n"
"buffer_store_dwordx4 v[124:127], v253, s[56:59], s72 offen sc1\n"
"v_mul_f32 v190, v245, v190\n"
"v_mul_f32 v191, v245, v191\n"
"v_cvt_pk_bf16_f32 v180, v180, v181\n"
"v_cvt_pk_bf16_f32 v181, v182, v183\n"
"v_cvt_pk_bf16_f32 v182, v188, v189\n"
"v_cvt_pk_bf16_f32 v183, v190, v191\n"
"v_mfma_f32_16x16x32_bf16 v[12:15], a[232:235], v[0:3], v[12:15]\n"
"s_waitcnt lgkmcnt(9)\n"
"ds_read_b64_tr_b16 a[226:227], v240 offset:640\n"
"v_mul_f32 v60, v246, v60\n"
"v_mfma_f32_16x16x32_bf16 v[20:23], a[240:243], v[0:3], 0\n"
"s_waitcnt lgkmcnt(8)\n"
"ds_read_b64_tr_b16 a[236:237], v242 offset:544\n"
"v_mul_f32 v61, v246, v61\n"
"v_mfma_f32_16x16x32_bf16 v[20:23], a[244:247], v[32:35], v[20:23]\n"
"ds_read_b64_tr_b16 a[238:239], v242 offset:672\n"
"v_mul_f32 v62, v246, v62\n"
"v_mfma_f32_16x16x32_bf16 v[52:55], a[244:247], v[40:43], 0\n"
"ds_read_b64_tr_b16 a[232:233], v240 offset:544\n"
"v_mul_f32 v63, v246, v63\n"
"v_mfma_f32_16x16x32_bf16 v[52:55], a[240:243], v[8:11], v[52:55]\n"
"ds_read_b64_tr_b16 a[234:235], v240 offset:672\n"
"v_mul_f32 v68, v246, v68\n"
"v_mfma_f32_16x16x32_bf16 v[212:215], a[240:243], v[16:19], 0\n"
"ds_read_b64_tr_b16 a[240:241], v241 offset:512\n"
"v_mul_f32 v69, v246, v69\n"
"v_mfma_f32_16x16x32_bf16 v[212:215], a[244:247], v[48:51], v[212:215]\n"
"s_waitcnt lgkmcnt(10)\n"
"buffer_store_dwordx4 v[180:183], v253, s[56:59], s74 offen sc1\n"
"v_mul_f32 v70, v246, v70\n"
"v_mul_f32 v71, v246, v71\n"
"v_max3_f32 v228, v228, v229, v230\n"
"v_cvt_pk_bf16_f32 v60, v60, v61\n"
"v_cvt_pk_bf16_f32 v61, v62, v63\n"
"v_cvt_pk_bf16_f32 v62, v68, v69\n"
"v_cvt_pk_bf16_f32 v63, v70, v71\n"
"v_max_f32 v228, v228, v231\n"
"v_mfma_f32_16x16x32_bf16 v[220:223], a[252:255], v[48:51], 0\n"
"s_waitcnt lgkmcnt(9)\n"
"ds_read_b64_tr_b16 a[242:243], v241 offset:640\n"
"v_cmp_lt_f32 vcc, s85, v228\n"
"v_mfma_f32_16x16x32_bf16 v[220:223], a[248:251], v[16:19], v[220:223]\n"
"ds_read_b64_tr_b16 a[244:245], v243 offset:512\n"
"v_mul_f32 v108, v247, v108\n"
"v_mfma_f32_16x16x32_bf16 v[92:95], a[248:251], v[8:11], 0\n"
"ds_read_b64_tr_b16 a[246:247], v243 offset:640\n"
"v_mul_f32 v109, v247, v109\n"
"v_mfma_f32_16x16x32_bf16 v[92:95], a[252:255], v[40:43], v[92:95]\n"
"s_cbranch_vccnz AGAIN_%=\n"
"v_mfma_f32_16x16x32_bf16 v[28:31], a[252:255], v[32:35], 0\n"
"ds_read_b64_tr_b16 a[252:253], v243 offset:544\n"
"v_mul_f32 v110, v247, v110\n"
"v_mfma_f32_16x16x32_bf16 v[28:31], a[248:251], v[0:3], v[28:31]\n"
"s_waitcnt lgkmcnt(9)\n"
"ds_read_b64_tr_b16 a[248:249], v241 offset:544\n"
"v_mul_f32 v111, v247, v111\n"
"v_mfma_f32_16x16x32_bf16 v[4:7], a[224:227], v[56:59], v[4:7]\n"
"ds_read_b64_tr_b16 a[250:251], v241 offset:672\n"
"v_mul_f32 v116, v247, v116\n"
"v_mul_f32 v117, v247, v117\n"
"v_mul_f32 v118, v247, v118\n"
"v_mul_f32 v119, v247, v119\n"
"v_cvt_pk_bf16_f32 v108, v108, v109\n"
"v_cvt_pk_bf16_f32 v109, v110, v111\n"
"v_cvt_pk_bf16_f32 v110, v116, v117\n"
"v_cvt_pk_bf16_f32 v111, v118, v119\n"
"v_mfma_f32_16x16x32_bf16 v[4:7], a[228:231], v[88:91], v[4:7]\n"
"ds_read_b64_tr_b16 a[254:255], v243 offset:672\n"
"v_mul_f32 v140, v244, v140\n"
"v_mfma_f32_16x16x32_bf16 v[36:39], a[228:231], v[96:99], v[36:39]\n"
"buffer_store_dwordx4 v[60:63], v253, s[56:59], s76 offen sc1\n"
"v_mfma_f32_16x16x32_bf16 v[36:39], a[224:227], v[64:67], v[36:39]\n"
"v_mul_f32 v141, v244, v141\n"
"v_mul_f32 v142, v244, v142\n"
"v_mfma_f32_16x16x32_bf16 v[84:87], a[224:227], v[72:75], v[84:87]\n"
"v_mul_f32 v143, v244, v143\n"
"v_mul_f32 v172, v244, v172\n"
"v_mfma_f32_16x16x32_bf16 v[84:87], a[228:231], v[104:107], v[84:87]\n"
"v_mul_f32 v173, v244, v173\n"
"v_mul_f32 v174, v244, v174\n"
"v_mfma_f32_16x16x32_bf16 v[156:159], a[228:231], a[12:15], v[156:159]\n"
"s_waitcnt vmcnt(27) lgkmcnt(0)\n"
"s_barrier\n"
"v_mfma_f32_16x16x32_bf16 v[156:159], a[224:227], v[80:83], v[156:159]\n"
"buffer_load_dwordx4 v249, s[52:55], s80 offen offset:0 lds\n"
"v_mul_f32 v175, v244, v175\n"
"v_cvt_pk_bf16_f32 v140, v140, v141\n"
"v_cvt_pk_bf16_f32 v141, v142, v143\n"
"v_cvt_pk_bf16_f32 v142, v172, v173\n"
"v_cvt_pk_bf16_f32 v143, v174, v175\n"
"v_mfma_f32_16x16x32_bf16 v[164:167], a[232:235], v[80:83], v[164:167]\n"
"v_mul_f32 v196, v245, v196\n"
"v_mul_f32 v197, v245, v197\n"
"v_mfma_f32_16x16x32_bf16 v[164:167], a[236:239], a[12:15], v[164:167]\n"
"v_mul_f32 v198, v245, v198\n"
"v_mul_f32 v199, v245, v199\n"
"v_mfma_f32_16x16x32_bf16 v[204:207], a[236:239], v[104:107], v[204:207]\n"
"buffer_load_dwordx4 v250, s[52:55], s80 offen offset:1024 lds\n"
"v_mfma_f32_16x16x32_bf16 v[204:207], a[232:235], v[72:75], v[204:207]\n"
"s_addk_i32 m0, 0x820\n"
"v_mul_f32 v148, v245, v148\n"
"v_mul_f32 v149, v245, v149\n"
"v_mfma_f32_16x16x32_bf16 v[44:47], a[232:235], v[64:67], v[44:47]\n"
"v_mul_f32 v150, v245, v150\n"
"v_mul_f32 v151, v245, v151\n"
"v_mfma_f32_16x16x32_bf16 v[44:47], a[236:239], v[96:99], v[44:47]\n"
"buffer_store_dwordx4 v[108:111], v253, s[56:59], s78 offen sc1\n"
"v_mfma_f32_16x16x32_bf16 v[12:15], a[236:239], v[88:91], v[12:15]\n"
"v_cvt_pk_bf16_f32 v196, v196, v197\n"
"v_add_u32 v242, 0x20700, v242\n"
"v_cvt_pk_bf16_f32 v197, v198, v199\n"
"v_cvt_pk_bf16_f32 v198, v148, v149\n"
"v_cvt_pk_bf16_f32 v199, v150, v151\n"
"v_mfma_f32_16x16x32_bf16 v[12:15], a[232:235], v[56:59], v[12:15]\n"
"ds_read_b64_tr_b16 a[228:229], v242 offset:0\n"
"v_add_u32 v240, 0x20700, v240\n"
"v_mfma_f32_16x16x32_bf16 v[20:23], a[240:243], v[56:59], v[20:23]\n"
"buffer_load_dwordx4 v251, s[52:55], s80 offen offset:0 lds\n"
"v_mfma_f32_16x16x32_bf16 v[20:23], a[244:247], v[88:91], v[20:23]\n"
"ds_read_b64_tr_b16 a[230:231], v242 offset:128\n"
"v_mul_f32 v76, v246, v76\n"
"v_mfma_f32_16x16x32_bf16 v[52:55], a[244:247], v[96:99], v[52:55]\n"
"ds_read_b64_tr_b16 a[224:225], v240 offset:0\n"
"v_mul_f32 v77, v246, v77\n"
"v_mfma_f32_16x16x32_bf16 v[52:55], a[240:243], v[64:67], v[52:55]\n"
"buffer_load_dwordx4 v252, s[52:55], s80 offen offset:1024 lds\n"
"v_mfma_f32_16x16x32_bf16 v[212:215], a[240:243], v[72:75], v[212:215]\n"
"s_add_u32 m0, s79, 0x20700\n"
"v_mul_f32 v78, v246, v78\n"
"v_mul_f32 v79, v246, v79\n"
"v_mfma_f32_16x16x32_bf16 v[212:215], a[244:247], v[104:107], v[212:215]\n"
"s_add_u32 s80, s97, 0x0\n"
"ds_read_b64_tr_b16 a[226:227], v240 offset:128\n"
"v_mul_f32 v100, v246, v100\n"
"v_mul_f32 v101, v246, v101\n"
"v_mul_f32 v102, v246, v102\n"
"v_mul_f32 v103, v246, v103\n"
"v_cvt_pk_bf16_f32 v76, v76, v77\n"
"v_cvt_pk_bf16_f32 v77, v78, v79\n"
"v_cvt_pk_bf16_f32 v78, v100, v101\n"
"v_cvt_pk_bf16_f32 v79, v102, v103\n"
"v_mfma_f32_16x16x32_bf16 v[220:223], a[252:255], v[104:107], v[220:223]\n"
"buffer_store_dwordx4 v[140:143], v253, s[56:59], s73 offen sc1\n"
"v_mfma_f32_16x16x32_bf16 v[220:223], a[248:251], v[72:75], v[220:223]\n"
"ds_read_b64_tr_b16 a[236:237], v242 offset:32\n"
"v_add_u32 v241, 0x20700, v241\n"
"v_mfma_f32_16x16x32_bf16 v[92:95], a[248:251], v[64:67], v[92:95]\n"
"ds_read_b64_tr_b16 a[232:233], v240 offset:32\n"
"v_add_u32 v243, 0x20700, v243\n"
"v_mfma_f32_16x16x32_bf16 v[92:95], a[252:255], v[96:99], v[92:95]\n"
"buffer_store_dwordx4 v[196:199], v253, s[56:59], s75 offen sc1\n"
"v_mfma_f32_16x16x32_bf16 v[28:31], a[252:255], v[88:91], v[28:31]\n"
"ds_read_b64_tr_b16 a[234:235], v240 offset:160\n"
"v_add_u32 v116, s96, v248\n"
"v_mfma_f32_16x16x32_bf16 v[28:31], a[248:251], v[56:59], v[28:31]\n"
"s_waitcnt lgkmcnt(3)\n"
"ds_read_b64_tr_b16 a[238:239], v242 offset:160\n"
"v_add_u32 v132, s68, v248\n"
"v_mfma_f32_16x16x32_bf16 v[4:7], a[224:227], v[112:115], v[4:7]\n"
"buffer_store_dwordx4 v[76:79], v253, s[56:59], s77 offen sc1\n"
"v_mfma_f32_16x16x32_bf16 v[4:7], a[228:231], v[144:147], v[4:7]\n"
"ds_read_b64_tr_b16 a[240:241], v241 offset:0\n"
"v_add_u32 v172, s69, v248\n"
"v_mfma_f32_16x16x32_bf16 v[36:39], a[228:231], v[152:155], v[36:39]\n"
"ds_read_b64_tr_b16 a[242:243], v241 offset:128\n"
"v_add_u32 v253, 0x100000, v253\n"
"v_mfma_f32_16x16x32_bf16 v[36:39], a[224:227], v[120:123], v[36:39]\n"
"ds_read_b64_tr_b16 a[244:245], v243 offset:0\n"
"v_bfe_u32 v238, v248, 4, 6\n"
"v_mfma_f32_16x16x32_bf16 v[84:87], a[224:227], v[128:131], v[84:87]\n"
"ds_read_b64_tr_b16 a[246:247], v243 offset:128\n"
"v_and_b32 v239, 15, v238\n"
"v_mfma_f32_16x16x32_bf16 v[84:87], a[228:231], v[160:163], v[84:87]\n"
"ds_read_b64_tr_b16 a[252:253], v243 offset:32\n"
"v_mul_u32_u24 v234, 1792, v239\n"
"v_mfma_f32_16x16x32_bf16 v[156:159], a[228:231], a[20:23], v[156:159]\n"
"ds_read_b64_tr_b16 a[254:255], v243 offset:160\n"
"v_and_b32 v239, 32, v238\n"
"v_mfma_f32_16x16x32_bf16 v[156:159], a[224:227], v[136:139], v[156:159]\n"
"s_waitcnt lgkmcnt(7)\n"
"ds_read_b64_tr_b16 a[248:249], v241 offset:32\n"
"v_add_u32 v234, v234, v239\n"
"v_mfma_f32_16x16x32_bf16 v[164:167], a[232:235], v[136:139], v[164:167]\n"
"s_waitcnt lgkmcnt(7)\n"
"ds_read_b64_tr_b16 a[250:251], v241 offset:160\n"
"v_bfe_u32 v239, v238, 4, 1\n"
"v_mfma_f32_16x16x32_bf16 v[164:167], a[236:239], a[20:23], v[164:167]\n"
"ds_read_b64_tr_b16 a[228:229], v242 offset:512\n"
"v_lshl_add_u32 v234, v239, 3, v234\n"
"v_mfma_f32_16x16x32_bf16 v[204:207], a[236:239], v[160:163], v[204:207]\n"
"ds_read_b64_tr_b16 a[230:231], v242 offset:640\n"
"v_lshrrev_b32 v239, 2, v238\n"
"v_mfma_f32_16x16x32_bf16 v[204:207], a[232:235], v[128:131], v[204:207]\n"
"ds_read_b64_tr_b16 a[224:225], v240 offset:512\n"
"v_add_u32 v239, s27, v239\n"
"v_mfma_f32_16x16x32_bf16 v[44:47], a[232:235], v[120:123], v[44:47]\n"
"ds_read_b64_tr_b16 a[226:227], v240 offset:640\n"
"v_lshrrev_b32 v236, 1, v238\n"
"v_mfma_f32_16x16x32_bf16 v[44:47], a[236:239], v[152:155], v[44:47]\n"
"ds_write_b128 v116, v[108:111] offset:0\n"
"v_mul_u32_u24 v232, 8, v239\n"
"v_mfma_f32_16x16x32_bf16 v[12:15], a[236:239], v[144:147], v[12:15]\n"
"ds_read_b64_tr_b16 a[236:237], v242 offset:544\n"
"v_lshlrev_b32 v236, 24, v236\n"
"v_mfma_f32_16x16x32_bf16 v[12:15], a[232:235], v[112:115], v[12:15]\n"
"s_waitcnt lgkmcnt(11)\n"
"ds_read_b64_tr_b16 a[238:239], v242 offset:672\n"
"v_and_b32 v237, 1, v238\n"
"v_mfma_f32_16x16x32_bf16 v[20:23], a[240:243], v[112:115], v[20:23]\n"
"s_waitcnt lgkmcnt(11)\n"
"ds_read_b64_tr_b16 a[232:233], v240 offset:544\n"
"v_add_u32 v242, 0xfffe7ac0, v242\n"
"v_mfma_f32_16x16x32_bf16 v[20:23], a[244:247], v[144:147], v[20:23]\n"
"ds_read_b64_tr_b16 a[234:235], v240 offset:672\n"
"v_add_u32 v232, s86, v232\n"
"v_mfma_f32_16x16x32_bf16 v[52:55], a[244:247], v[152:155], v[52:55]\n"
"s_lshl_b32 s86, %2, 4\n"
"s_add_u32 s86, s86, s22\n"
"s_mul_i32 s87, s86, 8\n"
"ds_write_b128 v132, v[124:127] offset:0\n"
"v_add_u32 v240, 0xfffe7ac0, v240\n"
"v_mfma_f32_16x16x32_bf16 v[52:55], a[240:243], v[120:123], v[52:55]\n"
"ds_write_b128 v172, v[140:143] offset:0\n"
"v_lshl_add_u32 v236, v237, 13, v236\n"
"v_mfma_f32_16x16x32_bf16 v[212:215], a[240:243], v[128:131], v[212:215]\n"
"ds_read_b64_tr_b16 a[240:241], v241 offset:512\n"
"v_lshrrev_b32 v237, 1, v238\n"
"v_mfma_f32_16x16x32_bf16 v[212:215], a[244:247], v[160:163], v[212:215]\n"
"s_waitcnt lgkmcnt(11)\n"
"ds_read_b64_tr_b16 a[242:243], v241 offset:640\n"
"v_lshl_add_u32 v236, v237, 9, v236\n"
"v_mfma_f32_16x16x32_bf16 v[220:223], a[252:255], v[160:163], v[220:223]\n"
"ds_read_b64_tr_b16 a[244:245], v243 offset:512\n"
"v_add_u32 v236, s87, v236\n"
"v_mfma_f32_16x16x32_bf16 v[220:223], a[248:251], v[128:131], v[220:223]\n"
"s_cmp_eq_u32 s86, 0\n"
"s_cselect_b32 s86, 4096, 0x40000000\n"
"ds_read_b64_tr_b16 a[246:247], v243 offset:640\n"
"v_lshlrev_b32 v239, 24, v238\n"
"v_mfma_f32_16x16x32_bf16 v[92:95], a[248:251], v[120:123], v[92:95]\n"
"v_add_u32 v237, s86, v239\n"
"v_mov_b32 v231, s91\n"
"v_mfma_f32_16x16x32_bf16 v[92:95], a[252:255], v[152:155], v[92:95]\n"
"s_lshl_b32 s86, s31, 3\n"
"s_add_u32 s86, s86, s90\n"
"v_lshrrev_b32 v224, 2, v238\n"
"v_bfe_u32 v225, v238, 3, 1\n"
"v_mfma_f32_16x16x32_bf16 v[28:31], a[252:255], v[144:147], v[28:31]\n"
"ds_read_b64_tr_b16 a[252:253], v243 offset:544\n"
"v_mov_b32 v230, s86\n"
"v_mfma_f32_16x16x32_bf16 v[28:31], a[248:251], v[112:115], v[28:31]\n"
"s_waitcnt lgkmcnt(9)\n"
"ds_read_b64_tr_b16 a[248:249], v241 offset:544\n"
"v_add_u32 v230, 1, v230\n"
"v_mfma_f32_16x16x32_bf16 v[4:7], a[224:227], v[168:171], v[4:7]\n"
"s_lshl_b32 s86, 1, s22\n"
"ds_read_b64_tr_b16 a[250:251], v241 offset:672\n"
"v_xor_b32 v224, v224, v238\n"
"v_mfma_f32_16x16x32_bf16 v[4:7], a[228:231], v[200:203], v[4:7]\n"
"ds_read_b64_tr_b16 a[254:255], v243 offset:672\n"
"v_add_u32 v241, 0xfffe7ac0, v241\n"
"v_mfma_f32_16x16x32_bf16 v[36:39], a[228:231], v[208:211], v[36:39]\n"
"v_add_u32 v243, 0xfffe7ac0, v243\n"
"v_and_b32 v224, 4, v224\n"
"v_mfma_f32_16x16x32_bf16 v[36:39], a[224:227], v[176:179], v[36:39]\n"
"v_lshl_add_u32 v224, v225, 1, v224\n"
"v_mov_b32 v226, 0x3f80\n"
"v_mfma_f32_16x16x32_bf16 v[84:87], a[224:227], v[184:187], v[84:87]\n"
"v_mul_f32 v4, v244, v4\n"
"v_mul_f32 v5, v244, v5\n"
"v_mfma_f32_16x16x32_bf16 v[84:87], a[228:231], v[216:219], v[84:87]\n"
"v_mul_f32 v6, v244, v6\n"
"v_mul_f32 v7, v244, v7\n"
"v_mfma_f32_16x16x32_bf16 v[156:159], a[228:231], a[28:31], v[156:159]\n"
"s_waitcnt vmcnt(31) lgkmcnt(0)\n"
"s_barrier\n"
"v_mfma_f32_16x16x32_bf16 v[156:159], a[224:227], v[192:195], v[156:159]\n"
"buffer_load_dwordx4 v249, s[52:55], s81 offen offset:0 lds\n"
"v_mfma_f32_16x16x32_bf16 v[164:167], a[232:235], v[192:195], v[164:167]\n"
"ds_read_b64_tr_b16 a[228:229], v242 offset:0\n"
"v_cvt_pk_bf16_f32 v4, v4, v5\n"
"v_mfma_f32_16x16x32_bf16 v[164:167], a[236:239], a[28:31], v[164:167]\n"
"ds_read_b64_tr_b16 a[224:225], v240 offset:0\n"
"v_cvt_pk_bf16_f32 v5, v6, v7\n"
"v_mfma_f32_16x16x32_bf16 v[204:207], a[236:239], v[216:219], v[204:207]\n"
"buffer_load_dwordx4 v250, s[52:55], s81 offen offset:1024 lds\n"
"v_mfma_f32_16x16x32_bf16 v[204:207], a[232:235], v[184:187], v[204:207]\n"
"s_addk_i32 m0, 0x820\n"
"ds_read_b64_tr_b16 a[226:227], v240 offset:128\n"
"v_mul_f32 v36, v245, v36\n"
"v_mfma_f32_16x16x32_bf16 v[44:47], a[232:235], v[176:179], v[44:47]\n"
"ds_read_b64_tr_b16 a[230:231], v242 offset:128\n"
"v_mul_f32 v37, v245, v37\n"
"v_mfma_f32_16x16x32_bf16 v[44:47], a[236:239], v[208:211], v[44:47]\n"
"buffer_load_dwordx4 v251, s[52:55], s81 offen offset:0 lds\n"
"v_mfma_f32_16x16x32_bf16 v[12:15], a[236:239], v[200:203], v[12:15]\n"
"ds_read_b64_tr_b16 a[236:237], v242 offset:32\n"
"v_mul_f32 v38, v245, v38\n"
"v_mfma_f32_16x16x32_bf16 v[12:15], a[232:235], v[168:171], v[12:15]\n"
"ds_read_b64_tr_b16 a[238:239], v242 offset:160\n"
"v_mul_f32 v39, v245, v39\n"
"v_mfma_f32_16x16x32_bf16 v[20:23], a[240:243], v[168:171], v[20:23]\n"
"buffer_load_dwordx4 v252, s[52:55], s81 offen offset:1024 lds\n"
"v_mfma_f32_16x16x32_bf16 v[20:23], a[244:247], v[200:203], v[20:23]\n"
"s_add_u32 m0, s79, 0x81c0\n"
"ds_read_b64_tr_b16 a[232:233], v240 offset:32\n"
"v_mul_f32 v44, v245, v44\n"
"v_mfma_f32_16x16x32_bf16 v[52:55], a[244:247], v[208:211], v[52:55]\n"
"s_add_u32 s81, s97, 0x1c000\n"
"ds_read_b64_tr_b16 a[234:235], v240 offset:160\n"
"v_mul_f32 v12, v244, v12\n"
"v_mfma_f32_16x16x32_bf16 v[52:55], a[240:243], v[176:179], v[52:55]\n"
"v_mul_f32 v13, v244, v13\n"
"v_mul_f32 v14, v244, v14\n"
"v_mfma_f32_16x16x32_bf16 v[212:215], a[240:243], v[184:187], v[212:215]\n"
"ds_read_b64_tr_b16 a[240:241], v241 offset:0\n"
"v_mul_f32 v15, v244, v15\n"
"v_mfma_f32_16x16x32_bf16 v[212:215], a[244:247], v[216:219], v[212:215]\n"
"ds_read_b64_tr_b16 a[242:243], v241 offset:128\n"
"v_cvt_pk_bf16_f32 v6, v12, v13\n"
"v_mfma_f32_16x16x32_bf16 v[220:223], a[252:255], v[216:219], v[220:223]\n"
"ds_read_b64_tr_b16 a[244:245], v243 offset:0\n"
"v_cvt_pk_bf16_f32 v7, v14, v15\n"
"v_mfma_f32_16x16x32_bf16 v[220:223], a[248:251], v[184:187], v[220:223]\n"
"ds_read_b64_tr_b16 a[246:247], v243 offset:128\n"
"v_mul_f32 v45, v245, v45\n"
"v_mfma_f32_16x16x32_bf16 v[92:95], a[248:251], v[176:179], v[92:95]\n"
"v_mul_f32 v46, v245, v46\n"
"v_mul_f32 v47, v245, v47\n"
"v_mfma_f32_16x16x32_bf16 v[92:95], a[252:255], v[208:211], v[92:95]\n"
"v_cvt_pk_bf16_f32 v36, v36, v37\n"
"v_mul_f32 v84, v246, v84\n"
"v_mfma_f32_16x16x32_bf16 v[28:31], a[252:255], v[200:203], v[28:31]\n"
"ds_read_b64_tr_b16 a[252:253], v243 offset:32\n"
"v_cvt_pk_bf16_f32 v37, v38, v39\n"
"v_mfma_f32_16x16x32_bf16 v[28:31], a[248:251], v[168:171], v[28:31]\n"
"s_waitcnt lgkmcnt(7)\n"
"ds_read_b64_tr_b16 a[254:255], v243 offset:160\n"
"v_cvt_pk_bf16_f32 v38, v44, v45\n"
"v_mfma_f32_16x16x32_bf16 v[124:127], a[224:227], v[0:3], 0\n"
"ds_read_b64_tr_b16 a[248:249], v241 offset:32\n"
"v_cvt_pk_bf16_f32 v39, v46, v47\n"
"v_mfma_f32_16x16x32_bf16 v[124:127], a[228:231], v[32:35], v[124:127]\n"
"ds_read_b64_tr_b16 a[250:251], v241 offset:160\n"
"v_mul_f32 v85, v246, v85\n"
"v_mfma_f32_16x16x32_bf16 v[180:183], a[228:231], v[40:43], 0\n"
"v_mul_f32 v86, v246, v86\n"
"v_mul_f32 v87, v246, v87\n"
"v_mfma_f32_16x16x32_bf16 v[180:183], a[224:227], v[8:11], v[180:183]\n"
"v_mul_f32 v204, v246, v204\n"
"v_mul_f32 v205, v246, v205\n"
"v_mfma_f32_16x16x32_bf16 v[60:63], a[224:227], v[16:19], 0\n"
"v_mul_f32 v206, v246, v206\n"
"v_mul_f32 v207, v246, v207\n"
"v_mfma_f32_16x16x32_bf16 v[60:63], a[228:231], v[48:51], v[60:63]\n"
"PG14_%=:\n"
"s_cbranch_execz PGB3_%=\n"
"v_cvt_pk_bf16_f32 v84, v84, v85\n"
"v_mul_f32 v156, v247, v156\n"
"v_mfma_f32_16x16x32_bf16 v[108:111], a[228:231], a[4:7], 0\n"
"ds_read_b64_tr_b16 a[228:229], v242 offset:512\n"
"v_cvt_pk_bf16_f32 v85, v86, v87\n"
"v_mfma_f32_16x16x32_bf16 v[108:111], a[224:227], v[24:27], v[108:111]\n"
"s_waitcnt lgkmcnt(5)\n"
"ds_read_b64_tr_b16 a[230:231], v242 offset:640\n"
"v_cvt_pk_bf16_f32 v86, v204, v205\n"
"v_mfma_f32_16x16x32_bf16 v[116:119], a[232:235], v[24:27], 0\n"
"ds_read_b64_tr_b16 a[224:225], v240 offset:512\n"
"v_cvt_pk_bf16_f32 v87, v206, v207\n"
"v_mfma_f32_16x16x32_bf16 v[116:119], a[236:239], a[4:7], v[116:119]\n"
"ds_read_b64_tr_b16 a[226:227], v240 offset:640\n"
"v_mul_f32 v157, v247, v157\n"
"v_mfma_f32_16x16x32_bf16 v[68:71], a[236:239], v[48:51], 0\n"
"v_mul_f32 v158, v247, v158\n"
"v_mul_f32 v159, v247, v159\n"
"v_mfma_f32_16x16x32_bf16 v[68:71], a[232:235], v[16:19], v[68:71]\n"
"v_mul_f32 v164, v247, v164\n"
"v_mul_f32 v165, v247, v165\n"
"v_mfma_f32_16x16x32_bf16 v[188:191], a[232:235], v[8:11], 0\n"
"v_mul_f32 v166, v247, v166\n"
"v_mul_f32 v167, v247, v167\n"
"v_mfma_f32_16x16x32_bf16 v[188:191], a[236:239], v[40:43], v[188:191]\n"
"v_cvt_pk_bf16_f32 v156, v156, v157\n"
"v_mul_f32 v20, v244, v20\n"
"v_mfma_f32_16x16x32_bf16 v[132:135], a[236:239], v[32:35], 0\n"
"buffer_store_dwordx4 v[4:7], v253, s[56:59], s72 offen sc1\n"
"v_mfma_f32_16x16x32_bf16 v[132:135], a[232:235], v[0:3], v[132:135]\n"
"ds_read_b64_tr_b16 a[236:237], v242 offset:544\n"
"v_cvt_pk_bf16_f32 v157, v158, v159\n"
"v_mfma_f32_16x16x32_bf16 v[140:143], a[240:243], v[0:3], 0\n"
"ds_read_b64_tr_b16 a[238:239], v242 offset:672\n"
"v_cvt_pk_bf16_f32 v158, v164, v165\n"
"v_mfma_f32_16x16x32_bf16 v[140:143], a[244:247], v[32:35], v[140:143]\n"
"ds_read_b64_tr_b16 a[232:233], v240 offset:544\n"
"v_cvt_pk_bf16_f32 v159, v166, v167\n"
"v_mfma_f32_16x16x32_bf16 v[196:199], a[244:247], v[40:43], 0\n"
"ds_read_b64_tr_b16 a[234:235], v240 offset:672\n"
"v_mul_f32 v21, v244, v21\n"
"v_mfma_f32_16x16x32_bf16 v[196:199], a[240:243], v[8:11], v[196:199]\n"
"v_mul_f32 v22, v244, v22\n"
"v_mul_f32 v23, v244, v23\n"
"v_mfma_f32_16x16x32_bf16 v[76:79], a[240:243], v[16:19], 0\n"
"ds_read_b64_tr_b16 a[240:241], v241 offset:512\n"
"v_mul_f32 v28, v244, v28\n"
"v_mfma_f32_16x16x32_bf16 v[76:79], a[244:247], v[48:51], v[76:79]\n"
"s_waitcnt lgkmcnt(5)\n"
"ds_read_b64_tr_b16 a[242:243], v241 offset:640\n"
"v_mul_f32 v29, v244, v29\n"
"v_mfma_f32_16x16x32_bf16 v[100:103], a[252:255], v[48:51], 0\n"
"ds_read_b64_tr_b16 a[244:245], v243 offset:512\n"
"v_mul_f32 v30, v244, v30\n"
"v_mfma_f32_16x16x32_bf16 v[100:103], a[248:251], v[16:19], v[100:103]\n"
"s_waitcnt vmcnt(5)\n"
"buffer_store_dwordx2 v[230:231], v236, s[60:63], 0 offen sc1\n"
"v_mfma_f32_16x16x32_bf16 v[148:151], a[248:251], v[8:11], 0\n"
"ds_read_b64_tr_b16 a[246:247], v243 offset:640\n"
"v_mul_f32 v31, v244, v31\n"
"v_mfma_f32_16x16x32_bf16 v[148:151], a[252:255], v[40:43], v[148:151]\n"
"v_cvt_pk_bf16_f32 v20, v20, v21\n"
"v_mul_f32 v52, v245, v52\n"
"v_mfma_f32_16x16x32_bf16 v[172:175], a[252:255], v[32:35], 0\n"
"ds_read_b64_tr_b16 a[252:253], v243 offset:544\n"
"v_cvt_pk_bf16_f32 v21, v22, v23\n"
"v_mfma_f32_16x16x32_bf16 v[172:175], a[248:251], v[0:3], v[172:175]\n"
"ds_read_b64_tr_b16 a[248:249], v241 offset:544\n"
"v_cvt_pk_bf16_f32 v22, v28, v29\n"
"v_mfma_f32_16x16x32_bf16 v[124:127], a[224:227], v[56:59], v[124:127]\n"
"ds_read_b64_tr_b16 a[250:251], v241 offset:672\n"
"v_cvt_pk_bf16_f32 v23, v30, v31\n"
"v_mfma_f32_16x16x32_bf16 v[124:127], a[228:231], v[88:91], v[124:127]\n"
"ds_read_b64_tr_b16 a[254:255], v243 offset:672\n"
"v_mul_f32 v53, v245, v53\n"
"v_mfma_f32_16x16x32_bf16 v[180:183], a[228:231], v[96:99], v[180:183]\n"
"buffer_store_dwordx4 v[36:39], v253, s[56:59], s74 offen sc1\n"
"v_mfma_f32_16x16x32_bf16 v[180:183], a[224:227], v[64:67], v[180:183]\n"
"v_mul_f32 v54, v245, v54\n"
"v_mul_f32 v55, v245, v55\n"
"v_mfma_f32_16x16x32_bf16 v[60:63], a[224:227], v[72:75], v[60:63]\n"
"v_mul_f32 v92, v245, v92\n"
"v_mul_f32 v93, v245, v93\n"
"v_mfma_f32_16x16x32_bf16 v[60:63], a[228:231], v[104:107], v[60:63]\n"
"v_mul_f32 v94, v245, v94\n"
"v_mul_f32 v95, v245, v95\n"
"v_mfma_f32_16x16x32_bf16 v[108:111], a[228:231], a[12:15], v[108:111]\n"
"s_waitcnt lgkmcnt(0)\n"
"s_barrier\n"
"v_mfma_f32_16x16x32_bf16 v[108:111], a[224:227], v[80:83], v[108:111]\n"
"buffer_load_dwordx4 v249, s[52:55], s80 offen offset:0 lds\n"
"v_mfma_f32_16x16x32_bf16 v[116:119], a[232:235], v[80:83], v[116:119]\n"
"ds_read_b64_tr_b16 a[228:229], v242 offset:49824\n"
"v_cvt_pk_bf16_f32 v52, v52, v53\n"
"v_mfma_f32_16x16x32_bf16 v[116:119], a[236:239], a[12:15], v[116:119]\n"
"ds_read_b64_tr_b16 a[230:231], v242 offset:49952\n"
"v_cvt_pk_bf16_f32 v53, v54, v55\n"
"v_mfma_f32_16x16x32_bf16 v[68:71], a[236:239], v[104:107], v[68:71]\n"
"buffer_load_dwordx4 v250, s[52:55], s80 offen offset:1024 lds\n"
"v_mfma_f32_16x16x32_bf16 v[68:71], a[232:235], v[72:75], v[68:71]\n"
"s_addk_i32 m0, 0x820\n"
"ds_read_b64_tr_b16 a[224:225], v240 offset:49824\n"
"v_cvt_pk_bf16_f32 v54, v92, v93\n"
"v_mfma_f32_16x16x32_bf16 v[188:191], a[232:235], v[64:67], v[188:191]\n"
"ds_read_b64_tr_b16 a[226:227], v240 offset:49952\n"
"v_cvt_pk_bf16_f32 v55, v94, v95\n"
"v_mfma_f32_16x16x32_bf16 v[188:191], a[236:239], v[96:99], v[188:191]\n"
"buffer_load_dwordx4 v251, s[52:55], s80 offen offset:0 lds\n"
"v_mfma_f32_16x16x32_bf16 v[132:135], a[236:239], v[88:91], v[132:135]\n"
"ds_read_b64_tr_b16 a[236:237], v242 offset:49856\n"
"v_mul_f32 v212, v246, v212\n"
"v_mfma_f32_16x16x32_bf16 v[132:135], a[232:235], v[56:59], v[132:135]\n"
"ds_read_b64_tr_b16 a[232:233], v240 offset:49856\n"
"v_mul_f32 v213, v246, v213\n"
"v_mfma_f32_16x16x32_bf16 v[140:143], a[240:243], v[56:59], v[140:143]\n"
"buffer_load_dwordx4 v252, s[52:55], s80 offen offset:1024 lds\n"
"v_mfma_f32_16x16x32_bf16 v[140:143], a[244:247], v[88:91], v[140:143]\n"
"s_add_u32 m0, s79, 0x14460\n"
"ds_read_b64_tr_b16 a[234:235], v240 offset:49984\n"
"v_mul_f32 v214, v246, v214\n"
"v_mfma_f32_16x16x32_bf16 v[196:199], a[244:247], v[96:99], v[196:199]\n"
"ds_read_b64_tr_b16 a[238:239], v242 offset:49984\n"
"v_mul_f32 v215, v246, v215\n"
"v_mfma_f32_16x16x32_bf16 v[196:199], a[240:243], v[64:67], v[196:199]\n"
"buffer_store_dwordx4 v[84:87], v253, s[56:59], s76 offen sc1\n"
"v_mfma_f32_16x16x32_bf16 v[76:79], a[240:243], v[72:75], v[76:79]\n"
"ds_read_b64_tr_b16 a[240:241], v241 offset:49824\n"
"v_mul_f32 v220, v246, v220\n"
"v_mfma_f32_16x16x32_bf16 v[76:79], a[244:247], v[104:107], v[76:79]\n"
"ds_read_b64_tr_b16 a[242:243], v241 offset:49952\n"
"v_mul_f32 v221, v246, v221\n"
"v_mfma_f32_16x16x32_bf16 v[100:103], a[252:255], v[104:107], v[100:103]\n"
"ds_read_b64_tr_b16 a[244:245], v243 offset:49824\n"
"v_mul_f32 v222, v246, v222\n"
"v_mfma_f32_16x16x32_bf16 v[100:103], a[248:251], v[72:75], v[100:103]\n"
"ds_read_b64_tr_b16 a[246:247], v243 offset:49952\n"
"v_mul_f32 v223, v246, v223\n"
"v_mfma_f32_16x16x32_bf16 v[148:151], a[248:251], v[64:67], v[148:151]\n"
"v_cvt_pk_bf16_f32 v212, v212, v213\n"
"v_add_u32 v12, s68, v248\n"
"v_mfma_f32_16x16x32_bf16 v[148:151], a[252:255], v[96:99], v[148:151]\n"
"ds_write_b128 v12, v[4:7] offset:2048\n"
"v_cvt_pk_bf16_f32 v213, v214, v215\n"
"v_mfma_f32_16x16x32_bf16 v[172:175], a[252:255], v[88:91], v[172:175]\n"
"ds_read_b64_tr_b16 a[252:253], v243 offset:49856\n"
"v_cvt_pk_bf16_f32 v214, v220, v221\n"
"v_mfma_f32_16x16x32_bf16 v[172:175], a[248:251], v[56:59], v[172:175]\n"
"s_waitcnt lgkmcnt(8)\n"
"ds_read_b64_tr_b16 a[254:255], v243 offset:49984\n"
"v_cvt_pk_bf16_f32 v215, v222, v223\n"
"v_mfma_f32_16x16x32_bf16 v[124:127], a[224:227], v[112:115], v[124:127]\n"
"buffer_store_dwordx4 v[156:159], v253, s[56:59], s78 offen sc1\n"
"v_mfma_f32_16x16x32_bf16 v[124:127], a[228:231], v[144:147], v[124:127]\n"
"ds_read_b64_tr_b16 a[248:249], v241 offset:49856\n"
"v_add_u32 v164, s96, v248\n"
"v_mfma_f32_16x16x32_bf16 v[180:183], a[228:231], v[152:155], v[180:183]\n"
"ds_read_b64_tr_b16 a[250:251], v241 offset:49984\n"
"v_add_u32 v28, s69, v248\n"
"v_mfma_f32_16x16x32_bf16 v[180:183], a[224:227], v[120:123], v[180:183]\n"
"ds_write_b128 v164, v[156:159] offset:2048\n"
"v_bfe_u32 v225, v238, 1, 1\n"
"v_mfma_f32_16x16x32_bf16 v[60:63], a[224:227], v[128:131], v[60:63]\n"
"ds_write_b128 v28, v[20:23] offset:2048\n"
"v_add_u32 v224, v224, v225\n"
"v_mfma_f32_16x16x32_bf16 v[60:63], a[228:231], v[160:163], v[60:63]\n"
"v_and_b32 v225, 1, v238\n"
"v_cmp_eq_u32 vcc, 0, v224\n"
"v_mfma_f32_16x16x32_bf16 v[108:111], a[228:231], a[20:23], v[108:111]\n"
"ds_read_b64_tr_b16 a[228:229], v242 offset:50336\n"
"v_lshlrev_b32 v225, 4, v225\n"
"v_mfma_f32_16x16x32_bf16 v[108:111], a[224:227], v[136:139], v[108:111]\n"
"s_waitcnt lgkmcnt(8)\n"
"ds_read_b64_tr_b16 a[230:231], v242 offset:50464\n"
"v_lshlrev_b32 v225, v225, v226\n"
"v_mfma_f32_16x16x32_bf16 v[116:119], a[232:235], v[136:139], v[116:119]\n"
"ds_read_b64_tr_b16 a[224:225], v240 offset:50336\n"
"v_cndmask_b32 v226, 0, v225, vcc\n"
"v_mfma_f32_16x16x32_bf16 v[116:119], a[236:239], a[20:23], v[116:119]\n"
"ds_read_b64_tr_b16 a[226:227], v240 offset:50464\n"
"v_accvgpr_write_b32 a16, v226\n"
"v_mfma_f32_16x16x32_bf16 v[68:71], a[236:239], v[160:163], v[68:71]\n"
"v_and_b32 v226, 0xff00ff00, v226\n"
"v_cmp_eq_u32 vcc, 1, v224\n"
"v_mfma_f32_16x16x32_bf16 v[68:71], a[232:235], v[128:131], v[68:71]\n"
"v_accvgpr_write_b32 a0, v226\n"
"v_lshlrev_b32 v233, 4, v238\n"
"v_mfma_f32_16x16x32_bf16 v[188:191], a[232:235], v[120:123], v[188:191]\n"
"v_add_u32 v230, 1, v230\n"
"v_cndmask_b32 v226, 0, v225, vcc\n"
"v_mfma_f32_16x16x32_bf16 v[188:191], a[236:239], v[152:155], v[188:191]\n"
"v_accvgpr_write_b32 a17, v226\n"
"v_cmp_eq_u32 vcc, 2, v224\n"
"v_mfma_f32_16x16x32_bf16 v[132:135], a[236:239], v[144:147], v[132:135]\n"
"buffer_store_dwordx4 v[20:23], v253, s[56:59], s73 offen sc1\n"
"v_mfma_f32_16x16x32_bf16 v[132:135], a[232:235], v[112:115], v[132:135]\n"
"ds_read_b64_tr_b16 a[236:237], v242 offset:50368\n"
"v_and_b32 v226, 0xff00ff00, v226\n"
"v_mfma_f32_16x16x32_bf16 v[140:143], a[240:243], v[112:115], v[140:143]\n"
"ds_read_b64_tr_b16 a[238:239], v242 offset:50496\n"
"v_accvgpr_write_b32 a1, v226\n"
"v_mfma_f32_16x16x32_bf16 v[140:143], a[244:247], v[144:147], v[140:143]\n"
"ds_read_b64_tr_b16 a[232:233], v240 offset:50368\n"
"v_cndmask_b32 v226, 0, v225, vcc\n"
"v_mfma_f32_16x16x32_bf16 v[196:199], a[244:247], v[152:155], v[196:199]\n"
"ds_read_b64_tr_b16 a[234:235], v240 offset:50496\n"
"v_accvgpr_write_b32 a18, v226\n"
"v_mfma_f32_16x16x32_bf16 v[196:199], a[240:243], v[120:123], v[196:199]\n"
"v_and_b32 v226, 0xff00ff00, v226\n"
"v_cmp_eq_u32 vcc, 3, v224\n"
"v_mfma_f32_16x16x32_bf16 v[76:79], a[240:243], v[128:131], v[76:79]\n"
"ds_read_b64_tr_b16 a[240:241], v241 offset:50336\n"
"v_accvgpr_write_b32 a2, v226\n"
"v_mfma_f32_16x16x32_bf16 v[76:79], a[244:247], v[160:163], v[76:79]\n"
"s_waitcnt lgkmcnt(5)\n"
"ds_read_b64_tr_b16 a[242:243], v241 offset:50464\n"
"v_cndmask_b32 v226, 0, v225, vcc\n"
"v_mfma_f32_16x16x32_bf16 v[100:103], a[252:255], v[160:163], v[100:103]\n"
"ds_read_b64_tr_b16 a[244:245], v243 offset:50336\n"
"v_accvgpr_write_b32 a19, v226\n"
"v_mfma_f32_16x16x32_bf16 v[100:103], a[248:251], v[128:131], v[100:103]\n"
"ds_read_b64_tr_b16 a[246:247], v243 offset:50464\n"
"v_and_b32 v226, 0xff00ff00, v226\n"
"v_mfma_f32_16x16x32_bf16 v[148:151], a[248:251], v[120:123], v[148:151]\n"
"v_accvgpr_write_b32 a3, v226\n"
"v_and_b32 v225, 0x1f0, v248\n"
"v_mfma_f32_16x16x32_bf16 v[148:151], a[252:255], v[152:155], v[148:151]\n"
"v_mfma_f32_16x16x32_bf16 v[172:175], a[252:255], v[144:147], v[172:175]\n"
"ds_read_b64_tr_b16 a[252:253], v243 offset:50368\n"
"v_mfma_f32_16x16x32_bf16 v[172:175], a[248:251], v[112:115], v[172:175]\n"
"ds_read_b64_tr_b16 a[248:249], v241 offset:50368\n"
"v_mfma_f32_16x16x32_bf16 v[124:127], a[224:227], v[168:171], v[124:127]\n"
"ds_read_b64_tr_b16 a[250:251], v241 offset:50496\n"
"v_mfma_f32_16x16x32_bf16 v[124:127], a[228:231], v[200:203], v[124:127]\n"
"ds_read_b64_tr_b16 a[254:255], v243 offset:50496\n"
"v_mfma_f32_16x16x32_bf16 v[180:183], a[228:231], v[208:211], v[180:183]\n"
"buffer_store_dwordx4 v[52:55], v253, s[56:59], s75 offen sc1\n"
"v_mfma_f32_16x16x32_bf16 v[180:183], a[224:227], v[176:179], v[180:183]\n"
"v_mfma_f32_16x16x32_bf16 v[60:63], a[224:227], v[184:187], v[60:63]\n"
"v_mfma_f32_16x16x32_bf16 v[60:63], a[228:231], v[216:219], v[60:63]\n"
"buffer_load_dwordx2 v[228:229], v232, s[60:63], 0 offen sc1\n"
"v_mfma_f32_16x16x32_bf16 v[108:111], a[228:231], a[28:31], v[108:111]\n"
"s_waitcnt lgkmcnt(0)\n"
"s_barrier\n"
"v_mfma_f32_16x16x32_bf16 v[108:111], a[224:227], v[192:195], v[108:111]\n"
"ds_read_b64_tr_b16 a[228:229], v242 offset:16608\n"
"v_mul_f32 v124, v244, v124\n"
"v_mfma_f32_16x16x32_bf16 v[116:119], a[232:235], v[192:195], v[116:119]\n"
"buffer_load_dwordx4 v249, s[52:55], s81 offen offset:0 lds\n"
"v_mfma_f32_16x16x32_bf16 v[116:119], a[236:239], a[28:31], v[116:119]\n"
"ds_read_b64_tr_b16 a[224:225], v240 offset:16608\n"
"v_mul_f32 v125, v244, v125\n"
"v_mfma_f32_16x16x32_bf16 v[68:71], a[236:239], v[216:219], v[68:71]\n"
"ds_read_b64_tr_b16 a[226:227], v240 offset:16736\n"
"v_mul_f32 v126, v244, v126\n"
"v_mfma_f32_16x16x32_bf16 v[68:71], a[232:235], v[184:187], v[68:71]\n"
"buffer_load_dwordx4 v250, s[52:55], s81 offen offset:1024 lds\n"
"v_mfma_f32_16x16x32_bf16 v[188:191], a[232:235], v[176:179], v[188:191]\n"
"s_addk_i32 m0, 0x820\n"
"ds_read_b64_tr_b16 a[230:231], v242 offset:16736\n"
"v_mul_f32 v127, v244, v127\n"
"v_mfma_f32_16x16x32_bf16 v[188:191], a[236:239], v[208:211], v[188:191]\n"
"v_cvt_pk_bf16_f32 v124, v124, v125\n"
"v_mul_f32 v180, v245, v180\n"
"v_mfma_f32_16x16x32_bf16 v[132:135], a[236:239], v[200:203], v[132:135]\n"
"buffer_load_dwordx4 v251, s[52:55], s81 offen offset:0 lds\n"
"v_mfma_f32_16x16x32_bf16 v[132:135], a[232:235], v[168:171], v[132:135]\n"
"ds_read_b64_tr_b16 a[236:237], v242 offset:16640\n"
"v_cvt_pk_bf16_f32 v125, v126, v127\n"
"v_mfma_f32_16x16x32_bf16 v[140:143], a[240:243], v[168:171], v[140:143]\n"
"ds_read_b64_tr_b16 a[232:233], v240 offset:16640\n"
"v_mul_f32 v181, v245, v181\n"
"v_mfma_f32_16x16x32_bf16 v[140:143], a[244:247], v[200:203], v[140:143]\n"
"buffer_load_dwordx4 v252, s[52:55], s81 offen offset:1024 lds\n"
"v_mfma_f32_16x16x32_bf16 v[196:199], a[244:247], v[208:211], v[196:199]\n"
"ds_read_b64_tr_b16 a[234:235], v240 offset:16768\n"
"v_mul_f32 v132, v244, v132\n"
"v_mfma_f32_16x16x32_bf16 v[196:199], a[240:243], v[176:179], v[196:199]\n"
"ds_read_b64_tr_b16 a[238:239], v242 offset:16768\n"
"v_mul_f32 v133, v244, v133\n"
"v_mfma_f32_16x16x32_bf16 v[76:79], a[240:243], v[184:187], v[76:79]\n"
"buffer_store_dwordx4 v[212:215], v253, s[56:59], s77 offen sc1\n"
"v_mfma_f32_16x16x32_bf16 v[76:79], a[244:247], v[216:219], v[76:79]\n"
"ds_read_b64_tr_b16 a[240:241], v241 offset:16608\n"
"v_mul_f32 v134, v244, v134\n"
"v_mfma_f32_16x16x32_bf16 v[100:103], a[252:255], v[216:219], v[100:103]\n"
"v_add_u32 v253, 0x100000, v253\n"
"v_mul_f32 v135, v244, v135\n"
"v_mfma_f32_16x16x32_bf16 v[100:103], a[248:251], v[184:187], v[100:103]\n"
"v_xor_b32 v253, 0x2000, v253\n"
"v_cvt_pk_bf16_f32 v126, v132, v133\n"
"v_mfma_f32_16x16x32_bf16 v[148:151], a[248:251], v[176:179], v[148:151]\n"
"ds_read_b64_tr_b16 a[242:243], v241 offset:16736\n"
"v_cvt_pk_bf16_f32 v127, v134, v135\n"
"v_mfma_f32_16x16x32_bf16 v[148:151], a[252:255], v[208:211], v[148:151]\n"
"ds_read_b64_tr_b16 a[244:245], v243 offset:16608\n"
"v_mul_f32 v182, v245, v182\n"
"v_mfma_f32_16x16x32_bf16 v[172:175], a[252:255], v[200:203], v[172:175]\n"
"ds_read_b64_tr_b16 a[246:247], v243 offset:16736\n"
"v_mul_f32 v183, v245, v183\n"
"v_mfma_f32_16x16x32_bf16 v[172:175], a[248:251], v[168:171], v[172:175]\n"
"s_waitcnt lgkmcnt(7)\n"
"ds_read_b64_tr_b16 a[252:253], v243 offset:16640\n"
"v_mul_f32 v188, v245, v188\n"
"v_mfma_f32_16x16x32_bf16 v[4:7], a[224:227], v[0:3], 0\n"
"ds_read_b64_tr_b16 a[254:255], v243 offset:16768\n"
"v_mul_f32 v189, v245, v189\n"
"v_mfma_f32_16x16x32_bf16 v[4:7], a[228:231], v[32:35], v[4:7]\n"
"ds_read_b64_tr_b16 a[248:249], v241 offset:16640\n"
"v_mul_f32 v190, v245, v190\n"
"v_mfma_f32_16x16x32_bf16 v[36:39], a[228:231], v[40:43], 0\n"
"ds_read_b64_tr_b16 a[250:251], v241 offset:16768\n"
"v_mul_f32 v191, v245, v191\n"
"v_mfma_f32_16x16x32_bf16 v[36:39], a[224:227], v[8:11], v[36:39]\n"
"v_cvt_pk_bf16_f32 v180, v180, v181\n"
"v_mul_f32 v60, v246, v60\n"
"v_mfma_f32_16x16x32_bf16 v[84:87], a[224:227], v[16:19], 0\n"
"v_cvt_pk_bf16_f32 v181, v182, v183\n"
"v_mul_f32 v61, v246, v61\n"
"v_mfma_f32_16x16x32_bf16 v[84:87], a[228:231], v[48:51], v[84:87]\n"
"v_cvt_pk_bf16_f32 v182, v188, v189\n"
"v_cvt_pk_bf16_f32 v183, v190, v191\n"
"v_mfma_f32_16x16x32_bf16 v[156:159], a[228:231], a[4:7], 0\n"
"ds_read_b64_tr_b16 a[228:229], v242 offset:17120\n"
"v_mul_f32 v62, v246, v62\n"
"v_mfma_f32_16x16x32_bf16 v[156:159], a[224:227], v[24:27], v[156:159]\n"
"s_waitcnt lgkmcnt(8)\n"
"ds_read_b64_tr_b16 a[230:231], v242 offset:17248\n"
"v_mul_f32 v63, v246, v63\n"
"v_mfma_f32_16x16x32_bf16 v[164:167], a[232:235], v[24:27], 0\n"
"ds_read_b64_tr_b16 a[224:225], v240 offset:17120\n"
"v_mul_f32 v68, v246, v68\n"
"v_mfma_f32_16x16x32_bf16 v[164:167], a[236:239], a[4:7], v[164:167]\n"
"ds_read_b64_tr_b16 a[226:227], v240 offset:17248\n"
"v_mul_f32 v69, v246, v69\n"
"v_mfma_f32_16x16x32_bf16 v[204:207], a[236:239], v[48:51], 0\n"
"v_mul_f32 v70, v246, v70\n"
"v_mul_f32 v71, v246, v71\n"
"v_mfma_f32_16x16x32_bf16 v[204:207], a[232:235], v[16:19], v[204:207]\n"
"v_cvt_pk_bf16_f32 v60, v60, v61\n"
"v_mul_f32 v108, v247, v108\n"
"v_mfma_f32_16x16x32_bf16 v[44:47], a[232:235], v[8:11], 0\n"
"v_cvt_pk_bf16_f32 v61, v62, v63\n"
"v_mul_f32 v109, v247, v109\n"
"v_mfma_f32_16x16x32_bf16 v[44:47], a[236:239], v[40:43], v[44:47]\n"
"v_cvt_pk_bf16_f32 v62, v68, v69\n"
"v_cvt_pk_bf16_f32 v63, v70, v71\n"
"v_mfma_f32_16x16x32_bf16 v[12:15], a[236:239], v[32:35], 0\n"
"buffer_store_dwordx4 v[124:127], v253, s[56:59], s72 offen sc1\n"
"v_mfma_f32_16x16x32_bf16 v[12:15], a[232:235], v[0:3], v[12:15]\n"
"s_waitcnt lgkmcnt(4)\n"
"ds_read_b64_tr_b16 a[236:237], v242 offset:17152\n"
"v_mul_f32 v110, v247, v110\n"
"v_mfma_f32_16x16x32_bf16 v[20:23], a[240:243], v[0:3], 0\n"
"ds_read_b64_tr_b16 a[232:233], v240 offset:17152\n"
"v_mul_f32 v111, v247, v111\n"
"v_mfma_f32_16x16x32_bf16 v[20:23], a[244:247], v[32:35], v[20:23]\n"
"ds_read_b64_tr_b16 a[234:235], v240 offset:17280\n"
"v_mul_f32 v116, v247, v116\n"
"v_mfma_f32_16x16x32_bf16 v[52:55], a[244:247], v[40:43], 0\n"
"ds_read_b64_tr_b16 a[238:239], v242 offset:17280\n"
"v_add_u32 v240, 0x14460, v240\n"
"v_mfma_f32_16x16x32_bf16 v[52:55], a[240:243], v[8:11], v[52:55]\n"
"v_add_u32 v242, 0x14460, v242\n"
"v_mul_f32 v117, v247, v117\n"
"v_mfma_f32_16x16x32_bf16 v[212:215], a[240:243], v[16:19], 0\n"
"ds_read_b64_tr_b16 a[240:241], v241 offset:17120\n"
"v_mul_f32 v118, v247, v118\n"
"v_mfma_f32_16x16x32_bf16 v[212:215], a[244:247], v[48:51], v[212:215]\n"
"ds_read_b64_tr_b16 a[242:243], v241 offset:17248\n"
"v_mul_f32 v119, v247, v119\n"
"v_mfma_f32_16x16x32_bf16 v[220:223], a[252:255], v[48:51], 0\n"
"ds_read_b64_tr_b16 a[244:245], v243 offset:17120\n"
"v_cvt_pk_bf16_f32 v108, v108, v109\n"
"v_mfma_f32_16x16x32_bf16 v[220:223], a[248:251], v[16:19], v[220:223]\n"
"s_waitcnt vmcnt(6)\n"
"v_and_b32 v227, -9, v228\n"
"v_cmp_ne_u32 vcc, s91, v229\n"
"v_mfma_f32_16x16x32_bf16 v[92:95], a[248:251], v[8:11], 0\n"
"ds_read_b64_tr_b16 a[246:247], v243 offset:17248\n"
"v_subrev_u32 v227, s17, v227\n"
"v_mfma_f32_16x16x32_bf16 v[92:95], a[252:255], v[40:43], v[92:95]\n"
"v_cmp_lt_u32 s[88:89], 6, v227\n"
"v_cvt_pk_bf16_f32 v109, v110, v111\n"
"v_mfma_f32_16x16x32_bf16 v[28:31], a[252:255], v[32:35], 0\n"
"ds_read_b64_tr_b16 a[252:253], v243 offset:17152\n"
"v_cvt_pk_bf16_f32 v110, v116, v117\n"
"v_mfma_f32_16x16x32_bf16 v[28:31], a[248:251], v[0:3], v[28:31]\n"
"s_waitcnt lgkmcnt(8)\n"
"s_or_b64 vcc, vcc, s[88:89]\n"
"s_cbranch_scc1 SPIN0_%=\n"
"BACK0_%=:\n"
"v_mfma_f32_16x16x32_bf16 v[4:7], a[224:227], v[56:59], v[4:7]\n"
"s_add_u32 s17, s90, 2\n"
"buffer_store_dwordx4 v[180:183], v253, s[56:59], s74 offen sc1\n"
"v_mfma_f32_16x16x32_bf16 v[4:7], a[228:231], v[88:91], v[4:7]\n"
"ds_read_b64_tr_b16 a[254:255], v243 offset:17280\n"
"v_and_b32 v227, 8, v228\n"
"v_mfma_f32_16x16x32_bf16 v[36:39], a[228:231], v[96:99], v[36:39]\n"
"ds_read_b64_tr_b16 a[248:249], v241 offset:17152\n"
"v_cmp_ne_u32 s[88:89], 0, v227\n"
"v_bfe_u32 v227, v248, 9, 1\n"
"v_mfma_f32_16x16x32_bf16 v[36:39], a[224:227], v[64:67], v[36:39]\n"
"ds_read_b64_tr_b16 a[250:251], v241 offset:17280\n"
"v_lshlrev_b32 v227, 2, v227\n"
"v_mfma_f32_16x16x32_bf16 v[84:87], a[224:227], v[72:75], v[84:87]\n"
"v_lshrrev_b32 v238, v227, s88\n"
"v_lshrrev_b32 v239, v227, s89\n"
"v_lshrrev_b32 v227, 2, v227\n"
"v_lshrrev_b32 v224, v227, s86\n"
"v_bfe_u32 v254, v238, 0, 1\n"
"v_mfma_f32_16x16x32_bf16 v[84:87], a[228:231], v[104:107], v[84:87]\n"
"s_lshl_b32 s86, %2, 9\n"
"s_add_u32 s86, s86, 149472\n"
"v_lshl_add_u32 v254, v254, 13, v233\n"
"v_bfe_u32 v226, v224, 0, 1\n"
"v_lshl_add_u32 v254, v226, 29, v254\n"
"v_mfma_f32_16x16x32_bf16 v[156:159], a[228:231], a[12:15], v[156:159]\n"
"v_cvt_pk_bf16_f32 v111, v118, v119\n"
"v_bfe_u32 v235, v238, 8, 1\n"
"v_mfma_f32_16x16x32_bf16 v[156:159], a[224:227], v[80:83], v[156:159]\n"
"s_waitcnt lgkmcnt(5)\n"
"v_lshl_add_u32 v235, v235, 13, v233\n"
"v_bfe_u32 v226, v224, 2, 1\n"
"v_mfma_f32_16x16x32_bf16 v[164:167], a[232:235], v[80:83], v[164:167]\n"
"s_waitcnt lgkmcnt(0)\n"
"s_barrier\n"
"v_mfma_f32_16x16x32_bf16 v[164:167], a[236:239], a[12:15], v[164:167]\n"
"buffer_load_dwordx4 a[32:35], v254, s[56:59], s34 offen offset:0 sc1\n"
"v_mfma_f32_16x16x32_bf16 v[204:207], a[236:239], v[104:107], v[204:207]\n"
"ds_read_b64_tr_b16 a[228:229], v242 offset:0\n"
"v_lshl_add_u32 v235, v226, 29, v235\n"
"v_mfma_f32_16x16x32_bf16 v[204:207], a[232:235], v[72:75], v[204:207]\n"
"ds_read_b64_tr_b16 a[230:231], v242 offset:128\n"
"v_add_u32 v241, 0x14460, v241\n"
"v_mfma_f32_16x16x32_bf16 v[44:47], a[232:235], v[64:67], v[44:47]\n"
"ds_read_b64_tr_b16 a[224:225], v240 offset:0\n"
"v_mul_f32 v140, v244, v140\n"
"v_mfma_f32_16x16x32_bf16 v[44:47], a[236:239], v[96:99], v[44:47]\n"
"ds_read_b64_tr_b16 a[226:227], v240 offset:128\n"
"v_mul_f32 v141, v244, v141\n"
"v_mfma_f32_16x16x32_bf16 v[12:15], a[236:239], v[88:91], v[12:15]\n"
"buffer_store_dwordx4 v[60:63], v253, s[56:59], s76 offen sc1\n"
"v_mfma_f32_16x16x32_bf16 v[12:15], a[232:235], v[56:59], v[12:15]\n"
"ds_read_b64_tr_b16 a[236:237], v242 offset:32\n"
"v_mul_f32 v142, v244, v142\n"
"v_mfma_f32_16x16x32_bf16 v[20:23], a[240:243], v[56:59], v[20:23]\n"
"ds_read_b64_tr_b16 a[232:233], v240 offset:32\n"
"v_mul_f32 v143, v244, v143\n"
"v_mfma_f32_16x16x32_bf16 v[20:23], a[244:247], v[88:91], v[20:23]\n"
"ds_read_b64_tr_b16 a[234:235], v240 offset:160\n"
"v_mul_f32 v172, v244, v172\n"
"v_mfma_f32_16x16x32_bf16 v[52:55], a[244:247], v[96:99], v[52:55]\n"
"ds_read_b64_tr_b16 a[238:239], v242 offset:160\n"
"v_mul_f32 v173, v244, v173\n"
"v_mfma_f32_16x16x32_bf16 v[52:55], a[240:243], v[64:67], v[52:55]\n"
"v_mul_f32 v174, v244, v174\n"
"v_mul_f32 v175, v244, v175\n"
"v_mfma_f32_16x16x32_bf16 v[212:215], a[240:243], v[72:75], v[212:215]\n"
"ds_read_b64_tr_b16 a[240:241], v241 offset:0\n"
"v_cvt_pk_bf16_f32 v140, v140, v141\n"
"v_mfma_f32_16x16x32_bf16 v[212:215], a[244:247], v[104:107], v[212:215]\n"
"ds_read_b64_tr_b16 a[242:243], v241 offset:128\n"
"v_cvt_pk_bf16_f32 v141, v142, v143\n"
"v_mfma_f32_16x16x32_bf16 v[220:223], a[252:255], v[104:107], v[220:223]\n"
"v_cvt_pk_bf16_f32 v142, v172, v173\n"
"v_cvt_pk_bf16_f32 v143, v174, v175\n"
"v_mfma_f32_16x16x32_bf16 v[220:223], a[248:251], v[72:75], v[220:223]\n"
"v_add_u32 v243, 0x14460, v243\n"
"v_bfe_u32 v254, v238, 16, 1\n"
"v_mfma_f32_16x16x32_bf16 v[92:95], a[248:251], v[64:67], v[92:95]\n"
"ds_read_b64_tr_b16 a[244:245], v243 offset:0\n"
"v_lshl_add_u32 v254, v254, 13, v233\n"
"v_mfma_f32_16x16x32_bf16 v[92:95], a[252:255], v[96:99], v[92:95]\n"
"ds_read_b64_tr_b16 a[246:247], v243 offset:128\n"
"v_bfe_u32 v226, v224, 4, 1\n"
"v_mfma_f32_16x16x32_bf16 v[28:31], a[252:255], v[88:91], v[28:31]\n"
"ds_read_b64_tr_b16 a[252:253], v243 offset:32\n"
"v_lshl_add_u32 v254, v226, 29, v254\n"
"v_mfma_f32_16x16x32_bf16 v[28:31], a[248:251], v[56:59], v[28:31]\n"
"s_waitcnt lgkmcnt(8)\n"
"ds_read_b64_tr_b16 a[254:255], v243 offset:160\n"
"v_mul_f32 v196, v245, v196\n"
"v_mfma_f32_16x16x32_bf16 v[4:7], a[224:227], v[112:115], v[4:7]\n"
"buffer_store_dwordx4 v[108:111], v253, s[56:59], s78 offen sc1\n"
"v_mfma_f32_16x16x32_bf16 v[4:7], a[228:231], v[144:147], v[4:7]\n"
"ds_read_b64_tr_b16 a[248:249], v241 offset:32\n"
"v_mul_f32 v197, v245, v197\n"
"v_mfma_f32_16x16x32_bf16 v[36:39], a[228:231], v[152:155], v[36:39]\n"
"ds_read_b64_tr_b16 a[250:251], v241 offset:160\n"
"v_mul_f32 v198, v245, v198\n"
"v_mfma_f32_16x16x32_bf16 v[36:39], a[224:227], v[120:123], v[36:39]\n"
"v_mul_f32 v199, v245, v199\n"
"v_mul_f32 v148, v245, v148\n"
"v_mfma_f32_16x16x32_bf16 v[84:87], a[224:227], v[128:131], v[84:87]\n"
"buffer_load_dwordx4 a[36:39], v235, s[56:59], s34 offen offset:1024 sc1\n"
"v_mfma_f32_16x16x32_bf16 v[84:87], a[228:231], v[160:163], v[84:87]\n"
"v_mul_f32 v149, v245, v149\n"
"v_mul_f32 v150, v245, v150\n"
"v_mfma_f32_16x16x32_bf16 v[156:159], a[228:231], a[20:23], v[156:159]\n"
"ds_read_b64_tr_b16 a[228:229], v242 offset:512\n"
"v_mul_f32 v151, v245, v151\n"
"v_mfma_f32_16x16x32_bf16 v[156:159], a[224:227], v[136:139], v[156:159]\n"
"s_waitcnt lgkmcnt(7)\n"
"ds_read_b64_tr_b16 a[230:231], v242 offset:640\n"
"v_cvt_pk_bf16_f32 v196, v196, v197\n"
"v_mfma_f32_16x16x32_bf16 v[164:167], a[232:235], v[136:139], v[164:167]\n"
"ds_read_b64_tr_b16 a[224:225], v240 offset:512\n"
"v_cvt_pk_bf16_f32 v197, v198, v199\n"
"v_mfma_f32_16x16x32_bf16 v[164:167], a[236:239], a[20:23], v[164:167]\n"
"v_cvt_pk_bf16_f32 v198, v148, v149\n"
"v_cvt_pk_bf16_f32 v199, v150, v151\n"
"v_mfma_f32_16x16x32_bf16 v[204:207], a[236:239], v[160:163], v[204:207]\n"
"ds_read_b64_tr_b16 a[226:227], v240 offset:640\n"
"v_mul_f32 v76, v246, v76\n"
"v_mfma_f32_16x16x32_bf16 v[204:207], a[232:235], v[128:131], v[204:207]\n"
"v_mul_f32 v77, v246, v77\n"
"v_mul_f32 v78, v246, v78\n"
"v_mfma_f32_16x16x32_bf16 v[44:47], a[232:235], v[120:123], v[44:47]\n"
"v_mul_f32 v79, v246, v79\n"
"v_mul_f32 v100, v246, v100\n"
"v_mfma_f32_16x16x32_bf16 v[44:47], a[236:239], v[152:155], v[44:47]\n"
"v_mul_f32 v101, v246, v101\n"
"v_mul_f32 v102, v246, v102\n"
"v_mfma_f32_16x16x32_bf16 v[12:15], a[236:239], v[144:147], v[12:15]\n"
"buffer_store_dwordx4 v[140:143], v253, s[56:59], s73 offen sc1\n"
"v_mfma_f32_16x16x32_bf16 v[12:15], a[232:235], v[112:115], v[12:15]\n"
"ds_read_b64_tr_b16 a[236:237], v242 offset:544\n"
"v_mul_f32 v103, v246, v103\n"
"v_mfma_f32_16x16x32_bf16 v[20:23], a[240:243], v[112:115], v[20:23]\n"
"s_waitcnt lgkmcnt(5)\n"
"ds_read_b64_tr_b16 a[232:233], v240 offset:544\n"
"v_cvt_pk_bf16_f32 v76, v76, v77\n"
"v_mfma_f32_16x16x32_bf16 v[20:23], a[244:247], v[144:147], v[20:23]\n"
"ds_read_b64_tr_b16 a[234:235], v240 offset:672\n"
"v_cvt_pk_bf16_f32 v77, v78, v79\n"
"v_mfma_f32_16x16x32_bf16 v[52:55], a[244:247], v[152:155], v[52:55]\n"
"ds_read_b64_tr_b16 a[238:239], v242 offset:672\n"
"v_cvt_pk_bf16_f32 v78, v100, v101\n"
"v_mfma_f32_16x16x32_bf16 v[52:55], a[240:243], v[120:123], v[52:55]\n"
"v_cvt_pk_bf16_f32 v79, v102, v103\n"
"v_bfe_u32 v235, v238, 24, 1\n"
"v_mfma_f32_16x16x32_bf16 v[212:215], a[240:243], v[128:131], v[212:215]\n"
"ds_read_b64_tr_b16 a[240:241], v241 offset:512\n"
"v_lshl_add_u32 v235, v235, 13, v233\n"
"v_mfma_f32_16x16x32_bf16 v[212:215], a[244:247], v[160:163], v[212:215]\n"
"ds_read_b64_tr_b16 a[242:243], v241 offset:640\n"
"v_bfe_u32 v226, v224, 6, 1\n"
"v_mfma_f32_16x16x32_bf16 v[220:223], a[252:255], v[160:163], v[220:223]\n"
"ds_read_b64_tr_b16 a[244:245], v243 offset:512\n"
"v_lshl_add_u32 v235, v226, 29, v235\n"
"v_mfma_f32_16x16x32_bf16 v[220:223], a[248:251], v[128:131], v[220:223]\n"
"s_waitcnt vmcnt(7)\n"
"buffer_store_dwordx2 v[230:231], v236, s[60:63], 0 offen sc1\n"
"v_mfma_f32_16x16x32_bf16 v[92:95], a[248:251], v[120:123], v[92:95]\n"
"ds_read_b64_tr_b16 a[246:247], v243 offset:640\n"
"v_add_u32 v242, 0xffff3d60, v242\n"
"v_mfma_f32_16x16x32_bf16 v[92:95], a[252:255], v[152:155], v[92:95]\n"
"v_add_u32 v240, 0xffff3d60, v240\n"
"v_add_u32 v132, s68, v248\n"
"v_mfma_f32_16x16x32_bf16 v[28:31], a[252:255], v[144:147], v[28:31]\n"
"buffer_load_dwordx4 a[40:43], v254, s[56:59], s34 offen offset:2048 sc1\n"
"v_mfma_f32_16x16x32_bf16 v[28:31], a[248:251], v[112:115], v[28:31]\n"
"s_waitcnt lgkmcnt(7)\n"
"ds_read_b64_tr_b16 a[252:253], v243 offset:544\n"
"v_bfe_u32 v254, v239, 0, 1\n"
"v_mfma_f32_16x16x32_bf16 v[4:7], a[224:227], v[168:171], v[4:7]\n"
"ds_read_b64_tr_b16 a[254:255], v243 offset:672\n"
"v_lshl_add_u32 v254, v254, 13, v233\n"
"v_mfma_f32_16x16x32_bf16 v[4:7], a[228:231], v[200:203], v[4:7]\n"
"buffer_store_dwordx4 v[196:199], v253, s[56:59], s75 offen sc1\n"
"v_mfma_f32_16x16x32_bf16 v[36:39], a[228:231], v[208:211], v[36:39]\n"
"ds_read_b64_tr_b16 a[248:249], v241 offset:544\n"
"v_bfe_u32 v226, v224, 8, 1\n"
"v_mfma_f32_16x16x32_bf16 v[36:39], a[224:227], v[176:179], v[36:39]\n"
"ds_read_b64_tr_b16 a[250:251], v241 offset:672\n"
"v_lshl_add_u32 v254, v226, 29, v254\n"
"v_mfma_f32_16x16x32_bf16 v[84:87], a[224:227], v[184:187], v[84:87]\n"
"ds_write_b128 v132, v[124:127] offset:4096\n"
"v_add_u32 v116, s96, v248\n"
"v_mfma_f32_16x16x32_bf16 v[84:87], a[228:231], v[216:219], v[84:87]\n"
"ds_write_b128 v116, v[108:111] offset:4096\n"
"v_add_u32 v241, 0xffff3d60, v241\n"
"v_mfma_f32_16x16x32_bf16 v[156:159], a[228:231], a[28:31], v[156:159]\n"
"PG15_%=:\n"
"s_cbranch_execz PGB3_%=\n"
"v_mul_f32 v4, v244, v4\n"
"v_mul_f32 v5, v244, v5\n"
"v_mfma_f32_16x16x32_bf16 v[156:159], a[224:227], v[192:195], v[156:159]\n"
"s_waitcnt lgkmcnt(7)\n"
"v_mul_f32 v6, v244, v6\n"
"v_mul_f32 v7, v244, v7\n"
"v_mfma_f32_16x16x32_bf16 v[164:167], a[232:235], v[192:195], v[164:167]\n"
"s_waitcnt lgkmcnt(2)\n"
"s_barrier\n"
"v_mfma_f32_16x16x32_bf16 v[164:167], a[236:239], a[28:31], v[164:167]\n"
"buffer_store_dwordx4 v[76:79], v253, s[56:59], s77 offen sc1\n"
"v_mfma_f32_16x16x32_bf16 v[204:207], a[236:239], v[216:219], v[204:207]\n"
"ds_read_b64_tr_b16 a[228:229], v242 offset:0\n"
"v_cvt_pk_bf16_f32 v4, v4, v5\n"
"v_mfma_f32_16x16x32_bf16 v[204:207], a[232:235], v[184:187], v[204:207]\n"
"ds_read_b64_tr_b16 a[224:225], v240 offset:0\n"
"v_cvt_pk_bf16_f32 v5, v6, v7\n"
"v_mfma_f32_16x16x32_bf16 v[44:47], a[232:235], v[176:179], v[44:47]\n"
"ds_read_b64_tr_b16 a[226:227], v240 offset:128\n"
"v_add_u32 v253, 0x100000, v253\n"
"v_mfma_f32_16x16x32_bf16 v[44:47], a[236:239], v[208:211], v[44:47]\n"
"ds_read_b64_tr_b16 a[230:231], v242 offset:128\n"
"v_add_u32 v243, 0xffff3d60, v243\n"
"v_mfma_f32_16x16x32_bf16 v[12:15], a[236:239], v[200:203], v[12:15]\n"
"ds_read_b64_tr_b16 a[236:237], v242 offset:32\n"
"v_add_u32 v172, s69, v248\n"
"v_mfma_f32_16x16x32_bf16 v[12:15], a[232:235], v[168:171], v[12:15]\n"
"ds_read_b64_tr_b16 a[238:239], v242 offset:160\n"
"v_mul_f32 v36, v245, v36\n"
"v_mfma_f32_16x16x32_bf16 v[20:23], a[240:243], v[168:171], v[20:23]\n"
"buffer_load_dwordx4 a[44:47], v235, s[56:59], s34 offen offset:3072 sc1\n"
"v_mfma_f32_16x16x32_bf16 v[20:23], a[244:247], v[200:203], v[20:23]\n"
"ds_read_b64_tr_b16 a[232:233], v240 offset:32\n"
"v_bfe_u32 v235, v239, 8, 1\n"
"v_mfma_f32_16x16x32_bf16 v[52:55], a[244:247], v[208:211], v[52:55]\n"
"ds_read_b64_tr_b16 a[234:235], v240 offset:160\n"
"v_mul_f32 v12, v244, v12\n"
"v_mfma_f32_16x16x32_bf16 v[52:55], a[240:243], v[176:179], v[52:55]\n"
"v_mul_f32 v13, v244, v13\n"
"v_mul_f32 v14, v244, v14\n"
"v_mfma_f32_16x16x32_bf16 v[212:215], a[240:243], v[184:187], v[212:215]\n"
"ds_read_b64_tr_b16 a[240:241], v241 offset:0\n"
"v_mul_f32 v15, v244, v15\n"
"v_mfma_f32_16x16x32_bf16 v[212:215], a[244:247], v[216:219], v[212:215]\n"
"v_cvt_pk_bf16_f32 v6, v12, v13\n"
"v_cvt_pk_bf16_f32 v7, v14, v15\n"
"v_mfma_f32_16x16x32_bf16 v[220:223], a[252:255], v[216:219], v[220:223]\n"
"ds_read_b64_tr_b16 a[242:243], v241 offset:128\n"
"v_mul_f32 v37, v245, v37\n"
"v_mfma_f32_16x16x32_bf16 v[220:223], a[248:251], v[184:187], v[220:223]\n"
"ds_read_b64_tr_b16 a[244:245], v243 offset:0\n"
"v_mul_f32 v38, v245, v38\n"
"v_mfma_f32_16x16x32_bf16 v[92:95], a[248:251], v[176:179], v[92:95]\n"
"ds_read_b64_tr_b16 a[246:247], v243 offset:128\n"
"v_mul_f32 v39, v245, v39\n"
"v_mfma_f32_16x16x32_bf16 v[92:95], a[252:255], v[208:211], v[92:95]\n"
"ds_write_b128 v172, v[140:143] offset:4096\n"
"v_lshl_add_u32 v235, v235, 13, v233\n"
"v_mfma_f32_16x16x32_bf16 v[28:31], a[252:255], v[200:203], v[28:31]\n"
"ds_read_b64_tr_b16 a[252:253], v243 offset:32\n"
"v_bfe_u32 v226, v224, 10, 1\n"
"v_mfma_f32_16x16x32_bf16 v[28:31], a[248:251], v[168:171], v[28:31]\n"
"s_waitcnt lgkmcnt(8)\n"
"ds_read_b64_tr_b16 a[254:255], v243 offset:160\n"
"v_mul_f32 v44, v245, v44\n"
"v_mfma_f32_16x16x32_bf16 v[124:127], a[224:227], v[0:3], 0\n"
"ds_read_b64_tr_b16 a[248:249], v241 offset:32\n"
"v_mul_f32 v45, v245, v45\n"
"v_mfma_f32_16x16x32_bf16 v[124:127], a[228:231], v[32:35], v[124:127]\n"
"ds_read_b64_tr_b16 a[250:251], v241 offset:160\n"
"v_mul_f32 v46, v245, v46\n"
"v_mfma_f32_16x16x32_bf16 v[180:183], a[228:231], v[40:43], 0\n"
"v_mul_f32 v47, v245, v47\n"
"v_cvt_pk_bf16_f32 v36, v36, v37\n"
"v_mfma_f32_16x16x32_bf16 v[180:183], a[224:227], v[8:11], v[180:183]\n"
"v_cvt_pk_bf16_f32 v37, v38, v39\n"
"v_lshl_add_u32 v235, v226, 29, v235\n"
"v_mfma_f32_16x16x32_bf16 v[60:63], a[224:227], v[16:19], 0\n"
"v_cvt_pk_bf16_f32 v38, v44, v45\n"
"v_cvt_pk_bf16_f32 v39, v46, v47\n"
"v_mfma_f32_16x16x32_bf16 v[60:63], a[228:231], v[48:51], v[60:63]\n"
"v_mul_f32 v84, v246, v84\n"
"v_mul_f32 v85, v246, v85\n"
"v_mfma_f32_16x16x32_bf16 v[108:111], a[228:231], a[4:7], 0\n"
"ds_read_b64_tr_b16 a[228:229], v242 offset:512\n"
"v_mul_f32 v86, v246, v86\n"
"v_mfma_f32_16x16x32_bf16 v[108:111], a[224:227], v[24:27], v[108:111]\n"
"s_waitcnt lgkmcnt(7)\n"
"ds_read_b64_tr_b16 a[230:231], v242 offset:640\n"
"v_mul_f32 v87, v246, v87\n"
"v_mfma_f32_16x16x32_bf16 v[116:119], a[232:235], v[24:27], 0\n"
"buffer_load_dwordx4 a[48:51], v254, s[56:59], s35 offen offset:0 sc1\n"
"v_mfma_f32_16x16x32_bf16 v[116:119], a[236:239], a[4:7], v[116:119]\n"
"ds_read_b64_tr_b16 a[224:225], v240 offset:512\n"
"v_mul_f32 v204, v246, v204\n"
"v_mfma_f32_16x16x32_bf16 v[68:71], a[236:239], v[48:51], 0\n"
"ds_read_b64_tr_b16 a[226:227], v240 offset:640\n"
"v_mul_f32 v205, v246, v205\n"
"v_mfma_f32_16x16x32_bf16 v[68:71], a[232:235], v[16:19], v[68:71]\n"
"v_mul_f32 v206, v246, v206\n"
"v_mul_f32 v207, v246, v207\n"
"v_mfma_f32_16x16x32_bf16 v[188:191], a[232:235], v[8:11], 0\n"
"v_cvt_pk_bf16_f32 v84, v84, v85\n"
"v_bfe_u32 v254, v239, 16, 1\n"
"v_mfma_f32_16x16x32_bf16 v[188:191], a[236:239], v[40:43], v[188:191]\n"
"v_cvt_pk_bf16_f32 v85, v86, v87\n"
"v_lshl_add_u32 v254, v254, 13, v233\n"
"v_mfma_f32_16x16x32_bf16 v[132:135], a[236:239], v[32:35], 0\n"
"buffer_store_dwordx4 v[4:7], v253, s[56:59], s72 offen sc1\n"
"v_mfma_f32_16x16x32_bf16 v[132:135], a[232:235], v[0:3], v[132:135]\n"
"ds_read_b64_tr_b16 a[236:237], v242 offset:544\n"
"v_cvt_pk_bf16_f32 v86, v204, v205\n"
"v_mfma_f32_16x16x32_bf16 v[140:143], a[240:243], v[0:3], 0\n"
"s_waitcnt lgkmcnt(5)\n"
"ds_read_b64_tr_b16 a[232:233], v240 offset:544\n"
"v_cvt_pk_bf16_f32 v87, v206, v207\n"
"v_mfma_f32_16x16x32_bf16 v[140:143], a[244:247], v[32:35], v[140:143]\n"
"ds_read_b64_tr_b16 a[234:235], v240 offset:672\n"
"v_bfe_u32 v226, v224, 12, 1\n"
"v_mfma_f32_16x16x32_bf16 v[196:199], a[244:247], v[40:43], 0\n"
"ds_read_b64_tr_b16 a[238:239], v242 offset:672\n"
"v_lshl_add_u32 v254, v226, 29, v254\n"
"v_mfma_f32_16x16x32_bf16 v[196:199], a[240:243], v[8:11], v[196:199]\n"
"v_mul_f32 v156, v247, v156\n"
"v_mul_f32 v157, v247, v157\n"
"v_mfma_f32_16x16x32_bf16 v[76:79], a[240:243], v[16:19], 0\n"
"ds_read_b64_tr_b16 a[240:241], v241 offset:512\n"
"v_mul_f32 v158, v247, v158\n"
"v_mfma_f32_16x16x32_bf16 v[76:79], a[244:247], v[48:51], v[76:79]\n"
"ds_read_b64_tr_b16 a[242:243], v241 offset:640\n"
"v_mul_f32 v159, v247, v159\n"
"v_mfma_f32_16x16x32_bf16 v[100:103], a[252:255], v[48:51], 0\n"
"ds_read_b64_tr_b16 a[244:245], v243 offset:512\n"
"v_mul_f32 v164, v247, v164\n"
"v_mfma_f32_16x16x32_bf16 v[100:103], a[248:251], v[16:19], v[100:103]\n"
"ds_read_b64_tr_b16 a[246:247], v243 offset:640\n"
"v_mul_f32 v165, v247, v165\n"
"v_mfma_f32_16x16x32_bf16 v[148:151], a[248:251], v[8:11], 0\n"
"v_mul_f32 v166, v247, v166\n"
"v_mul_f32 v167, v247, v167\n"
"v_mfma_f32_16x16x32_bf16 v[148:151], a[252:255], v[40:43], v[148:151]\n"
"v_cvt_pk_bf16_f32 v156, v156, v157\n"
"v_bfe_u32 v226, v224, 14, 1\n"
"v_mfma_f32_16x16x32_bf16 v[172:175], a[252:255], v[32:35], 0\n"
"ds_read_b64_tr_b16 a[252:253], v243 offset:544\n"
"v_cvt_pk_bf16_f32 v157, v158, v159\n"
"v_mfma_f32_16x16x32_bf16 v[172:175], a[248:251], v[0:3], v[172:175]\n"
"s_waitcnt lgkmcnt(8)\n"
"ds_read_b64_tr_b16 a[254:255], v243 offset:672\n"
"v_cvt_pk_bf16_f32 v158, v164, v165\n"
"v_mfma_f32_16x16x32_bf16 v[124:127], a[224:227], v[56:59], v[124:127]\n"
"buffer_load_dwordx4 a[52:55], v235, s[56:59], s35 offen offset:1024 sc1\n"
"v_mfma_f32_16x16x32_bf16 v[124:127], a[228:231], v[88:91], v[124:127]\n"
"ds_read_b64_tr_b16 a[248:249], v241 offset:544\n"
"v_cvt_pk_bf16_f32 v159, v166, v167\n"
"v_mfma_f32_16x16x32_bf16 v[180:183], a[228:231], v[96:99], v[180:183]\n"
"ds_read_b64_tr_b16 a[250:251], v241 offset:672\n"
"v_bfe_u32 v235, v239, 24, 1\n"
"v_mfma_f32_16x16x32_bf16 v[180:183], a[224:227], v[64:67], v[180:183]\n"
"buffer_store_dwordx4 v[36:39], v253, s[56:59], s74 offen sc1\n"
"v_mfma_f32_16x16x32_bf16 v[60:63], a[224:227], v[72:75], v[60:63]\n"
"v_lshl_add_u32 v235, v235, 13, v233\n"
"v_mul_f32 v20, v244, v20\n"
"v_mfma_f32_16x16x32_bf16 v[60:63], a[228:231], v[104:107], v[60:63]\n"
"v_lshl_add_u32 v235, v226, 29, v235\n"
"v_mul_f32 v21, v244, v21\n"
"v_mfma_f32_16x16x32_bf16 v[108:111], a[228:231], a[12:15], v[108:111]\n"
"buffer_load_dwordx2 v[228:229], v232, s[60:63], 0 offen sc1\n"
"v_mfma_f32_16x16x32_bf16 v[108:111], a[224:227], v[80:83], v[108:111]\n"
"s_waitcnt lgkmcnt(0)\n"
"s_barrier\n"
"v_mfma_f32_16x16x32_bf16 v[116:119], a[232:235], v[80:83], v[116:119]\n"
"ds_read_b64_tr_b16 a[228:229], v242 offset:33216\n"
"v_mul_f32 v22, v244, v22\n"
"v_mfma_f32_16x16x32_bf16 v[116:119], a[236:239], a[12:15], v[116:119]\n"
"ds_read_b64_tr_b16 a[230:231], v242 offset:33344\n"
"v_mul_f32 v23, v244, v23\n"
"v_mfma_f32_16x16x32_bf16 v[68:71], a[236:239], v[104:107], v[68:71]\n"
"ds_read_b64_tr_b16 a[224:225], v240 offset:33216\n"
"v_mul_f32 v28, v244, v28\n"
"v_mfma_f32_16x16x32_bf16 v[68:71], a[232:235], v[72:75], v[68:71]\n"
"ds_read_b64_tr_b16 a[226:227], v240 offset:33344\n"
"v_mul_f32 v29, v244, v29\n"
"v_mfma_f32_16x16x32_bf16 v[188:191], a[232:235], v[64:67], v[188:191]\n"
"v_mul_f32 v30, v244, v30\n"
"v_mul_f32 v31, v244, v31\n"
"v_mfma_f32_16x16x32_bf16 v[188:191], a[236:239], v[96:99], v[188:191]\n"
"v_cvt_pk_bf16_f32 v20, v20, v21\n"
"v_mul_f32 v52, v245, v52\n"
"v_mfma_f32_16x16x32_bf16 v[132:135], a[236:239], v[88:91], v[132:135]\n"
"buffer_store_dwordx4 v[84:87], v253, s[56:59], s76 offen sc1\n"
"v_mfma_f32_16x16x32_bf16 v[132:135], a[232:235], v[56:59], v[132:135]\n"
"ds_read_b64_tr_b16 a[236:237], v242 offset:33248\n"
"v_cvt_pk_bf16_f32 v21, v22, v23\n"
"v_mfma_f32_16x16x32_bf16 v[140:143], a[240:243], v[56:59], v[140:143]\n"
"ds_read_b64_tr_b16 a[232:233], v240 offset:33248\n"
"v_cvt_pk_bf16_f32 v22, v28, v29\n"
"v_mfma_f32_16x16x32_bf16 v[140:143], a[244:247], v[88:91], v[140:143]\n"
"ds_read_b64_tr_b16 a[234:235], v240 offset:33376\n"
"v_cvt_pk_bf16_f32 v23, v30, v31\n"
"v_mfma_f32_16x16x32_bf16 v[196:199], a[244:247], v[96:99], v[196:199]\n"
"ds_read_b64_tr_b16 a[238:239], v242 offset:33376\n"
"v_mul_f32 v53, v245, v53\n"
"v_mfma_f32_16x16x32_bf16 v[196:199], a[240:243], v[64:67], v[196:199]\n"
"v_mul_f32 v54, v245, v54\n"
"v_mul_f32 v55, v245, v55\n"
"v_mfma_f32_16x16x32_bf16 v[76:79], a[240:243], v[72:75], v[76:79]\n"
"buffer_load_dwordx4 a[56:59], v254, s[56:59], s35 offen offset:2048 sc1\n"
"v_mfma_f32_16x16x32_bf16 v[76:79], a[244:247], v[104:107], v[76:79]\n"
"ds_read_b64_tr_b16 a[240:241], v241 offset:33216\n"
"v_mul_f32 v92, v245, v92\n"
"v_mfma_f32_16x16x32_bf16 v[100:103], a[252:255], v[104:107], v[100:103]\n"
"ds_read_b64_tr_b16 a[242:243], v241 offset:33344\n"
"v_mul_f32 v93, v245, v93\n"
"v_mfma_f32_16x16x32_bf16 v[100:103], a[248:251], v[72:75], v[100:103]\n"
"ds_read_b64_tr_b16 a[244:245], v243 offset:33216\n"
"v_mul_f32 v94, v245, v94\n"
"v_mfma_f32_16x16x32_bf16 v[148:151], a[248:251], v[64:67], v[148:151]\n"
"ds_read_b64_tr_b16 a[246:247], v243 offset:33344\n"
"v_mul_f32 v95, v245, v95\n"
"v_mfma_f32_16x16x32_bf16 v[148:151], a[252:255], v[96:99], v[148:151]\n"
"v_cvt_pk_bf16_f32 v52, v52, v53\n"
"v_add_u32 v233, 0x100000, v233\n"
"v_mfma_f32_16x16x32_bf16 v[172:175], a[252:255], v[88:91], v[172:175]\n"
"ds_read_b64_tr_b16 a[252:253], v243 offset:33248\n"
"v_cvt_pk_bf16_f32 v53, v54, v55\n"
"v_mfma_f32_16x16x32_bf16 v[172:175], a[248:251], v[56:59], v[172:175]\n"
"s_waitcnt lgkmcnt(8)\n"
"ds_read_b64_tr_b16 a[254:255], v243 offset:33376\n"
"v_cvt_pk_bf16_f32 v54, v92, v93\n"
"v_mfma_f32_16x16x32_bf16 v[124:127], a[224:227], v[112:115], v[124:127]\n"
"buffer_store_dwordx4 v[156:159], v253, s[56:59], s78 offen sc1\n"
"v_mfma_f32_16x16x32_bf16 v[124:127], a[228:231], v[144:147], v[124:127]\n"
"ds_read_b64_tr_b16 a[248:249], v241 offset:33248\n"
"v_cvt_pk_bf16_f32 v55, v94, v95\n"
"v_mfma_f32_16x16x32_bf16 v[180:183], a[228:231], v[152:155], v[180:183]\n"
"ds_read_b64_tr_b16 a[250:251], v241 offset:33376\n"
"v_bfe_u32 v254, v238, 0, 1\n"
"v_mfma_f32_16x16x32_bf16 v[180:183], a[224:227], v[120:123], v[180:183]\n"
"v_lshl_add_u32 v254, v254, 13, v233\n"
"v_bfe_u32 v226, v224, 0, 1\n"
"v_mfma_f32_16x16x32_bf16 v[60:63], a[224:227], v[128:131], v[60:63]\n"
"v_lshl_add_u32 v254, v226, 29, v254\n"
"v_mul_f32 v212, v246, v212\n"
"v_mfma_f32_16x16x32_bf16 v[60:63], a[228:231], v[160:163], v[60:63]\n"
"v_mul_f32 v213, v246, v213\n"
"v_mul_f32 v214, v246, v214\n"
"v_mfma_f32_16x16x32_bf16 v[108:111], a[228:231], a[20:23], v[108:111]\n"
"ds_read_b64_tr_b16 a[228:229], v242 offset:33728\n"
"v_mul_f32 v215, v246, v215\n"
"v_mfma_f32_16x16x32_bf16 v[108:111], a[224:227], v[136:139], v[108:111]\n"
"s_waitcnt lgkmcnt(6)\n"
"ds_read_b64_tr_b16 a[230:231], v242 offset:33856\n"
"v_mul_f32 v220, v246, v220\n"
"v_mfma_f32_16x16x32_bf16 v[116:119], a[232:235], v[136:139], v[116:119]\n"
"ds_read_b64_tr_b16 a[224:225], v240 offset:33728\n"
"v_mul_f32 v221, v246, v221\n"
"v_mfma_f32_16x16x32_bf16 v[116:119], a[236:239], a[20:23], v[116:119]\n"
"ds_read_b64_tr_b16 a[226:227], v240 offset:33856\n"
"v_mul_f32 v222, v246, v222\n"
"v_mfma_f32_16x16x32_bf16 v[68:71], a[236:239], v[160:163], v[68:71]\n"
"v_mul_f32 v223, v246, v223\n"
"v_cvt_pk_bf16_f32 v212, v212, v213\n"
"v_mfma_f32_16x16x32_bf16 v[68:71], a[232:235], v[128:131], v[68:71]\n"
"v_cvt_pk_bf16_f32 v213, v214, v215\n"
"v_add_u32 v12, s68, v248\n"
"v_mfma_f32_16x16x32_bf16 v[188:191], a[232:235], v[120:123], v[188:191]\n"
"buffer_load_dwordx4 a[60:63], v235, s[56:59], s35 offen offset:3072 sc1\n"
"v_mfma_f32_16x16x32_bf16 v[188:191], a[236:239], v[152:155], v[188:191]\n"
"v_cvt_pk_bf16_f32 v214, v220, v221\n"
"v_cvt_pk_bf16_f32 v215, v222, v223\n"
"v_mfma_f32_16x16x32_bf16 v[132:135], a[236:239], v[144:147], v[132:135]\n"
"ds_read_b64_tr_b16 a[236:237], v242 offset:33760\n"
"v_bfe_u32 v235, v238, 8, 1\n"
"v_mfma_f32_16x16x32_bf16 v[132:135], a[232:235], v[112:115], v[132:135]\n"
"buffer_store_dwordx4 v[20:23], v253, s[56:59], s73 offen sc1\n"
"v_mfma_f32_16x16x32_bf16 v[140:143], a[240:243], v[112:115], v[140:143]\n"
"s_waitcnt lgkmcnt(5)\n"
"ds_read_b64_tr_b16 a[232:233], v240 offset:33760\n"
"v_lshl_add_u32 v235, v235, 13, v233\n"
"v_mfma_f32_16x16x32_bf16 v[140:143], a[244:247], v[144:147], v[140:143]\n"
"ds_read_b64_tr_b16 a[234:235], v240 offset:33888\n"
"v_bfe_u32 v226, v224, 2, 1\n"
"v_mfma_f32_16x16x32_bf16 v[196:199], a[244:247], v[152:155], v[196:199]\n"
"ds_read_b64_tr_b16 a[238:239], v242 offset:33888\n"
"v_add_u32 v240, 0xfffefc80, v240\n"
"v_mfma_f32_16x16x32_bf16 v[196:199], a[240:243], v[120:123], v[196:199]\n"
"v_add_u32 v242, 0xfffefc80, v242\n"
"v_lshl_add_u32 v235, v226, 29, v235\n"
"v_mfma_f32_16x16x32_bf16 v[76:79], a[240:243], v[128:131], v[76:79]\n"
"ds_read_b64_tr_b16 a[240:241], v241 offset:33728\n"
"v_add_u32 v164, s96, v248\n"
"v_mfma_f32_16x16x32_bf16 v[76:79], a[244:247], v[160:163], v[76:79]\n"
"ds_read_b64_tr_b16 a[242:243], v241 offset:33856\n"
"v_add_u32 v28, s69, v248\n"
"v_mfma_f32_16x16x32_bf16 v[100:103], a[252:255], v[160:163], v[100:103]\n"
"ds_read_b64_tr_b16 a[244:245], v243 offset:33728\n"
"v_bfe_u32 v226, v224, 4, 1\n"
"v_mfma_f32_16x16x32_bf16 v[100:103], a[248:251], v[128:131], v[100:103]\n"
"s_waitcnt vmcnt(5)\n"
"v_and_b32 v227, -9, v228\n"
"v_cmp_ne_u32 vcc, s91, v229\n"
"v_mfma_f32_16x16x32_bf16 v[148:151], a[248:251], v[120:123], v[148:151]\n"
"ds_read_b64_tr_b16 a[246:247], v243 offset:33856\n"
"v_subrev_u32 v227, s17, v227\n"
"v_mfma_f32_16x16x32_bf16 v[148:151], a[252:255], v[152:155], v[148:151]\n"
"ds_write_b128 v12, v[4:7] offset:6144\n"
"v_cmp_lt_u32 s[88:89], 5, v227\n"
"v_mfma_f32_16x16x32_bf16 v[172:175], a[252:255], v[144:147], v[172:175]\n"
"ds_read_b64_tr_b16 a[252:253], v243 offset:33760\n"
"v_add_u32 v230, 2, v230\n"
"v_mfma_f32_16x16x32_bf16 v[172:175], a[248:251], v[112:115], v[172:175]\n"
"s_waitcnt lgkmcnt(9)\n"
"s_or_b64 vcc, vcc, s[88:89]\n"
"s_cbranch_scc1 SPIN1_%=\n"
"BACK1_%=:\n"
"v_mfma_f32_16x16x32_bf16 v[124:127], a[224:227], v[168:171], v[124:127]\n"
"buffer_store_dwordx4 v[52:55], v253, s[56:59], s75 offen sc1\n"
"v_mfma_f32_16x16x32_bf16 v[124:127], a[228:231], v[200:203], v[124:127]\n"
"ds_read_b64_tr_b16 a[254:255], v243 offset:33888\n"
"v_add_u32 v225, s86, v225\n"
"v_mfma_f32_16x16x32_bf16 v[180:183], a[228:231], v[208:211], v[180:183]\n"
"ds_read_b64_tr_b16 a[248:249], v241 offset:33760\n"
"v_add_u32 v243, 0xfffefc80, v243\n"
"v_mfma_f32_16x16x32_bf16 v[180:183], a[224:227], v[176:179], v[180:183]\n"
"ds_read_b64_tr_b16 a[250:251], v241 offset:33888\n"
"v_add_u32 v241, 0xfffefc80, v241\n"
"v_mfma_f32_16x16x32_bf16 v[60:63], a[224:227], v[184:187], v[60:63]\n"
"ds_write_b128 v164, v[156:159] offset:6144\n"
"v_mul_f32 v124, v244, v124\n"
"v_mfma_f32_16x16x32_bf16 v[60:63], a[228:231], v[216:219], v[60:63]\n"
"v_mul_f32 v125, v244, v125\n"
"v_mul_f32 v126, v244, v126\n"
"v_mfma_f32_16x16x32_bf16 v[108:111], a[228:231], a[28:31], v[108:111]\n"
"v_mul_f32 v127, v244, v127\n"
"v_cvt_pk_bf16_f32 v124, v124, v125\n"
"v_mfma_f32_16x16x32_bf16 v[108:111], a[224:227], v[192:195], v[108:111]\n"
"s_waitcnt lgkmcnt(7)\n"
"ds_write_b128 v28, v[20:23] offset:6144\n"
"v_cvt_pk_bf16_f32 v125, v126, v127\n"
"v_mfma_f32_16x16x32_bf16 v[116:119], a[232:235], v[192:195], v[116:119]\n"
"s_waitcnt lgkmcnt(2)\n"
"s_barrier\n"
"v_mfma_f32_16x16x32_bf16 v[116:119], a[236:239], a[28:31], v[116:119]\n"
"buffer_load_dwordx4 a[64:67], v254, s[56:59], s34 offen offset:0 sc1\n"
"v_mfma_f32_16x16x32_bf16 v[68:71], a[236:239], v[216:219], v[68:71]\n"
"ds_read_b64_tr_b16 a[228:229], v242 offset:0\n"
"v_bfe_u32 v254, v238, 16, 1\n"
"v_mfma_f32_16x16x32_bf16 v[68:71], a[232:235], v[184:187], v[68:71]\n"
"ds_read_b64_tr_b16 a[224:225], v240 offset:0\n"
"v_lshl_add_u32 v254, v254, 13, v233\n"
"v_mfma_f32_16x16x32_bf16 v[188:191], a[232:235], v[176:179], v[188:191]\n"
"ds_read_b64_tr_b16 a[226:227], v240 offset:128\n"
"v_lshl_add_u32 v254, v226, 29, v254\n"
"v_mfma_f32_16x16x32_bf16 v[188:191], a[236:239], v[208:211], v[188:191]\n"
"ds_read_b64_tr_b16 a[230:231], v242 offset:128\n"
"v_mul_f32 v180, v245, v180\n"
"v_mfma_f32_16x16x32_bf16 v[132:135], a[236:239], v[200:203], v[132:135]\n"
"buffer_store_dwordx4 v[212:215], v253, s[56:59], s77 offen sc1\n"
"v_mfma_f32_16x16x32_bf16 v[132:135], a[232:235], v[168:171], v[132:135]\n"
"ds_read_b64_tr_b16 a[236:237], v242 offset:32\n"
"v_mul_f32 v181, v245, v181\n"
"v_mfma_f32_16x16x32_bf16 v[140:143], a[240:243], v[168:171], v[140:143]\n"
"ds_read_b64_tr_b16 a[232:233], v240 offset:32\n"
"v_add_u32 v253, 0x100000, v253\n"
"v_mfma_f32_16x16x32_bf16 v[140:143], a[244:247], v[200:203], v[140:143]\n"
"ds_read_b64_tr_b16 a[234:235], v240 offset:160\n"
"v_xor_b32 v253, 0x2000, v253\n"
"v_mfma_f32_16x16x32_bf16 v[196:199], a[244:247], v[208:211], v[196:199]\n"
"ds_read_b64_tr_b16 a[238:239], v242 offset:160\n"
"v_mul_f32 v132, v244, v132\n"
"v_mfma_f32_16x16x32_bf16 v[196:199], a[240:243], v[176:179], v[196:199]\n"
"v_mul_f32 v133, v244, v133\n"
"v_mul_f32 v134, v244, v134\n"
"v_mfma_f32_16x16x32_bf16 v[76:79], a[240:243], v[184:187], v[76:79]\n"
"ds_read_b64_tr_b16 a[240:241], v241 offset:0\n"
"v_mul_f32 v135, v244, v135\n"
"v_mfma_f32_16x16x32_bf16 v[76:79], a[244:247], v[216:219], v[76:79]\n"
"v_cvt_pk_bf16_f32 v126, v132, v133\n"
"v_cvt_pk_bf16_f32 v127, v134, v135\n"
"v_mfma_f32_16x16x32_bf16 v[100:103], a[252:255], v[216:219], v[100:103]\n"
"ds_read_b64_tr_b16 a[242:243], v241 offset:128\n"
"v_mul_f32 v182, v245, v182\n"
"v_mfma_f32_16x16x32_bf16 v[100:103], a[248:251], v[184:187], v[100:103]\n"
"ds_read_b64_tr_b16 a[244:245], v243 offset:0\n"
"v_mul_f32 v183, v245, v183\n"
"v_mfma_f32_16x16x32_bf16 v[148:151], a[248:251], v[176:179], v[148:151]\n"
"ds_read_b64_tr_b16 a[246:247], v243 offset:128\n"
"v_mul_f32 v188, v245, v188\n"
"v_mfma_f32_16x16x32_bf16 v[148:151], a[252:255], v[208:211], v[148:151]\n"
"v_mul_f32 v189, v245, v189\n"
"v_mul_f32 v190, v245, v190\n"
"v_mfma_f32_16x16x32_bf16 v[172:175], a[252:255], v[200:203], v[172:175]\n"
"ds_read_b64_tr_b16 a[252:253], v243 offset:32\n"
"v_mul_f32 v191, v245, v191\n"
"v_mfma_f32_16x16x32_bf16 v[172:175], a[248:251], v[168:171], v[172:175]\n"
"s_waitcnt lgkmcnt(8)\n"
"ds_read_b64_tr_b16 a[254:255], v243 offset:160\n"
"v_cvt_pk_bf16_f32 v180, v180, v181\n"
"v_mfma_f32_16x16x32_bf16 v[4:7], a[224:227], v[0:3], 0\n"
"ds_read_b64_tr_b16 a[248:249], v241 offset:32\n"
"v_cvt_pk_bf16_f32 v181, v182, v183\n"
"v_mfma_f32_16x16x32_bf16 v[4:7], a[228:231], v[32:35], v[4:7]\n"
"ds_read_b64_tr_b16 a[250:251], v241 offset:160\n"
"v_cvt_pk_bf16_f32 v182, v188, v189\n"
"v_mfma_f32_16x16x32_bf16 v[36:39], a[228:231], v[40:43], 0\n"
"v_cvt_pk_bf16_f32 v183, v190, v191\n"
"v_mul_f32 v60, v246, v60\n"
"v_mfma_f32_16x16x32_bf16 v[36:39], a[224:227], v[8:11], v[36:39]\n"
"v_mul_f32 v61, v246, v61\n"
"v_mul_f32 v62, v246, v62\n"
"v_mfma_f32_16x16x32_bf16 v[84:87], a[224:227], v[16:19], 0\n"
"buffer_load_dwordx4 a[68:71], v235, s[56:59], s34 offen offset:1024 sc1\n"
"v_mfma_f32_16x16x32_bf16 v[84:87], a[228:231], v[48:51], v[84:87]\n"
"v_mul_f32 v63, v246, v63\n"
"v_mul_f32 v68, v246, v68\n"
"v_mfma_f32_16x16x32_bf16 v[156:159], a[228:231], a[4:7], 0\n"
"ds_read_b64_tr_b16 a[228:229], v242 offset:512\n"
"v_mul_f32 v69, v246, v69\n"
"v_mfma_f32_16x16x32_bf16 v[156:159], a[224:227], v[24:27], v[156:159]\n"
"s_waitcnt lgkmcnt(6)\n"
"ds_read_b64_tr_b16 a[230:231], v242 offset:640\n"
"v_mul_f32 v70, v246, v70\n"
"v_mfma_f32_16x16x32_bf16 v[164:167], a[232:235], v[24:27], 0\n"
"ds_read_b64_tr_b16 a[224:225], v240 offset:512\n"
"v_mul_f32 v71, v246, v71\n"
"v_mfma_f32_16x16x32_bf16 v[164:167], a[236:239], a[4:7], v[164:167]\n"
"ds_read_b64_tr_b16 a[226:227], v240 offset:640\n"
"v_cvt_pk_bf16_f32 v60, v60, v61\n"
"v_mfma_f32_16x16x32_bf16 v[204:207], a[236:239], v[48:51], 0\n"
"v_cvt_pk_bf16_f32 v61, v62, v63\n"
"v_bfe_u32 v235, v238, 24, 1\n"
"v_mfma_f32_16x16x32_bf16 v[204:207], a[232:235], v[16:19], v[204:207]\n"
"v_cvt_pk_bf16_f32 v62, v68, v69\n"
"v_cvt_pk_bf16_f32 v63, v70, v71\n"
"v_mfma_f32_16x16x32_bf16 v[44:47], a[232:235], v[8:11], 0\n"
"v_lshl_add_u32 v235, v235, 13, v233\n"
"v_bfe_u32 v226, v224, 6, 1\n"
"v_mfma_f32_16x16x32_bf16 v[44:47], a[236:239], v[40:43], v[44:47]\n"
"v_lshl_add_u32 v235, v226, 29, v235\n"
"v_mul_f32 v108, v247, v108\n"
"v_mfma_f32_16x16x32_bf16 v[12:15], a[236:239], v[32:35], 0\n"
"buffer_store_dwordx4 v[124:127], v253, s[56:59], s72 offen sc1\n"
"v_mfma_f32_16x16x32_bf16 v[12:15], a[232:235], v[0:3], v[12:15]\n"
"ds_read_b64_tr_b16 a[236:237], v242 offset:544\n"
"v_mul_f32 v109, v247, v109\n"
"v_mfma_f32_16x16x32_bf16 v[20:23], a[240:243], v[0:3], 0\n"
"s_waitcnt lgkmcnt(5)\n"
"ds_read_b64_tr_b16 a[232:233], v240 offset:544\n"
"v_mul_f32 v110, v247, v110\n"
"v_mfma_f32_16x16x32_bf16 v[20:23], a[244:247], v[32:35], v[20:23]\n"
"ds_read_b64_tr_b16 a[234:235], v240 offset:672\n"
"v_mul_f32 v111, v247, v111\n"
"v_mfma_f32_16x16x32_bf16 v[52:55], a[244:247], v[40:43], 0\n"
"ds_read_b64_tr_b16 a[238:239], v242 offset:672\n"
"v_add_u32 v240, 0x20700, v240\n"
"v_mfma_f32_16x16x32_bf16 v[52:55], a[240:243], v[8:11], v[52:55]\n"
"v_add_u32 v242, 0x20700, v242\n"
"v_mul_f32 v116, v247, v116\n"
"v_mfma_f32_16x16x32_bf16 v[212:215], a[240:243], v[16:19], 0\n"
"ds_read_b64_tr_b16 a[240:241], v241 offset:512\n"
"v_mul_f32 v117, v247, v117\n"
"v_mfma_f32_16x16x32_bf16 v[212:215], a[244:247], v[48:51], v[212:215]\n"
"ds_read_b64_tr_b16 a[242:243], v241 offset:640\n"
"v_mul_f32 v118, v247, v118\n"
"v_mfma_f32_16x16x32_bf16 v[220:223], a[252:255], v[48:51], 0\n"
"ds_read_b64_tr_b16 a[244:245], v243 offset:512\n"
"v_mul_f32 v119, v247, v119\n"
"v_mfma_f32_16x16x32_bf16 v[220:223], a[248:251], v[16:19], v[220:223]\n"
"ds_read_b64_tr_b16 a[246:247], v243 offset:640\n"
"v_cvt_pk_bf16_f32 v108, v108, v109\n"
"v_mfma_f32_16x16x32_bf16 v[92:95], a[248:251], v[8:11], 0\n"
"buffer_load_dwordx4 a[72:75], v254, s[56:59], s34 offen offset:2048 sc1\n"
"v_mfma_f32_16x16x32_bf16 v[92:95], a[252:255], v[40:43], v[92:95]\n"
"v_cvt_pk_bf16_f32 v109, v110, v111\n"
"v_bfe_u32 v254, v239, 0, 1\n"
"v_mfma_f32_16x16x32_bf16 v[28:31], a[252:255], v[32:35], 0\n"
"ds_read_b64_tr_b16 a[252:253], v243 offset:544\n"
"v_cvt_pk_bf16_f32 v110, v116, v117\n"
"v_mfma_f32_16x16x32_bf16 v[28:31], a[248:251], v[0:3], v[28:31]\n"
"s_waitcnt lgkmcnt(8)\n"
"ds_read_b64_tr_b16 a[254:255], v243 offset:672\n"
"v_cvt_pk_bf16_f32 v111, v118, v119\n"
"v_mfma_f32_16x16x32_bf16 v[4:7], a[224:227], v[56:59], v[4:7]\n"
"buffer_store_dwordx4 v[180:183], v253, s[56:59], s74 offen sc1\n"
"v_mfma_f32_16x16x32_bf16 v[4:7], a[228:231], v[88:91], v[4:7]\n"
"ds_read_b64_tr_b16 a[248:249], v241 offset:544\n"
"v_lshl_add_u32 v254, v254, 13, v233\n"
"v_mfma_f32_16x16x32_bf16 v[36:39], a[228:231], v[96:99], v[36:39]\n"
"ds_read_b64_tr_b16 a[250:251], v241 offset:672\n"
"v_bfe_u32 v226, v224, 8, 1\n"
"v_mfma_f32_16x16x32_bf16 v[36:39], a[224:227], v[64:67], v[36:39]\n"
"v_lshl_add_u32 v254, v226, 29, v254\n"
"v_add_u32 v241, 0x20700, v241\n"
"v_mfma_f32_16x16x32_bf16 v[84:87], a[224:227], v[72:75], v[84:87]\n"
"v_mul_f32 v140, v244, v140\n"
"v_mul_f32 v141, v244, v141\n"
"v_mfma_f32_16x16x32_bf16 v[84:87], a[228:231], v[104:107], v[84:87]\n"
"v_mul_f32 v142, v244, v142\n"
"v_mul_f32 v143, v244, v143\n"
"v_mfma_f32_16x16x32_bf16 v[156:159], a[228:231], a[12:15], v[156:159]\n"
"v_mul_f32 v172, v244, v172\n"
"v_mul_f32 v173, v244, v173\n"
"v_mfma_f32_16x16x32_bf16 v[156:159], a[224:227], v[80:83], v[156:159]\n"
"s_waitcnt lgkmcnt(0)\n"
"s_barrier\n"
"v_mfma_f32_16x16x32_bf16 v[164:167], a[232:235], v[80:83], v[164:167]\n"
"buffer_store_dwordx4 v[60:63], v253, s[56:59], s76 offen sc1\n"
"v_mfma_f32_16x16x32_bf16 v[164:167], a[236:239], a[12:15], v[164:167]\n"
"ds_read_b64_tr_b16 a[228:229], v242 offset:0\n"
"v_mul_f32 v174, v244, v174\n"
"v_mfma_f32_16x16x32_bf16 v[204:207], a[236:239], v[104:107], v[204:207]\n"
"ds_read_b64_tr_b16 a[230:231], v242 offset:128\n"
"v_mul_f32 v175, v244, v175\n"
"v_mfma_f32_16x16x32_bf16 v[204:207], a[232:235], v[72:75], v[204:207]\n"
"ds_read_b64_tr_b16 a[224:225], v240 offset:0\n"
"v_cvt_pk_bf16_f32 v140, v140, v141\n"
"v_mfma_f32_16x16x32_bf16 v[44:47], a[232:235], v[64:67], v[44:47]\n"
"ds_read_b64_tr_b16 a[226:227], v240 offset:128\n"
"v_cvt_pk_bf16_f32 v141, v142, v143\n"
"v_mfma_f32_16x16x32_bf16 v[44:47], a[236:239], v[96:99], v[44:47]\n"
"v_cvt_pk_bf16_f32 v142, v172, v173\n"
"v_cvt_pk_bf16_f32 v143, v174, v175\n"
"v_mfma_f32_16x16x32_bf16 v[12:15], a[236:239], v[88:91], v[12:15]\n"
"ds_read_b64_tr_b16 a[236:237], v242 offset:32\n"
"v_add_u32 v243, 0x20700, v243\n"
"v_mfma_f32_16x16x32_bf16 v[12:15], a[232:235], v[56:59], v[12:15]\n"
"ds_read_b64_tr_b16 a[238:239], v242 offset:160\n"
"v_mul_f32 v196, v245, v196\n"
"v_mfma_f32_16x16x32_bf16 v[20:23], a[240:243], v[56:59], v[20:23]\n"
"buffer_load_dwordx4 a[76:79], v235, s[56:59], s34 offen offset:3072 sc1\n"
"v_mfma_f32_16x16x32_bf16 v[20:23], a[244:247], v[88:91], v[20:23]\n"
"ds_read_b64_tr_b16 a[232:233], v240 offset:32\n"
"v_bfe_u32 v235, v239, 8, 1\n"
"v_mfma_f32_16x16x32_bf16 v[52:55], a[244:247], v[96:99], v[52:55]\n"
"ds_read_b64_tr_b16 a[234:235], v240 offset:160\n"
"v_mul_f32 v197, v245, v197\n"
"v_mfma_f32_16x16x32_bf16 v[52:55], a[240:243], v[64:67], v[52:55]\n"
"v_mul_f32 v198, v245, v198\n"
"v_mul_f32 v199, v245, v199\n"
"v_mfma_f32_16x16x32_bf16 v[212:215], a[240:243], v[72:75], v[212:215]\n"
"ds_read_b64_tr_b16 a[240:241], v241 offset:0\n"
"v_lshl_add_u32 v235, v235, 13, v233\n"
"v_mfma_f32_16x16x32_bf16 v[212:215], a[244:247], v[104:107], v[212:215]\n"
"ds_read_b64_tr_b16 a[242:243], v241 offset:128\n"
"v_bfe_u32 v226, v224, 10, 1\n"
"v_mfma_f32_16x16x32_bf16 v[220:223], a[252:255], v[104:107], v[220:223]\n"
"ds_read_b64_tr_b16 a[244:245], v243 offset:0\n"
"v_mul_f32 v148, v245, v148\n"
"v_mfma_f32_16x16x32_bf16 v[220:223], a[248:251], v[72:75], v[220:223]\n"
"ds_read_b64_tr_b16 a[246:247], v243 offset:128\n"
"v_mul_f32 v149, v245, v149\n"
"v_mfma_f32_16x16x32_bf16 v[92:95], a[248:251], v[64:67], v[92:95]\n"
"v_mul_f32 v150, v245, v150\n"
"v_mul_f32 v151, v245, v151\n"
"v_mfma_f32_16x16x32_bf16 v[92:95], a[252:255], v[96:99], v[92:95]\n"
"v_cvt_pk_bf16_f32 v196, v196, v197\n"
"v_lshl_add_u32 v235, v226, 29, v235\n"
"v_mfma_f32_16x16x32_bf16 v[28:31], a[252:255], v[88:91], v[28:31]\n"
"ds_read_b64_tr_b16 a[252:253], v243 offset:32\n"
"v_cvt_pk_bf16_f32 v197, v198, v199\n"
"v_mfma_f32_16x16x32_bf16 v[28:31], a[248:251], v[56:59], v[28:31]\n"
"s_waitcnt lgkmcnt(7)\n"
"ds_read_b64_tr_b16 a[254:255], v243 offset:160\n"
"v_cvt_pk_bf16_f32 v198, v148, v149\n"
"v_mfma_f32_16x16x32_bf16 v[4:7], a[224:227], v[112:115], v[4:7]\n"
"buffer_store_dwordx4 v[108:111], v253, s[56:59], s78 offen sc1\n"
"v_mfma_f32_16x16x32_bf16 v[4:7], a[228:231], v[144:147], v[4:7]\n"
"ds_read_b64_tr_b16 a[248:249], v241 offset:32\n"
"v_cvt_pk_bf16_f32 v199, v150, v151\n"
"v_mfma_f32_16x16x32_bf16 v[36:39], a[228:231], v[152:155], v[36:39]\n"
"ds_read_b64_tr_b16 a[250:251], v241 offset:160\n"
"v_mul_f32 v76, v246, v76\n"
"v_mfma_f32_16x16x32_bf16 v[36:39], a[224:227], v[120:123], v[36:39]\n"
"v_mul_f32 v77, v246, v77\n"
"v_mul_f32 v78, v246, v78\n"
"v_mfma_f32_16x16x32_bf16 v[84:87], a[224:227], v[128:131], v[84:87]\n"
"v_mul_f32 v79, v246, v79\n"
"v_mul_f32 v100, v246, v100\n"
"v_mfma_f32_16x16x32_bf16 v[84:87], a[228:231], v[160:163], v[84:87]\n"
"v_mul_f32 v101, v246, v101\n"
"v_mul_f32 v102, v246, v102\n"
"v_mfma_f32_16x16x32_bf16 v[156:159], a[228:231], a[20:23], v[156:159]\n"
"ds_read_b64_tr_b16 a[228:229], v242 offset:512\n"
"v_mul_f32 v103, v246, v103\n"
"v_mfma_f32_16x16x32_bf16 v[156:159], a[224:227], v[136:139], v[156:159]\n"
"s_waitcnt lgkmcnt(5)\n"
"ds_read_b64_tr_b16 a[230:231], v242 offset:640\n"
"v_cvt_pk_bf16_f32 v76, v76, v77\n"
"v_mfma_f32_16x16x32_bf16 v[164:167], a[232:235], v[136:139], v[164:167]\n"
"buffer_load_dwordx4 a[80:83], v254, s[56:59], s35 offen offset:0 sc1\n"
"v_mfma_f32_16x16x32_bf16 v[164:167], a[236:239], a[20:23], v[164:167]\n"
"ds_read_b64_tr_b16 a[224:225], v240 offset:512\n"
"v_cvt_pk_bf16_f32 v77, v78, v79\n"
"v_mfma_f32_16x16x32_bf16 v[204:207], a[236:239], v[160:163], v[204:207]\n"
"ds_read_b64_tr_b16 a[226:227], v240 offset:640\n"
"v_cvt_pk_bf16_f32 v78, v100, v101\n"
"v_mfma_f32_16x16x32_bf16 v[204:207], a[232:235], v[128:131], v[204:207]\n"
"v_cvt_pk_bf16_f32 v79, v102, v103\n"
"v_bfe_u32 v254, v239, 16, 1\n"
"v_mfma_f32_16x16x32_bf16 v[44:47], a[232:235], v[120:123], v[44:47]\n"
"v_lshl_add_u32 v254, v254, 13, v233\n"
"v_bfe_u32 v226, v224, 12, 1\n"
"v_mfma_f32_16x16x32_bf16 v[44:47], a[236:239], v[152:155], v[44:47]\n"
"v_lshl_add_u32 v254, v226, 29, v254\n"
"v_add_u32 v132, s68, v248\n"
"v_mfma_f32_16x16x32_bf16 v[12:15], a[236:239], v[144:147], v[12:15]\n"
"buffer_store_dwordx4 v[140:143], v253, s[56:59], s73 offen sc1\n"
"v_mfma_f32_16x16x32_bf16 v[12:15], a[232:235], v[112:115], v[12:15]\n"
"ds_read_b64_tr_b16 a[236:237], v242 offset:544\n"
"v_add_u32 v116, s96, v248\n"
"v_mfma_f32_16x16x32_bf16 v[20:23], a[240:243], v[112:115], v[20:23]\n"
"ds_read_b64_tr_b16 a[232:233], v240 offset:544\n"
"v_bfe_u32 v226, v224, 14, 1\n"
"v_mfma_f32_16x16x32_bf16 v[20:23], a[244:247], v[144:147], v[20:23]\n"
"ds_read_b64_tr_b16 a[234:235], v240 offset:672\n"
"v_add_u32 v172, s69, v248\n"
"v_mfma_f32_16x16x32_bf16 v[52:55], a[244:247], v[152:155], v[52:55]\n"
"ds_read_b64_tr_b16 a[238:239], v242 offset:672\n"
"v_add_u32 v240, 0xfffe7ac0, v240\n"
"v_mfma_f32_16x16x32_bf16 v[52:55], a[240:243], v[120:123], v[52:55]\n"
"ds_write_b128 v132, v[124:127] offset:8192\n"
"v_add_u32 v242, 0xfffe7ac0, v242\n"
"v_mfma_f32_16x16x32_bf16 v[212:215], a[240:243], v[128:131], v[212:215]\n"
"ds_read_b64_tr_b16 a[240:241], v241 offset:512\n"
"v_mfma_f32_16x16x32_bf16 v[212:215], a[244:247], v[160:163], v[212:215]\n"
"s_waitcnt lgkmcnt(7)\n"
"ds_read_b64_tr_b16 a[242:243], v241 offset:640\n"
"v_mfma_f32_16x16x32_bf16 v[220:223], a[252:255], v[160:163], v[220:223]\n"
"buffer_load_dwordx4 a[84:87], v235, s[56:59], s35 offen offset:1024 sc1\n"
"v_mfma_f32_16x16x32_bf16 v[220:223], a[248:251], v[128:131], v[220:223]\n"
"ds_read_b64_tr_b16 a[244:245], v243 offset:512\n"
"v_bfe_u32 v235, v239, 24, 1\n"
"v_mfma_f32_16x16x32_bf16 v[92:95], a[248:251], v[120:123], v[92:95]\n"
"ds_read_b64_tr_b16 a[246:247], v243 offset:640\n"
"v_lshl_add_u32 v235, v235, 13, v233\n"
"v_mfma_f32_16x16x32_bf16 v[92:95], a[252:255], v[152:155], v[92:95]\n"
"ds_write_b128 v116, v[108:111] offset:8192\n"
"v_lshl_add_u32 v235, v226, 29, v235\n"
"v_mfma_f32_16x16x32_bf16 v[28:31], a[252:255], v[144:147], v[28:31]\n"
"ds_read_b64_tr_b16 a[252:253], v243 offset:544\n"
"v_mfma_f32_16x16x32_bf16 v[28:31], a[248:251], v[112:115], v[28:31]\n"
"s_waitcnt lgkmcnt(10)\n"
"buffer_store_dwordx4 v[196:199], v253, s[56:59], s75 offen sc1\n"
"v_mfma_f32_16x16x32_bf16 v[4:7], a[224:227], v[168:171], v[4:7]\n"
"ds_read_b64_tr_b16 a[254:255], v243 offset:672\n"
"v_add_u32 v243, 0xfffe7ac0, v243\n"
"v_mfma_f32_16x16x32_bf16 v[4:7], a[228:231], v[200:203], v[4:7]\n"
"ds_read_b64_tr_b16 a[248:249], v241 offset:544\n"
"v_mfma_f32_16x16x32_bf16 v[36:39], a[228:231], v[208:211], v[36:39]\n"
"ds_read_b64_tr_b16 a[250:251], v241 offset:672\n"
"v_add_u32 v241, 0xfffe7ac0, v241\n"
"v_mfma_f32_16x16x32_bf16 v[36:39], a[224:227], v[176:179], v[36:39]\n"
"ds_write_b128 v172, v[140:143] offset:8192\n"
"v_mfma_f32_16x16x32_bf16 v[84:87], a[224:227], v[184:187], v[84:87]\n"
"v_mfma_f32_16x16x32_bf16 v[84:87], a[228:231], v[216:219], v[84:87]\n"
"v_mul_f32 v4, v244, v4\n"
"v_mul_f32 v5, v244, v5\n"
"v_mfma_f32_16x16x32_bf16 v[156:159], a[228:231], a[28:31], v[156:159]\n"
"v_mul_f32 v6, v244, v6\n"
"v_mul_f32 v7, v244, v7\n"
"v_mfma_f32_16x16x32_bf16 v[156:159], a[224:227], v[192:195], v[156:159]\n"
"s_waitcnt lgkmcnt(1)\n"
"s_barrier\n"
"v_mfma_f32_16x16x32_bf16 v[164:167], a[232:235], v[192:195], v[164:167]\n"
"s_waitcnt vmcnt(11)\n"
"buffer_store_dwordx2 v[230:231], v236, s[60:63], 0 offen sc1\n"
"v_mfma_f32_16x16x32_bf16 v[164:167], a[236:239], a[28:31], v[164:167]\n"
"ds_read_b64_tr_b16 a[228:229], v242 offset:0\n"
"v_cvt_pk_bf16_f32 v4, v4, v5\n"
"v_mfma_f32_16x16x32_bf16 v[204:207], a[236:239], v[216:219], v[204:207]\n"
"ds_read_b64_tr_b16 a[224:225], v240 offset:0\n"
"v_cvt_pk_bf16_f32 v5, v6, v7\n"
"v_mfma_f32_16x16x32_bf16 v[204:207], a[232:235], v[184:187], v[204:207]\n"
"ds_read_b64_tr_b16 a[226:227], v240 offset:128\n"
"v_mul_f32 v36, v245, v36\n"
"v_mfma_f32_16x16x32_bf16 v[44:47], a[232:235], v[176:179], v[44:47]\n"
"ds_read_b64_tr_b16 a[230:231], v242 offset:128\n"
"v_mul_f32 v37, v245, v37\n"
"v_mfma_f32_16x16x32_bf16 v[44:47], a[236:239], v[208:211], v[44:47]\n"
"v_mul_f32 v38, v245, v38\n"
"v_mul_f32 v39, v245, v39\n"
"v_mfma_f32_16x16x32_bf16 v[12:15], a[236:239], v[200:203], v[12:15]\n"
"buffer_store_dwordx4 v[76:79], v253, s[56:59], s77 offen sc1\n"
"v_mfma_f32_16x16x32_bf16 v[12:15], a[232:235], v[168:171], v[12:15]\n"
"ds_read_b64_tr_b16 a[236:237], v242 offset:32\n"
"v_cvt_pk_bf16_f32 v36, v36, v37\n"
"v_mfma_f32_16x16x32_bf16 v[20:23], a[240:243], v[168:171], v[20:23]\n"
"ds_read_b64_tr_b16 a[232:233], v240 offset:32\n"
"v_add_u32 v253, 0x100000, v253\n"
"v_mfma_f32_16x16x32_bf16 v[20:23], a[244:247], v[200:203], v[20:23]\n"
"ds_read_b64_tr_b16 a[234:235], v240 offset:160\n"
"v_mul_f32 v44, v245, v44\n"
"v_mfma_f32_16x16x32_bf16 v[52:55], a[244:247], v[208:211], v[52:55]\n"
"ds_read_b64_tr_b16 a[238:239], v242 offset:160\n"
"v_mul_f32 v12, v244, v12\n"
"v_mfma_f32_16x16x32_bf16 v[52:55], a[240:243], v[176:179], v[52:55]\n"
"v_mul_f32 v13, v244, v13\n"
"v_mul_f32 v14, v244, v14\n"
"v_mfma_f32_16x16x32_bf16 v[212:215], a[240:243], v[184:187], v[212:215]\n"
"buffer_load_dwordx4 a[88:91], v254, s[56:59], s35 offen offset:2048 sc1\n"
"v_mfma_f32_16x16x32_bf16 v[212:215], a[244:247], v[216:219], v[212:215]\n"
"ds_read_b64_tr_b16 a[240:241], v241 offset:0\n"
"v_mul_f32 v15, v244, v15\n"
"v_mfma_f32_16x16x32_bf16 v[220:223], a[252:255], v[216:219], v[220:223]\n"
"v_cvt_pk_bf16_f32 v6, v12, v13\n"
"v_cvt_pk_bf16_f32 v7, v14, v15\n"
"v_mfma_f32_16x16x32_bf16 v[220:223], a[248:251], v[184:187], v[220:223]\n"
"ds_read_b64_tr_b16 a[242:243], v241 offset:128\n"
"v_mul_f32 v45, v245, v45\n"
"v_mfma_f32_16x16x32_bf16 v[92:95], a[248:251], v[176:179], v[92:95]\n"
"ds_read_b64_tr_b16 a[244:245], v243 offset:0\n"
"v_mul_f32 v46, v245, v46\n"
"v_mfma_f32_16x16x32_bf16 v[92:95], a[252:255], v[208:211], v[92:95]\n"
"ds_read_b64_tr_b16 a[246:247], v243 offset:128\n"
"v_mul_f32 v47, v245, v47\n"
"v_mfma_f32_16x16x32_bf16 v[28:31], a[252:255], v[200:203], v[28:31]\n"
"ds_read_b64_tr_b16 a[252:253], v243 offset:32\n"
"v_cvt_pk_bf16_f32 v37, v38, v39\n"
"v_mfma_f32_16x16x32_bf16 v[28:31], a[248:251], v[168:171], v[28:31]\n"
"s_waitcnt lgkmcnt(8)\n"
"ds_read_b64_tr_b16 a[254:255], v243 offset:160\n"
"v_cvt_pk_bf16_f32 v38, v44, v45\n"
"v_mfma_f32_16x16x32_bf16 v[124:127], a[224:227], v[0:3], 0\n"
"ds_read_b64_tr_b16 a[248:249], v241 offset:32\n"
"v_cvt_pk_bf16_f32 v39, v46, v47\n"
"v_mfma_f32_16x16x32_bf16 v[124:127], a[228:231], v[32:35], v[124:127]\n"
"ds_read_b64_tr_b16 a[250:251], v241 offset:160\n"
"v_mul_f32 v84, v246, v84\n"
"v_mfma_f32_16x16x32_bf16 v[180:183], a[228:231], v[40:43], 0\n"
"v_mul_f32 v85, v246, v85\n"
"v_mul_f32 v86, v246, v86\n"
"v_mfma_f32_16x16x32_bf16 v[180:183], a[224:227], v[8:11], v[180:183]\n"
"v_mul_f32 v87, v246, v87\n"
"v_mul_f32 v204, v246, v204\n"
"v_mfma_f32_16x16x32_bf16 v[60:63], a[224:227], v[16:19], 0\n"
"v_mul_f32 v205, v246, v205\n"
"v_mul_f32 v206, v246, v206\n"
"v_mfma_f32_16x16x32_bf16 v[60:63], a[228:231], v[48:51], v[60:63]\n"
"v_mul_f32 v207, v246, v207\n"
"v_cvt_pk_bf16_f32 v84, v84, v85\n"
"v_mfma_f32_16x16x32_bf16 v[108:111], a[228:231], a[4:7], 0\n"
"ds_read_b64_tr_b16 a[228:229], v242 offset:512\n"
"v_cvt_pk_bf16_f32 v85, v86, v87\n"
"v_mfma_f32_16x16x32_bf16 v[108:111], a[224:227], v[24:27], v[108:111]\n"
"s_waitcnt lgkmcnt(7)\n"
"ds_read_b64_tr_b16 a[224:225], v240 offset:512\n"
"v_cvt_pk_bf16_f32 v86, v204, v205\n"
"v_mfma_f32_16x16x32_bf16 v[116:119], a[232:235], v[24:27], 0\n"
"ds_read_b64_tr_b16 a[226:227], v240 offset:640\n"
"v_cvt_pk_bf16_f32 v87, v206, v207\n"
"v_mfma_f32_16x16x32_bf16 v[116:119], a[236:239], a[4:7], v[116:119]\n"
"ds_read_b64_tr_b16 a[230:231], v242 offset:640\n"
"v_mul_f32 v156, v247, v156\n"
"v_mfma_f32_16x16x32_bf16 v[68:71], a[236:239], v[48:51], 0\n"
"v_mul_f32 v157, v247, v157\n"
"v_mul_f32 v158, v247, v158\n"
"v_mfma_f32_16x16x32_bf16 v[68:71], a[232:235], v[16:19], v[68:71]\n"
"v_mul_f32 v159, v247, v159\n"
"v_mul_f32 v164, v247, v164\n"
"v_mfma_f32_16x16x32_bf16 v[188:191], a[232:235], v[8:11], 0\n"
"buffer_load_dwordx4 a[92:95], v235, s[56:59], s35 offen offset:3072 sc1\n"
"v_mfma_f32_16x16x32_bf16 v[188:191], a[236:239], v[40:43], v[188:191]\n"
"v_mul_f32 v165, v247, v165\n"
"v_mul_f32 v166, v247, v166\n"
"v_mfma_f32_16x16x32_bf16 v[132:135], a[236:239], v[32:35], 0\n"
"ds_read_b64_tr_b16 a[236:237], v242 offset:544\n"
"v_mul_f32 v167, v247, v167\n"
"v_mfma_f32_16x16x32_bf16 v[132:135], a[232:235], v[0:3], v[132:135]\n"
"buffer_store_dwordx4 v[4:7], v253, s[56:59], s72 offen sc1\n"
"v_mfma_f32_16x16x32_bf16 v[140:143], a[240:243], v[0:3], 0\n"
"s_waitcnt lgkmcnt(5)\n"
"ds_read_b64_tr_b16 a[232:233], v240 offset:544\n"
"v_cvt_pk_bf16_f32 v156, v156, v157\n"
"v_mfma_f32_16x16x32_bf16 v[140:143], a[244:247], v[32:35], v[140:143]\n"
"ds_read_b64_tr_b16 a[234:235], v240 offset:672\n"
"v_cvt_pk_bf16_f32 v157, v158, v159\n"
"v_mfma_f32_16x16x32_bf16 v[196:199], a[244:247], v[40:43], 0\n"
"ds_read_b64_tr_b16 a[238:239], v242 offset:672\n"
"v_cvt_pk_bf16_f32 v158, v164, v165\n"
"v_mfma_f32_16x16x32_bf16 v[196:199], a[240:243], v[8:11], v[196:199]\n"
"v_cvt_pk_bf16_f32 v159, v166, v167\n"
"v_mul_f32 v20, v244, v20\n"
"v_mfma_f32_16x16x32_bf16 v[76:79], a[240:243], v[16:19], 0\n"
"ds_read_b64_tr_b16 a[240:241], v241 offset:512\n"
"v_mul_f32 v21, v244, v21\n"
"v_mfma_f32_16x16x32_bf16 v[76:79], a[244:247], v[48:51], v[76:79]\n"
"ds_read_b64_tr_b16 a[242:243], v241 offset:640\n"
"v_mul_f32 v22, v244, v22\n"
"v_mfma_f32_16x16x32_bf16 v[100:103], a[252:255], v[48:51], 0\n"
"ds_read_b64_tr_b16 a[244:245], v243 offset:512\n"
"v_mul_f32 v23, v244, v23\n"
"v_mfma_f32_16x16x32_bf16 v[100:103], a[248:251], v[16:19], v[100:103]\n"
"buffer_store_dwordx4 v[36:39], v253, s[56:59], s74 offen sc1\n"
"v_mfma_f32_16x16x32_bf16 v[148:151], a[248:251], v[8:11], 0\n"
"ds_read_b64_tr_b16 a[246:247], v243 offset:640\n"
"v_mul_f32 v28, v244, v28\n"
"v_mfma_f32_16x16x32_bf16 v[148:151], a[252:255], v[40:43], v[148:151]\n"
"v_mul_f32 v29, v244, v29\n"
"v_mul_f32 v30, v244, v30\n"
"v_mfma_f32_16x16x32_bf16 v[172:175], a[252:255], v[32:35], 0\n"
"ds_read_b64_tr_b16 a[252:253], v243 offset:544\n"
"v_mul_f32 v31, v244, v31\n"
"v_mfma_f32_16x16x32_bf16 v[172:175], a[248:251], v[0:3], v[172:175]\n"
"s_waitcnt lgkmcnt(8)\n"
"ds_read_b64_tr_b16 a[254:255], v243 offset:672\n"
"v_cvt_pk_bf16_f32 v20, v20, v21\n"
"v_mfma_f32_16x16x32_bf16 v[124:127], a[224:227], v[56:59], v[124:127]\n"
"ds_read_b64_tr_b16 a[248:249], v241 offset:544\n"
"v_cvt_pk_bf16_f32 v21, v22, v23\n"
"v_mfma_f32_16x16x32_bf16 v[124:127], a[228:231], v[88:91], v[124:127]\n"
"ds_read_b64_tr_b16 a[250:251], v241 offset:672\n"
"v_cvt_pk_bf16_f32 v22, v28, v29\n"
"v_mfma_f32_16x16x32_bf16 v[180:183], a[228:231], v[96:99], v[180:183]\n"
"v_cvt_pk_bf16_f32 v23, v30, v31\n"
"v_mul_f32 v52, v245, v52\n"
"v_mfma_f32_16x16x32_bf16 v[180:183], a[224:227], v[64:67], v[180:183]\n"
"v_mul_f32 v53, v245, v53\n"
"v_mul_f32 v54, v245, v54\n"
"v_mfma_f32_16x16x32_bf16 v[60:63], a[224:227], v[72:75], v[60:63]\n"
"buffer_store_dwordx4 v[84:87], v253, s[56:59], s76 offen sc1\n"
"v_mfma_f32_16x16x32_bf16 v[60:63], a[228:231], v[104:107], v[60:63]\n"
"v_mul_f32 v55, v245, v55\n"
"v_mul_f32 v92, v245, v92\n"
"v_mfma_f32_16x16x32_bf16 v[108:111], a[228:231], a[12:15], v[108:111]\n"
"s_waitcnt lgkmcnt(0)\n"
"s_barrier\n"
"v_mfma_f32_16x16x32_bf16 v[108:111], a[224:227], v[80:83], v[108:111]\n"
"buffer_store_dwordx4 v[156:159], v253, s[56:59], s78 offen sc1\n"
"v_mfma_f32_16x16x32_bf16 v[116:119], a[232:235], v[80:83], v[116:119]\n"
"ds_read_b64_tr_b16 a[228:229], v242 offset:49824\n"
"v_mul_f32 v93, v245, v93\n"
"v_mfma_f32_16x16x32_bf16 v[116:119], a[236:239], a[12:15], v[116:119]\n"
"ds_read_b64_tr_b16 a[224:225], v240 offset:49824\n"
"v_mul_f32 v94, v245, v94\n"
"v_mfma_f32_16x16x32_bf16 v[68:71], a[236:239], v[104:107], v[68:71]\n"
"ds_read_b64_tr_b16 a[226:227], v240 offset:49952\n"
"v_mul_f32 v95, v245, v95\n"
"v_mfma_f32_16x16x32_bf16 v[68:71], a[232:235], v[72:75], v[68:71]\n"
"ds_read_b64_tr_b16 a[230:231], v242 offset:49952\n"
"v_cvt_pk_bf16_f32 v52, v52, v53\n"
"v_mfma_f32_16x16x32_bf16 v[188:191], a[232:235], v[64:67], v[188:191]\n"
"v_cvt_pk_bf16_f32 v53, v54, v55\n"
"v_mul_f32 v212, v246, v212\n"
"v_mfma_f32_16x16x32_bf16 v[188:191], a[236:239], v[96:99], v[188:191]\n"
"v_cvt_pk_bf16_f32 v54, v92, v93\n"
"v_cvt_pk_bf16_f32 v55, v94, v95\n"
"v_mfma_f32_16x16x32_bf16 v[132:135], a[236:239], v[88:91], v[132:135]\n"
"ds_read_b64_tr_b16 a[236:237], v242 offset:49856\n"
"v_mul_f32 v213, v246, v213\n"
"v_mfma_f32_16x16x32_bf16 v[132:135], a[232:235], v[56:59], v[132:135]\n"
"ds_read_b64_tr_b16 a[232:233], v240 offset:49856\n"
"v_mul_f32 v214, v246, v214\n"
"v_mfma_f32_16x16x32_bf16 v[140:143], a[240:243], v[56:59], v[140:143]\n"
"ds_read_b64_tr_b16 a[234:235], v240 offset:49984\n"
"v_mul_f32 v215, v246, v215\n"
"v_mfma_f32_16x16x32_bf16 v[140:143], a[244:247], v[88:91], v[140:143]\n"
"ds_read_b64_tr_b16 a[238:239], v242 offset:49984\n"
"v_mul_f32 v220, v246, v220\n"
"v_mfma_f32_16x16x32_bf16 v[196:199], a[244:247], v[96:99], v[196:199]\n"
"v_mul_f32 v221, v246, v221\n"
"v_mul_f32 v222, v246, v222\n"
"v_mfma_f32_16x16x32_bf16 v[196:199], a[240:243], v[64:67], v[196:199]\n"
"v_mul_f32 v223, v246, v223\n"
"v_cvt_pk_bf16_f32 v212, v212, v213\n"
"v_mfma_f32_16x16x32_bf16 v[76:79], a[240:243], v[72:75], v[76:79]\n"
"ds_read_b64_tr_b16 a[240:241], v240 offset:50336\n"
"v_cvt_pk_bf16_f32 v213, v214, v215\n"
"v_mfma_f32_16x16x32_bf16 v[76:79], a[244:247], v[104:107], v[76:79]\n"
"v_cvt_pk_bf16_f32 v214, v220, v221\n"
"v_cvt_pk_bf16_f32 v215, v222, v223\n"
"v_mfma_f32_16x16x32_bf16 v[100:103], a[252:255], v[104:107], v[100:103]\n"
"buffer_store_dwordx4 v[20:23], v253, s[56:59], s73 offen sc1\n"
"v_mfma_f32_16x16x32_bf16 v[100:103], a[248:251], v[72:75], v[100:103]\n"
"ds_read_b64_tr_b16 a[242:243], v240 offset:50464\n"
"v_add_u32 v12, s68, v248\n"
"v_mfma_f32_16x16x32_bf16 v[148:151], a[248:251], v[64:67], v[148:151]\n"
"ds_read_b64_tr_b16 a[244:245], v242 offset:50336\n"
"v_add_u32 v164, s96, v248\n"
"v_mfma_f32_16x16x32_bf16 v[148:151], a[252:255], v[96:99], v[148:151]\n"
"ds_read_b64_tr_b16 a[246:247], v242 offset:50464\n"
"v_add_u32 v28, s69, v248\n"
"v_mfma_f32_16x16x32_bf16 v[172:175], a[252:255], v[88:91], v[172:175]\n"
"ds_read_b64_tr_b16 a[192:193], v241 offset:49824\n"
"v_mfma_f32_16x16x32_bf16 v[172:175], a[248:251], v[56:59], v[172:175]\n"
"s_waitcnt lgkmcnt(7)\n"
"buffer_store_dwordx4 v[52:55], v253, s[56:59], s75 offen sc1\n"
"v_mfma_f32_16x16x32_bf16 v[124:127], a[224:227], v[112:115], v[124:127]\n"
"ds_read_b64_tr_b16 a[194:195], v241 offset:49952\n"
"v_mfma_f32_16x16x32_bf16 v[124:127], a[228:231], v[144:147], v[124:127]\n"
"ds_read_b64_tr_b16 a[252:253], v242 offset:50368\n"
"v_mfma_f32_16x16x32_bf16 v[180:183], a[228:231], v[152:155], v[180:183]\n"
"buffer_load_dwordx2 v[228:229], v232, s[60:63], 0 offen sc1\n"
"v_mfma_f32_16x16x32_bf16 v[180:183], a[224:227], v[120:123], v[180:183]\n"
"ds_read_b64_tr_b16 a[248:249], v240 offset:50368\n"
"v_mfma_f32_16x16x32_bf16 v[60:63], a[224:227], v[128:131], v[60:63]\n"
"ds_read_b64_tr_b16 a[250:251], v240 offset:50496\n"
"v_mfma_f32_16x16x32_bf16 v[60:63], a[228:231], v[160:163], v[60:63]\n"
"ds_read_b64_tr_b16 a[254:255], v242 offset:50496\n"
"v_mfma_f32_16x16x32_bf16 v[108:111], a[228:231], a[20:23], v[108:111]\n"
"buffer_store_dwordx4 v[212:215], v253, s[56:59], s77 offen sc1\n"
"v_mfma_f32_16x16x32_bf16 v[108:111], a[224:227], v[136:139], v[108:111]\n"
"s_waitcnt lgkmcnt(8)\n"
"ds_read_b64_tr_b16 a[196:197], v243 offset:49824\n"
"v_add_u32 v253, 0x100000, v253\n"
"v_mfma_f32_16x16x32_bf16 v[116:119], a[232:235], v[136:139], v[116:119]\n"
"ds_read_b64_tr_b16 a[198:199], v243 offset:49952\n"
"v_xor_b32 v253, 0x2000, v253\n"
"v_mfma_f32_16x16x32_bf16 v[116:119], a[236:239], a[20:23], v[116:119]\n"
"ds_read_b64_tr_b16 a[200:201], v241 offset:49856\n"
"v_mfma_f32_16x16x32_bf16 v[68:71], a[236:239], v[160:163], v[68:71]\n"
"ds_read_b64_tr_b16 a[202:203], v241 offset:49984\n"
"v_mfma_f32_16x16x32_bf16 v[68:71], a[232:235], v[128:131], v[68:71]\n"
"ds_read_b64_tr_b16 a[204:205], v243 offset:49856\n"
"v_mfma_f32_16x16x32_bf16 v[188:191], a[232:235], v[120:123], v[188:191]\n"
"ds_read_b64_tr_b16 a[206:207], v243 offset:49984\n"
"v_mfma_f32_16x16x32_bf16 v[188:191], a[236:239], v[152:155], v[188:191]\n"
"ds_read_b64_tr_b16 a[208:209], v241 offset:50336\n"
"v_mfma_f32_16x16x32_bf16 v[132:135], a[236:239], v[144:147], v[132:135]\n"
"ds_read_b64_tr_b16 a[210:211], v241 offset:50464\n"
"v_mfma_f32_16x16x32_bf16 v[132:135], a[232:235], v[112:115], v[132:135]\n"
"ds_read_b64_tr_b16 a[212:213], v243 offset:50336\n"
"v_mfma_f32_16x16x32_bf16 v[124:127], a[240:243], v[168:171], v[124:127]\n"
"s_waitcnt lgkmcnt(10)\n"
"ds_read_b64_tr_b16 a[214:215], v243 offset:50464\n"
"v_mfma_f32_16x16x32_bf16 v[124:127], a[244:247], v[200:203], v[124:127]\n"
"ds_read_b64_tr_b16 a[216:217], v241 offset:50368\n"
"v_mfma_f32_16x16x32_bf16 v[180:183], a[244:247], v[208:211], v[180:183]\n"
"ds_read_b64_tr_b16 a[218:219], v241 offset:50496\n"
"v_mfma_f32_16x16x32_bf16 v[180:183], a[240:243], v[176:179], v[180:183]\n"
"ds_read_b64_tr_b16 a[220:221], v243 offset:50368\n"
"v_mfma_f32_16x16x32_bf16 v[60:63], a[240:243], v[184:187], v[60:63]\n"
"ds_read_b64_tr_b16 a[222:223], v243 offset:50496\n"
"v_mfma_f32_16x16x32_bf16 v[60:63], a[244:247], v[216:219], v[60:63]\n"
"v_mul_f32 v124, v244, v124\n"
"v_mul_f32 v125, v244, v125\n"
"v_mfma_f32_16x16x32_bf16 v[108:111], a[244:247], a[28:31], v[108:111]\n"
"v_mul_f32 v126, v244, v126\n"
"v_mul_f32 v127, v244, v127\n"
"v_mfma_f32_16x16x32_bf16 v[108:111], a[240:243], v[192:195], v[108:111]\n"
"v_cvt_pk_bf16_f32 v124, v124, v125\n"
"v_mul_f32 v180, v245, v180\n"
"v_mfma_f32_16x16x32_bf16 v[116:119], a[248:251], v[192:195], v[116:119]\n"
"s_waitcnt lgkmcnt(8)\n"
"v_cvt_pk_bf16_f32 v125, v126, v127\n"
"v_mul_f32 v181, v245, v181\n"
"v_mfma_f32_16x16x32_bf16 v[116:119], a[252:255], a[28:31], v[116:119]\n"
"v_mul_f32 v182, v245, v182\n"
"v_mul_f32 v183, v245, v183\n"
"v_mfma_f32_16x16x32_bf16 v[68:71], a[252:255], v[216:219], v[68:71]\n"
"v_cvt_pk_bf16_f32 v180, v180, v181\n"
"v_mul_f32 v60, v246, v60\n"
"v_mfma_f32_16x16x32_bf16 v[68:71], a[248:251], v[184:187], v[68:71]\n"
"v_cvt_pk_bf16_f32 v181, v182, v183\n"
"v_mul_f32 v61, v246, v61\n"
"v_mfma_f32_16x16x32_bf16 v[188:191], a[248:251], v[176:179], v[188:191]\n"
"v_mul_f32 v62, v246, v62\n"
"v_mul_f32 v63, v246, v63\n"
"v_mfma_f32_16x16x32_bf16 v[188:191], a[252:255], v[208:211], v[188:191]\n"
"v_cvt_pk_bf16_f32 v60, v60, v61\n"
"v_mul_f32 v108, v247, v108\n"
"v_mfma_f32_16x16x32_bf16 v[132:135], a[252:255], v[200:203], v[132:135]\n"
"v_mul_f32 v68, v246, v68\n"
"v_mul_f32 v69, v246, v69\n"
"v_mfma_f32_16x16x32_bf16 v[132:135], a[248:251], v[168:171], v[132:135]\n"
"v_mul_f32 v70, v246, v70\n"
"v_mul_f32 v71, v246, v71\n"
"v_mfma_f32_16x16x32_bf16 v[140:143], a[192:195], v[112:115], v[140:143]\n"
"v_mul_f32 v188, v245, v188\n"
"v_mul_f32 v189, v245, v189\n"
"v_mfma_f32_16x16x32_bf16 v[140:143], a[196:199], v[144:147], v[140:143]\n"
"s_waitcnt lgkmcnt(0)\n"
"v_mul_f32 v190, v245, v190\n"
"v_mul_f32 v191, v245, v191\n"
"v_mfma_f32_16x16x32_bf16 v[140:143], a[208:211], v[168:171], v[140:143]\n"
"v_mul_f32 v132, v244, v132\n"
"v_mul_f32 v133, v244, v133\n"
"v_mfma_f32_16x16x32_bf16 v[140:143], a[212:215], v[200:203], v[140:143]\n"
"v_mul_f32 v134, v244, v134\n"
"v_mul_f32 v135, v244, v135\n"
"v_mfma_f32_16x16x32_bf16 v[172:175], a[200:203], v[112:115], v[172:175]\n"
"v_cvt_pk_bf16_f32 v126, v132, v133\n"
"v_cvt_pk_bf16_f32 v127, v134, v135\n"
"v_mfma_f32_16x16x32_bf16 v[172:175], a[204:207], v[144:147], v[172:175]\n"
"v_cvt_pk_bf16_f32 v182, v188, v189\n"
"v_cvt_pk_bf16_f32 v183, v190, v191\n"
"v_mfma_f32_16x16x32_bf16 v[172:175], a[216:219], v[168:171], v[172:175]\n"
"v_cvt_pk_bf16_f32 v61, v62, v63\n"
"v_mul_f32 v109, v247, v109\n"
"v_mfma_f32_16x16x32_bf16 v[172:175], a[220:223], v[200:203], v[172:175]\n"
"v_cvt_pk_bf16_f32 v62, v68, v69\n"
"v_cvt_pk_bf16_f32 v63, v70, v71\n"
"v_mfma_f32_16x16x32_bf16 v[196:199], a[192:195], v[120:123], v[196:199]\n"
"v_mul_f32 v110, v247, v110\n"
"v_mul_f32 v111, v247, v111\n"
"v_mfma_f32_16x16x32_bf16 v[196:199], a[196:199], v[152:155], v[196:199]\n"
"v_mul_f32 v116, v247, v116\n"
"v_mul_f32 v117, v247, v117\n"
"v_mfma_f32_16x16x32_bf16 v[196:199], a[208:211], v[176:179], v[196:199]\n"
"v_mul_f32 v118, v247, v118\n"
"v_mul_f32 v119, v247, v119\n"
"v_mfma_f32_16x16x32_bf16 v[196:199], a[212:215], v[208:211], v[196:199]\n"
"buffer_store_dwordx4 v[124:127], v253, s[56:59], s72 offen sc1\n"
"v_mfma_f32_16x16x32_bf16 v[148:151], a[200:203], v[120:123], v[148:151]\n"
"v_cvt_pk_bf16_f32 v108, v108, v109\n"
"v_mul_f32 v140, v244, v140\n"
"v_mfma_f32_16x16x32_bf16 v[148:151], a[204:207], v[152:155], v[148:151]\n"
"v_cvt_pk_bf16_f32 v109, v110, v111\n"
"v_mul_f32 v141, v244, v141\n"
"v_cvt_pk_bf16_f32 v110, v116, v117\n"
"v_cvt_pk_bf16_f32 v111, v118, v119\n"
"v_mfma_f32_16x16x32_bf16 v[148:151], a[216:219], v[176:179], v[148:151]\n"
"buffer_store_dwordx4 v[180:183], v253, s[56:59], s74 offen sc1\n"
"v_mfma_f32_16x16x32_bf16 v[148:151], a[220:223], v[208:211], v[148:151]\n"
"v_mul_f32 v142, v244, v142\n"
"v_mul_f32 v143, v244, v143\n"
"v_mfma_f32_16x16x32_bf16 v[76:79], a[192:195], v[128:131], v[76:79]\n"
"v_mul_f32 v172, v244, v172\n"
"v_mul_f32 v173, v244, v173\n"
"v_mul_f32 v174, v244, v174\n"
"v_mul_f32 v175, v244, v175\n"
"v_cvt_pk_bf16_f32 v140, v140, v141\n"
"v_cvt_pk_bf16_f32 v141, v142, v143\n"
"v_cvt_pk_bf16_f32 v142, v172, v173\n"
"v_cvt_pk_bf16_f32 v143, v174, v175\n"
"v_mfma_f32_16x16x32_bf16 v[76:79], a[196:199], v[160:163], v[76:79]\n"
"buffer_store_dwordx4 v[60:63], v253, s[56:59], s76 offen sc1\n"
"v_mfma_f32_16x16x32_bf16 v[76:79], a[208:211], v[184:187], v[76:79]\n"
"v_mul_f32 v196, v245, v196\n"
"v_mul_f32 v197, v245, v197\n"
"v_mul_f32 v198, v245, v198\n"
"v_mul_f32 v199, v245, v199\n"
"v_mfma_f32_16x16x32_bf16 v[76:79], a[212:215], v[216:219], v[76:79]\n"
"v_mul_f32 v148, v245, v148\n"
"v_mul_f32 v149, v245, v149\n"
"v_mul_f32 v150, v245, v150\n"
"v_mul_f32 v151, v245, v151\n"
"v_cvt_pk_bf16_f32 v196, v196, v197\n"
"v_cvt_pk_bf16_f32 v197, v198, v199\n"
"v_cvt_pk_bf16_f32 v198, v148, v149\n"
"v_cvt_pk_bf16_f32 v199, v150, v151\n"
"v_mfma_f32_16x16x32_bf16 v[100:103], a[200:203], v[128:131], v[100:103]\n"
"buffer_store_dwordx4 v[108:111], v253, s[56:59], s78 offen sc1\n"
"v_mfma_f32_16x16x32_bf16 v[100:103], a[204:207], v[160:163], v[100:103]\n"
"v_mul_f32 v76, v246, v76\n"
"v_mul_f32 v77, v246, v77\n"
"v_mul_f32 v78, v246, v78\n"
"v_mul_f32 v79, v246, v79\n"
"v_mfma_f32_16x16x32_bf16 v[100:103], a[216:219], v[184:187], v[100:103]\n"
"v_cvt_pk_bf16_f32 v76, v76, v77\n"
"v_add_u32 v132, s68, v248\n"
"v_cvt_pk_bf16_f32 v77, v78, v79\n"
"v_add_u32 v116, s96, v248\n"
"v_add_u32 v172, s69, v248\n"
"v_mfma_f32_16x16x32_bf16 v[100:103], a[220:223], v[216:219], v[100:103]\n"
"buffer_store_dwordx4 v[140:143], v253, s[56:59], s73 offen sc1\n"
"ds_write_b128 v12, v[4:7] offset:10240\n"
"ds_write_b128 v164, v[156:159] offset:10240\n"
"buffer_store_dwordx4 v[196:199], v253, s[56:59], s75 offen sc1\n"
"ds_write_b128 v28, v[20:23] offset:10240\n"
"ds_write_b128 v132, v[124:127] offset:12288\n"
"ds_write_b128 v116, v[108:111] offset:12288\n"
"ds_write_b128 v172, v[140:143] offset:12288\n"
"v_mul_f32 v100, v246, v100\n"
"v_mul_f32 v101, v246, v101\n"
"v_mul_f32 v102, v246, v102\n"
"v_mul_f32 v103, v246, v103\n"
"v_cvt_pk_bf16_f32 v78, v100, v101\n"
"v_cvt_pk_bf16_f32 v79, v102, v103\n"
"buffer_store_dwordx4 v[76:79], v253, s[56:59], s77 offen sc1\n"
"s_add_u32 s17, s90, 4\n"
"s_waitcnt vmcnt(8)\n"
"v_and_b32 v227, -9, v228\n"
"v_subrev_u32 v227, s17, v227\n"
"v_cmp_ne_u32 vcc, s91, v229\n"
"v_cmp_lt_u32 s[88:89], 3, v227\n"
"s_nop 4\n"
"s_or_b64 vcc, vcc, s[88:89]\n"
"s_cbranch_scc1 SPIN3_%=\n"
"BACK3_%=:\n"
"v_add_u32 v230, 3, v230\n"
"s_waitcnt vmcnt(0)\n"
"buffer_store_dwordx2 v[230:231], v236, s[60:63], 0 offen sc1\n"
"s_nop 1\n"
"v_mov_b32 v230, s90\n"
"buffer_store_dwordx2 v[230:231], v237, s[60:63], 0 offen sc1\n"
"v_add_u32 v233, 0x100000, v233\n"
"v_not_b32 v238, v238\n"
"v_not_b32 v239, v239\n"
"v_bfe_u32 v254, v238, 0, 1\n"
"v_lshl_add_u32 v254, v254, 13, v233\n"
"v_bfe_u32 v226, v224, 0, 1\n"
"v_lshl_add_u32 v254, v226, 29, v254\n"
"buffer_load_dwordx4 a[96:99], v254, s[56:59], s34 offen offset:0 sc1\n"
"v_bfe_u32 v235, v238, 8, 1\n"
"v_lshl_add_u32 v235, v235, 13, v233\n"
"v_bfe_u32 v226, v224, 2, 1\n"
"v_lshl_add_u32 v235, v226, 29, v235\n"
"buffer_load_dwordx4 a[100:103], v235, s[56:59], s34 offen offset:1024 sc1\n"
"v_bfe_u32 v254, v238, 16, 1\n"
"v_lshl_add_u32 v254, v254, 13, v233\n"
"v_bfe_u32 v226, v224, 4, 1\n"
"v_lshl_add_u32 v254, v226, 29, v254\n"
"buffer_load_dwordx4 a[104:107], v254, s[56:59], s34 offen offset:2048 sc1\n"
"v_bfe_u32 v235, v238, 24, 1\n"
"v_lshl_add_u32 v235, v235, 13, v233\n"
"v_bfe_u32 v226, v224, 6, 1\n"
"v_lshl_add_u32 v235, v226, 29, v235\n"
"buffer_load_dwordx4 a[108:111], v235, s[56:59], s34 offen offset:3072 sc1\n"
"v_bfe_u32 v254, v239, 0, 1\n"
"v_lshl_add_u32 v254, v254, 13, v233\n"
"v_bfe_u32 v226, v224, 8, 1\n"
"v_lshl_add_u32 v254, v226, 29, v254\n"
"buffer_load_dwordx4 a[112:115], v254, s[56:59], s35 offen offset:0 sc1\n"
"v_bfe_u32 v235, v239, 8, 1\n"
"v_lshl_add_u32 v235, v235, 13, v233\n"
"v_bfe_u32 v226, v224, 10, 1\n"
"v_lshl_add_u32 v235, v226, 29, v235\n"
"buffer_load_dwordx4 a[116:119], v235, s[56:59], s35 offen offset:1024 sc1\n"
"v_bfe_u32 v254, v239, 16, 1\n"
"v_lshl_add_u32 v254, v254, 13, v233\n"
"v_bfe_u32 v226, v224, 12, 1\n"
"v_lshl_add_u32 v254, v226, 29, v254\n"
"buffer_load_dwordx4 a[120:123], v254, s[56:59], s35 offen offset:2048 sc1\n"
"v_bfe_u32 v235, v239, 24, 1\n"
"v_lshl_add_u32 v235, v235, 13, v233\n"
"v_bfe_u32 v226, v224, 14, 1\n"
"v_lshl_add_u32 v235, v226, 29, v235\n"
"buffer_load_dwordx4 a[124:127], v235, s[56:59], s35 offen offset:3072 sc1\n"
"v_add_u32 v233, 0x100000, v233\n"
"v_bfe_u32 v254, v238, 0, 1\n"
"v_lshl_add_u32 v254, v254, 13, v233\n"
"v_bfe_u32 v226, v224, 0, 1\n"
"v_lshl_add_u32 v254, v226, 29, v254\n"
"buffer_load_dwordx4 a[128:131], v254, s[56:59], s34 offen offset:0 sc1\n"
"v_bfe_u32 v235, v238, 8, 1\n"
"v_lshl_add_u32 v235, v235, 13, v233\n"
"v_bfe_u32 v226, v224, 2, 1\n"
"v_lshl_add_u32 v235, v226, 29, v235\n"
"buffer_load_dwordx4 a[132:135], v235, s[56:59], s34 offen offset:1024 sc1\n"
"v_bfe_u32 v254, v238, 16, 1\n"
"v_lshl_add_u32 v254, v254, 13, v233\n"
"v_bfe_u32 v226, v224, 4, 1\n"
"v_lshl_add_u32 v254, v226, 29, v254\n"
"buffer_load_dwordx4 a[136:139], v254, s[56:59], s34 offen offset:2048 sc1\n"
"v_bfe_u32 v235, v238, 24, 1\n"
"v_lshl_add_u32 v235, v235, 13, v233\n"
"v_bfe_u32 v226, v224, 6, 1\n"
"v_lshl_add_u32 v235, v226, 29, v235\n"
"buffer_load_dwordx4 a[140:143], v235, s[56:59], s34 offen offset:3072 sc1\n"
"v_bfe_u32 v254, v239, 0, 1\n"
"v_lshl_add_u32 v254, v254, 13, v233\n"
"v_bfe_u32 v226, v224, 8, 1\n"
"v_lshl_add_u32 v254, v226, 29, v254\n"
"buffer_load_dwordx4 a[144:147], v254, s[56:59], s35 offen offset:0 sc1\n"
"v_bfe_u32 v235, v239, 8, 1\n"
"v_lshl_add_u32 v235, v235, 13, v233\n"
"v_bfe_u32 v226, v224, 10, 1\n"
"v_lshl_add_u32 v235, v226, 29, v235\n"
"buffer_load_dwordx4 a[148:151], v235, s[56:59], s35 offen offset:1024 sc1\n"
"v_bfe_u32 v254, v239, 16, 1\n"
"v_lshl_add_u32 v254, v254, 13, v233\n"
"v_bfe_u32 v226, v224, 12, 1\n"
"v_lshl_add_u32 v254, v226, 29, v254\n"
"buffer_load_dwordx4 a[152:155], v254, s[56:59], s35 offen offset:2048 sc1\n"
"v_bfe_u32 v235, v239, 24, 1\n"
"v_lshl_add_u32 v235, v235, 13, v233\n"
"v_bfe_u32 v226, v224, 14, 1\n"
"v_lshl_add_u32 v235, v226, 29, v235\n"
"buffer_load_dwordx4 a[156:159], v235, s[56:59], s35 offen offset:3072 sc1\n"
"buffer_load_dwordx2 v[228:229], v232, s[60:63], 0 offen sc1\n"
"ds_read_b128 a[8:11], v225 offset:0\n"
"s_waitcnt lgkmcnt(0)\n"
"v_mfma_f32_16x16x32_bf16 v[40:43], a[0:3], a[8:11], 0\n"
"v_mfma_f32_16x16x32_bf16 v[40:43], a[16:19], a[32:35], v[40:43]\n"
"v_mfma_f32_16x16x32_bf16 v[40:43], a[16:19], a[36:39], v[40:43]\n"
"v_mfma_f32_16x16x32_bf16 v[40:43], a[16:19], a[40:43], v[40:43]\n"
"v_mfma_f32_16x16x32_bf16 v[40:43], a[16:19], a[44:47], v[40:43]\n"
"v_mfma_f32_16x16x32_bf16 v[40:43], a[16:19], a[48:51], v[40:43]\n"
"v_mfma_f32_16x16x32_bf16 v[40:43], a[16:19], a[52:55], v[40:43]\n"
"v_mfma_f32_16x16x32_bf16 v[40:43], a[16:19], a[56:59], v[40:43]\n"
"v_mfma_f32_16x16x32_bf16 v[40:43], a[16:19], a[60:63], v[40:43]\n"
"s_nop 7\n"
"v_cvt_pk_bf16_f32 v0, v40, v41\n"
"v_cvt_pk_bf16_f32 v1, v42, v43\n"
"s_add_u32 s19, s18, s98\n"
"buffer_store_dwordx2 v[0:1], v234, s[64:67], s19 offen \n"
"ds_read_b128 a[24:27], v225 offset:2048\n"
"s_waitcnt lgkmcnt(0)\n"
"v_mfma_f32_16x16x32_bf16 v[40:43], a[0:3], a[24:27], 0\n"
"v_mfma_f32_16x16x32_bf16 v[40:43], a[16:19], a[64:67], v[40:43]\n"
"v_mfma_f32_16x16x32_bf16 v[40:43], a[16:19], a[68:71], v[40:43]\n"
"v_mfma_f32_16x16x32_bf16 v[40:43], a[16:19], a[72:75], v[40:43]\n"
"v_mfma_f32_16x16x32_bf16 v[40:43], a[16:19], a[76:79], v[40:43]\n"
"v_mfma_f32_16x16x32_bf16 v[40:43], a[16:19], a[80:83], v[40:43]\n"
"v_mfma_f32_16x16x32_bf16 v[40:43], a[16:19], a[84:87], v[40:43]\n"
"v_mfma_f32_16x16x32_bf16 v[40:43], a[16:19], a[88:91], v[40:43]\n"
"v_mfma_f32_16x16x32_bf16 v[40:43], a[16:19], a[92:95], v[40:43]\n"
"s_nop 7\n"
"v_cvt_pk_bf16_f32 v2, v40, v41\n"
"v_cvt_pk_bf16_f32 v3, v42, v43\n"
"s_add_u32 s98, s98, 128\n"
"s_cmp_eq_u32 s98, 896\n"
"s_cselect_b32 s98, 0, s98\n"
"s_add_u32 s19, s18, s98\n"
"buffer_store_dwordx2 v[2:3], v234, s[64:67], s19 offen \n"
"ds_read_b128 a[8:11], v225 offset:4096\n"
"s_waitcnt lgkmcnt(0)\n"
"v_mfma_f32_16x16x32_bf16 v[40:43], a[0:3], a[8:11], 0\n"
"s_waitcnt vmcnt(18)\n"
"v_mfma_f32_16x16x32_bf16 v[40:43], a[16:19], a[96:99], v[40:43]\n"
"s_waitcnt vmcnt(17)\n"
"v_mfma_f32_16x16x32_bf16 v[40:43], a[16:19], a[100:103], v[40:43]\n"
"s_waitcnt vmcnt(16)\n"
"v_mfma_f32_16x16x32_bf16 v[40:43], a[16:19], a[104:107], v[40:43]\n"
"s_waitcnt vmcnt(15)\n"
"v_mfma_f32_16x16x32_bf16 v[40:43], a[16:19], a[108:111], v[40:43]\n"
"s_waitcnt vmcnt(14)\n"
"v_mfma_f32_16x16x32_bf16 v[40:43], a[16:19], a[112:115], v[40:43]\n"
"s_waitcnt vmcnt(13)\n"
"v_mfma_f32_16x16x32_bf16 v[40:43], a[16:19], a[116:119], v[40:43]\n"
"s_waitcnt vmcnt(12)\n"
"v_mfma_f32_16x16x32_bf16 v[40:43], a[16:19], a[120:123], v[40:43]\n"
"s_waitcnt vmcnt(11)\n"
"v_mfma_f32_16x16x32_bf16 v[40:43], a[16:19], a[124:127], v[40:43]\n"
"s_nop 7\n"
"v_cvt_pk_bf16_f32 v4, v40, v41\n"
"v_cvt_pk_bf16_f32 v5, v42, v43\n"
"s_add_u32 s98, s98, 128\n"
"s_cmp_eq_u32 s98, 896\n"
"s_cselect_b32 s98, 0, s98\n"
"s_add_u32 s19, s18, s98\n"
"buffer_store_dwordx2 v[4:5], v234, s[64:67], s19 offen \n"
"s_add_u32 s17, s90, 7\n"
"s_waitcnt vmcnt(3)\n"
"v_and_b32 v227, -9, v228\n"
"v_subrev_u32 v227, s17, v227\n"
"v_cmp_ne_u32 vcc, s91, v229\n"
"v_cmp_lt_u32 s[88:89], 0, v227\n"
"s_nop 4\n"
"s_or_b64 vcc, vcc, s[88:89]\n"
"s_cbranch_scc1 SPIN6_%=\n"
"BACK6_%=:\n"
"v_add_u32 v233, 0x100000, v233\n"
"v_not_b32 v238, v238\n"
"v_not_b32 v239, v239\n"
"v_bfe_u32 v254, v238, 0, 1\n"
"v_lshl_add_u32 v254, v254, 13, v233\n"
"v_bfe_u32 v226, v224, 0, 1\n"
"v_lshl_add_u32 v254, v226, 29, v254\n"
"buffer_load_dwordx4 a[160:163], v254, s[56:59], s34 offen offset:0 sc1\n"
"v_bfe_u32 v235, v238, 8, 1\n"
"v_lshl_add_u32 v235, v235, 13, v233\n"
"v_bfe_u32 v226, v224, 2, 1\n"
"v_lshl_add_u32 v235, v226, 29, v235\n"
"buffer_load_dwordx4 a[164:167], v235, s[56:59], s34 offen offset:1024 sc1\n"
"v_bfe_u32 v254, v238, 16, 1\n"
"v_lshl_add_u32 v254, v254, 13, v233\n"
"v_bfe_u32 v226, v224, 4, 1\n"
"v_lshl_add_u32 v254, v226, 29, v254\n"
"buffer_load_dwordx4 a[168:171], v254, s[56:59], s34 offen offset:2048 sc1\n"
"v_bfe_u32 v235, v238, 24, 1\n"
"v_lshl_add_u32 v235, v235, 13, v233\n"
"v_bfe_u32 v226, v224, 6, 1\n"
"v_lshl_add_u32 v235, v226, 29, v235\n"
"buffer_load_dwordx4 a[172:175], v235, s[56:59], s34 offen offset:3072 sc1\n"
"v_bfe_u32 v254, v239, 0, 1\n"
"v_lshl_add_u32 v254, v254, 13, v233\n"
"v_bfe_u32 v226, v224, 8, 1\n"
"v_lshl_add_u32 v254, v226, 29, v254\n"
"buffer_load_dwordx4 a[176:179], v254, s[56:59], s35 offen offset:0 sc1\n"
"v_bfe_u32 v235, v239, 8, 1\n"
"v_lshl_add_u32 v235, v235, 13, v233\n"
"v_bfe_u32 v226, v224, 10, 1\n"
"v_lshl_add_u32 v235, v226, 29, v235\n"
"buffer_load_dwordx4 a[180:183], v235, s[56:59], s35 offen offset:1024 sc1\n"
"v_bfe_u32 v254, v239, 16, 1\n"
"v_lshl_add_u32 v254, v254, 13, v233\n"
"v_bfe_u32 v226, v224, 12, 1\n"
"v_lshl_add_u32 v254, v226, 29, v254\n"
"buffer_load_dwordx4 a[184:187], v254, s[56:59], s35 offen offset:2048 sc1\n"
"v_bfe_u32 v235, v239, 24, 1\n"
"v_lshl_add_u32 v235, v235, 13, v233\n"
"v_bfe_u32 v226, v224, 14, 1\n"
"v_lshl_add_u32 v235, v226, 29, v235\n"
"buffer_load_dwordx4 a[188:191], v235, s[56:59], s35 offen offset:3072 sc1\n"
"v_add_u32 v233, 0x100000, v233\n"
"v_bfe_u32 v254, v238, 0, 1\n"
"v_lshl_add_u32 v254, v254, 13, v233\n"
"v_bfe_u32 v226, v224, 0, 1\n"
"v_lshl_add_u32 v254, v226, 29, v254\n"
"buffer_load_dwordx4 a[32:35], v254, s[56:59], s34 offen offset:0 sc1\n"
"v_bfe_u32 v235, v238, 8, 1\n"
"v_lshl_add_u32 v235, v235, 13, v233\n"
"v_bfe_u32 v226, v224, 2, 1\n"
"v_lshl_add_u32 v235, v226, 29, v235\n"
"buffer_load_dwordx4 a[36:39], v235, s[56:59], s34 offen offset:1024 sc1\n"
"v_bfe_u32 v254, v238, 16, 1\n"
"v_lshl_add_u32 v254, v254, 13, v233\n"
"v_bfe_u32 v226, v224, 4, 1\n"
"v_lshl_add_u32 v254, v226, 29, v254\n"
"buffer_load_dwordx4 a[40:43], v254, s[56:59], s34 offen offset:2048 sc1\n"
"v_bfe_u32 v235, v238, 24, 1\n"
"v_lshl_add_u32 v235, v235, 13, v233\n"
"v_bfe_u32 v226, v224, 6, 1\n"
"v_lshl_add_u32 v235, v226, 29, v235\n"
"buffer_load_dwordx4 a[44:47], v235, s[56:59], s34 offen offset:3072 sc1\n"
"v_bfe_u32 v254, v239, 0, 1\n"
"v_lshl_add_u32 v254, v254, 13, v233\n"
"v_bfe_u32 v226, v224, 8, 1\n"
"v_lshl_add_u32 v254, v226, 29, v254\n"
"buffer_load_dwordx4 a[48:51], v254, s[56:59], s35 offen offset:0 sc1\n"
"v_bfe_u32 v235, v239, 8, 1\n"
"v_lshl_add_u32 v235, v235, 13, v233\n"
"v_bfe_u32 v226, v224, 10, 1\n"
"v_lshl_add_u32 v235, v226, 29, v235\n"
"buffer_load_dwordx4 a[52:55], v235, s[56:59], s35 offen offset:1024 sc1\n"
"v_bfe_u32 v254, v239, 16, 1\n"
"v_lshl_add_u32 v254, v254, 13, v233\n"
"v_bfe_u32 v226, v224, 12, 1\n"
"v_lshl_add_u32 v254, v226, 29, v254\n"
"buffer_load_dwordx4 a[56:59], v254, s[56:59], s35 offen offset:2048 sc1\n"
"v_bfe_u32 v235, v239, 24, 1\n"
"v_lshl_add_u32 v235, v235, 13, v233\n"
"v_bfe_u32 v226, v224, 14, 1\n"
"v_lshl_add_u32 v235, v226, 29, v235\n"
"buffer_load_dwordx4 a[60:63], v235, s[56:59], s35 offen offset:3072 sc1\n"
"ds_read_b128 a[24:27], v225 offset:6144\n"
"s_waitcnt lgkmcnt(0)\n"
"v_mfma_f32_16x16x32_bf16 v[40:43], a[0:3], a[24:27], 0\n"
"v_mfma_f32_16x16x32_bf16 v[40:43], a[16:19], a[128:131], v[40:43]\n"
"v_mfma_f32_16x16x32_bf16 v[40:43], a[16:19], a[132:135], v[40:43]\n"
"v_mfma_f32_16x16x32_bf16 v[40:43], a[16:19], a[136:139], v[40:43]\n"
"v_mfma_f32_16x16x32_bf16 v[40:43], a[16:19], a[140:143], v[40:43]\n"
"v_mfma_f32_16x16x32_bf16 v[40:43], a[16:19], a[144:147], v[40:43]\n"
"v_mfma_f32_16x16x32_bf16 v[40:43], a[16:19], a[148:151], v[40:43]\n"
"v_mfma_f32_16x16x32_bf16 v[40:43], a[16:19], a[152:155], v[40:43]\n"
"v_mfma_f32_16x16x32_bf16 v[40:43], a[16:19], a[156:159], v[40:43]\n"
"s_nop 7\n"
"v_cvt_pk_bf16_f32 v6, v40, v41\n"
"v_cvt_pk_bf16_f32 v7, v42, v43\n"
"s_add_u32 s98, s98, 128\n"
"s_cmp_eq_u32 s98, 896\n"
"s_cselect_b32 s98, 0, s98\n"
"s_add_u32 s19, s18, s98\n"
"buffer_store_dwordx2 v[6:7], v234, s[64:67], s19 offen \n"
"v_add_u32 v233, 0x100000, v233\n"
"v_not_b32 v238, v238\n"
"v_not_b32 v239, v239\n"
"v_bfe_u32 v254, v238, 0, 1\n"
"v_lshl_add_u32 v254, v254, 13, v233\n"
"v_bfe_u32 v226, v224, 0, 1\n"
"v_lshl_add_u32 v254, v226, 29, v254\n"
"buffer_load_dwordx4 a[64:67], v254, s[56:59], s34 offen offset:0 sc1\n"
"v_bfe_u32 v235, v238, 8, 1\n"
"v_lshl_add_u32 v235, v235, 13, v233\n"
"v_bfe_u32 v226, v224, 2, 1\n"
"v_lshl_add_u32 v235, v226, 29, v235\n"
"buffer_load_dwordx4 a[68:71], v235, s[56:59], s34 offen offset:1024 sc1\n"
"v_bfe_u32 v254, v238, 16, 1\n"
"v_lshl_add_u32 v254, v254, 13, v233\n"
"v_bfe_u32 v226, v224, 4, 1\n"
"v_lshl_add_u32 v254, v226, 29, v254\n"
"buffer_load_dwordx4 a[72:75], v254, s[56:59], s34 offen offset:2048 sc1\n"
"v_bfe_u32 v235, v238, 24, 1\n"
"v_lshl_add_u32 v235, v235, 13, v233\n"
"v_bfe_u32 v226, v224, 6, 1\n"
"v_lshl_add_u32 v235, v226, 29, v235\n"
"buffer_load_dwordx4 a[76:79], v235, s[56:59], s34 offen offset:3072 sc1\n"
"v_bfe_u32 v254, v239, 0, 1\n"
"v_lshl_add_u32 v254, v254, 13, v233\n"
"v_bfe_u32 v226, v224, 8, 1\n"
"v_lshl_add_u32 v254, v226, 29, v254\n"
"buffer_load_dwordx4 a[80:83], v254, s[56:59], s35 offen offset:0 sc1\n"
"v_bfe_u32 v235, v239, 8, 1\n"
"v_lshl_add_u32 v235, v235, 13, v233\n"
"v_bfe_u32 v226, v224, 10, 1\n"
"v_lshl_add_u32 v235, v226, 29, v235\n"
"buffer_load_dwordx4 a[84:87], v235, s[56:59], s35 offen offset:1024 sc1\n"
"v_bfe_u32 v254, v239, 16, 1\n"
"v_lshl_add_u32 v254, v254, 13, v233\n"
"v_bfe_u32 v226, v224, 12, 1\n"
"v_lshl_add_u32 v254, v226, 29, v254\n"
"buffer_load_dwordx4 a[88:91], v254, s[56:59], s35 offen offset:2048 sc1\n"
"v_bfe_u32 v235, v239, 24, 1\n"
"v_lshl_add_u32 v235, v235, 13, v233\n"
"v_bfe_u32 v226, v224, 14, 1\n"
"v_lshl_add_u32 v235, v226, 29, v235\n"
"buffer_load_dwordx4 a[92:95], v235, s[56:59], s35 offen offset:3072 sc1\n"
"ds_read_b128 a[8:11], v225 offset:8192\n"
"s_waitcnt lgkmcnt(0)\n"
"v_mfma_f32_16x16x32_bf16 v[40:43], a[0:3], a[8:11], 0\n"
"s_waitcnt vmcnt(24)\n"
"v_mfma_f32_16x16x32_bf16 v[40:43], a[16:19], a[160:163], v[40:43]\n"
"s_waitcnt vmcnt(23)\n"
"v_mfma_f32_16x16x32_bf16 v[40:43], a[16:19], a[164:167], v[40:43]\n"
"s_waitcnt vmcnt(22)\n"
"v_mfma_f32_16x16x32_bf16 v[40:43], a[16:19], a[168:171], v[40:43]\n"
"s_waitcnt vmcnt(21)\n"
"v_mfma_f32_16x16x32_bf16 v[40:43], a[16:19], a[172:175], v[40:43]\n"
"s_waitcnt vmcnt(20)\n"
"v_mfma_f32_16x16x32_bf16 v[40:43], a[16:19], a[176:179], v[40:43]\n"
"s_waitcnt vmcnt(19)\n"
"v_mfma_f32_16x16x32_bf16 v[40:43], a[16:19], a[180:183], v[40:43]\n"
"s_waitcnt vmcnt(18)\n"
"v_mfma_f32_16x16x32_bf16 v[40:43], a[16:19], a[184:187], v[40:43]\n"
"s_waitcnt vmcnt(17)\n"
"v_mfma_f32_16x16x32_bf16 v[40:43], a[16:19], a[188:191], v[40:43]\n"
"s_nop 7\n"
"v_cvt_pk_bf16_f32 v8, v40, v41\n"
"v_cvt_pk_bf16_f32 v9, v42, v43\n"
"s_add_u32 s98, s98, 128\n"
"s_cmp_eq_u32 s98, 896\n"
"s_cselect_b32 s98, 0, s98\n"
"s_add_u32 s19, s18, s98\n"
"buffer_store_dwordx2 v[8:9], v234, s[64:67], s19 offen \n"
"s_barrier\n"
"ds_read_b128 a[24:27], v225 offset:10240\n"
"s_waitcnt lgkmcnt(0)\n"
"v_mfma_f32_16x16x32_bf16 v[40:43], a[0:3], a[24:27], 0\n"
"s_waitcnt vmcnt(17)\n"
"v_mfma_f32_16x16x32_bf16 v[40:43], a[16:19], a[32:35], v[40:43]\n"
"s_waitcnt vmcnt(16)\n"
"v_mfma_f32_16x16x32_bf16 v[40:43], a[16:19], a[36:39], v[40:43]\n"
"s_waitcnt vmcnt(15)\n"
"v_mfma_f32_16x16x32_bf16 v[40:43], a[16:19], a[40:43], v[40:43]\n"
"s_waitcnt vmcnt(14)\n"
"v_mfma_f32_16x16x32_bf16 v[40:43], a[16:19], a[44:47], v[40:43]\n"
"s_waitcnt vmcnt(13)\n"
"v_mfma_f32_16x16x32_bf16 v[40:43], a[16:19], a[48:51], v[40:43]\n"
"s_waitcnt vmcnt(12)\n"
"v_mfma_f32_16x16x32_bf16 v[40:43], a[16:19], a[52:55], v[40:43]\n"
"s_waitcnt vmcnt(11)\n"
"v_mfma_f32_16x16x32_bf16 v[40:43], a[16:19], a[56:59], v[40:43]\n"
"s_waitcnt vmcnt(10)\n"
"v_mfma_f32_16x16x32_bf16 v[40:43], a[16:19], a[60:63], v[40:43]\n"
"s_nop 7\n"
"v_cvt_pk_bf16_f32 v10, v40, v41\n"
"v_cvt_pk_bf16_f32 v11, v42, v43\n"
"s_add_u32 s98, s98, 128\n"
"s_cmp_eq_u32 s98, 896\n"
"s_cselect_b32 s98, 0, s98\n"
"s_add_u32 s19, s18, s98\n"
"buffer_store_dwordx2 v[10:11], v234, s[64:67], s19 offen \n"
"ds_read_b128 a[8:11], v225 offset:12288\n"
"s_waitcnt lgkmcnt(0)\n"
"v_mfma_f32_16x16x32_bf16 v[40:43], a[0:3], a[8:11], 0\n"
"s_waitcnt vmcnt(9)\n"
"v_mfma_f32_16x16x32_bf16 v[40:43], a[16:19], a[64:67], v[40:43]\n"
"s_waitcnt vmcnt(8)\n"
"v_mfma_f32_16x16x32_bf16 v[40:43], a[16:19], a[68:71], v[40:43]\n"
"s_waitcnt vmcnt(7)\n"
"v_mfma_f32_16x16x32_bf16 v[40:43], a[16:19], a[72:75], v[40:43]\n"
"s_waitcnt vmcnt(6)\n"
"v_mfma_f32_16x16x32_bf16 v[40:43], a[16:19], a[76:79], v[40:43]\n"
"s_waitcnt vmcnt(5)\n"
"v_mfma_f32_16x16x32_bf16 v[40:43], a[16:19], a[80:83], v[40:43]\n"
"s_waitcnt vmcnt(4)\n"
"v_mfma_f32_16x16x32_bf16 v[40:43], a[16:19], a[84:87], v[40:43]\n"
"s_waitcnt vmcnt(3)\n"
"v_mfma_f32_16x16x32_bf16 v[40:43], a[16:19], a[88:91], v[40:43]\n"
"s_waitcnt vmcnt(2)\n"
"v_mfma_f32_16x16x32_bf16 v[40:43], a[16:19], a[92:95], v[40:43]\n"
"s_nop 7\n"
"v_cvt_pk_bf16_f32 v12, v40, v41\n"
"v_cvt_pk_bf16_f32 v13, v42, v43\n"
"s_add_u32 s98, s98, 128\n"
"s_cmp_eq_u32 s98, 896\n"
"s_cselect_b32 s98, 0, s98\n"
"s_add_u32 s19, s18, s98\n"
"buffer_store_dwordx2 v[12:13], v234, s[64:67], s19 offen \n"
"s_branch END_%=\n"
"SPIN0_%=:\n"
"s_cmp_eq_u32 s16, 0\n"
"s_cbranch_scc1 BACK0_%=\n"
"s_sub_u32 s16, s16, 1\n"
"s_sleep 6\n"
"buffer_load_dwordx2 v[228:229], v232, s[60:63], 0 offen sc1\n"
"s_waitcnt vmcnt(0)\n"
"v_and_b32 v227, -9, v228\n"
"v_subrev_u32 v227, s17, v227\n"
"v_cmp_ne_u32 vcc, s91, v229\n"
"v_cmp_lt_u32 s[88:89], 6, v227\n"
"s_nop 4\n"
"s_or_b64 vcc, vcc, s[88:89]\n"
"s_cbranch_scc1 SPIN0_%=\n"
"s_branch BACK0_%=\n"
"SPIN1_%=:\n"
"s_cmp_eq_u32 s16, 0\n"
"s_cbranch_scc1 BACK1_%=\n"
"s_sub_u32 s16, s16, 1\n"
"s_sleep 6\n"
"buffer_load_dwordx2 v[228:229], v232, s[60:63], 0 offen sc1\n"
"s_waitcnt vmcnt(0)\n"
"v_and_b32 v227, -9, v228\n"
"v_subrev_u32 v227, s17, v227\n"
"v_cmp_ne_u32 vcc, s91, v229\n"
"v_cmp_lt_u32 s[88:89], 5, v227\n"
"s_nop 4\n"
"s_or_b64 vcc, vcc, s[88:89]\n"
"s_cbranch_scc1 SPIN1_%=\n"
"s_branch BACK1_%=\n"
"SPIN3_%=:\n"
"s_cmp_eq_u32 s16, 0\n"
"s_cbranch_scc1 BACK3_%=\n"
"s_sub_u32 s16, s16, 1\n"
"s_sleep 6\n"
"buffer_load_dwordx2 v[228:229], v232, s[60:63], 0 offen sc1\n"
"s_waitcnt vmcnt(0)\n"
"v_and_b32 v227, -9, v228\n"
"v_subrev_u32 v227, s17, v227\n"
"v_cmp_ne_u32 vcc, s91, v229\n"
"v_cmp_lt_u32 s[88:89], 3, v227\n"
"s_nop 4\n"
"s_or_b64 vcc, vcc, s[88:89]\n"
"s_cbranch_scc1 SPIN3_%=\n"
"s_branch BACK3_%=\n"
"SPIN6_%=:\n"
"s_cmp_eq_u32 s16, 0\n"
"s_cbranch_scc1 BACK6_%=\n"
"s_sub_u32 s16, s16, 1\n"
"s_sleep 6\n"
"buffer_load_dwordx2 v[228:229], v232, s[60:63], 0 offen sc1\n"
"s_waitcnt vmcnt(0)\n"
"v_and_b32 v227, -9, v228\n"
"v_subrev_u32 v227, s17, v227\n"
"v_cmp_ne_u32 vcc, s91, v229\n"
"v_cmp_lt_u32 s[88:89], 0, v227\n"
"s_nop 4\n"
"s_or_b64 vcc, vcc, s[88:89]\n"
"s_cbranch_scc1 SPIN6_%=\n"
"s_branch BACK6_%=\n"
"AGAIN_%=:\n"
"s_waitcnt vmcnt(0) lgkmcnt(0)\n"
"s_nop 15\n"
"s_barrier\n"
"s_mov_b32 s11, 1\n"
"s_branch START_%=\n"
"FALL_%=:\n"
"s_waitcnt vmcnt(0) lgkmcnt(0)\n"
"s_nop 15\n"
"s_barrier\n"
"v_bfe_u32 v126, v248, 4, 6\n"
"v_and_b32 v124, 7, v126\n"
"v_mul_u32_u24 v124, 2080, v124\n"
"v_bfe_u32 v125, v126, 3, 1\n"
"v_lshl_add_u32 v124, v125, 10, v124\n"
"v_and_b32 v125, 48, v126\n"
"s_mul_i32 s86, s84, 576\n"
"s_xor_b32 s86, s86, 0\n"
"v_add3_u32 v240, v124, v125, s86\n"
"s_mul_i32 s86, s84, 576\n"
"s_xor_b32 s86, s86, 64\n"
"v_add3_u32 v241, v124, v125, s86\n"
"s_mul_i32 s86, s84, 576\n"
"s_xor_b32 s86, s86, 512\n"
"v_add3_u32 v242, v124, v125, s86\n"
"s_mul_i32 s86, s84, 576\n"
"s_xor_b32 s86, s86, 576\n"
"v_add3_u32 v243, v124, v125, s86\n"
"v_mov_b32 v249, 0xff800000\n"
"v_mov_b32 v250, 0xff800000\n"
"v_mov_b32 v251, 0xff800000\n"
"v_mov_b32 v252, 0xff800000\n"
"s_mov_b32 s80, 0\n"
"FLOOP_%=:\n"
"s_add_u32 m0, s79, 0\n"
"s_nop 0\n"
"buffer_load_dwordx4 v248, s[44:47], s80 offen offset:0 lds\n"
"s_add_u32 m0, s79, 0\n"
"s_nop 0\n"
"buffer_load_dwordx4 v248, s[44:47], s80 offen offset:1024 lds\n"
"s_add_u32 m0, s79, 32\n"
"s_nop 0\n"
"buffer_load_dwordx4 v248, s[44:47], s80 offen offset:2048 lds\n"
"s_add_u32 m0, s79, 32\n"
"s_nop 0\n"
"buffer_load_dwordx4 v248, s[44:47], s80 offen offset:3072 lds\n"
"s_waitcnt vmcnt(0)\n"
"s_barrier\n"
"ds_read_b128 a[224:227], v240 offset:0\n"
"s_waitcnt lgkmcnt(0)\n"
"v_mfma_f32_16x16x32_bf16 v[4:7], a[224:227], a[0:3], 0\n"
"v_mfma_f32_16x16x32_bf16 v[12:15], a[224:227], a[8:11], 0\n"
"v_mfma_f32_16x16x32_bf16 v[20:23], a[224:227], a[16:19], 0\n"
"v_mfma_f32_16x16x32_bf16 v[28:31], a[224:227], a[24:27], 0\n"
"ds_read_b128 a[228:231], v242 offset:0\n"
"s_waitcnt lgkmcnt(0)\n"
"v_mfma_f32_16x16x32_bf16 v[60:63], a[228:231], a[0:3], 0\n"
"v_mfma_f32_16x16x32_bf16 v[68:71], a[228:231], a[8:11], 0\n"
"v_mfma_f32_16x16x32_bf16 v[76:79], a[228:231], a[16:19], 0\n"
"v_mfma_f32_16x16x32_bf16 v[84:87], a[228:231], a[24:27], 0\n"
"ds_read_b128 a[232:235], v241 offset:0\n"
"s_waitcnt lgkmcnt(0)\n"
"v_mfma_f32_16x16x32_bf16 v[4:7], a[232:235], a[32:35], v[4:7]\n"
"v_mfma_f32_16x16x32_bf16 v[12:15], a[232:235], a[40:43], v[12:15]\n"
"v_mfma_f32_16x16x32_bf16 v[20:23], a[232:235], a[48:51], v[20:23]\n"
"v_mfma_f32_16x16x32_bf16 v[28:31], a[232:235], a[4:7], v[28:31]\n"
"ds_read_b128 a[236:239], v243 offset:0\n"
"s_waitcnt lgkmcnt(0)\n"
"v_mfma_f32_16x16x32_bf16 v[60:63], a[236:239], a[32:35], v[60:63]\n"
"v_mfma_f32_16x16x32_bf16 v[68:71], a[236:239], a[40:43], v[68:71]\n"
"v_mfma_f32_16x16x32_bf16 v[76:79], a[236:239], a[48:51], v[76:79]\n"
"v_mfma_f32_16x16x32_bf16 v[84:87], a[236:239], a[4:7], v[84:87]\n"
"ds_read_b128 a[240:243], v240 offset:128\n"
"s_waitcnt lgkmcnt(0)\n"
"v_mfma_f32_16x16x32_bf16 v[4:7], a[240:243], a[56:59], v[4:7]\n"
"v_mfma_f32_16x16x32_bf16 v[12:15], a[240:243], a[64:67], v[12:15]\n"
"v_mfma_f32_16x16x32_bf16 v[20:23], a[240:243], a[72:75], v[20:23]\n"
"v_mfma_f32_16x16x32_bf16 v[28:31], a[240:243], a[80:83], v[28:31]\n"
"ds_read_b128 a[244:247], v242 offset:128\n"
"s_waitcnt lgkmcnt(0)\n"
"v_mfma_f32_16x16x32_bf16 v[60:63], a[244:247], a[56:59], v[60:63]\n"
"v_mfma_f32_16x16x32_bf16 v[68:71], a[244:247], a[64:67], v[68:71]\n"
"v_mfma_f32_16x16x32_bf16 v[76:79], a[244:247], a[72:75], v[76:79]\n"
"v_mfma_f32_16x16x32_bf16 v[84:87], a[244:247], a[80:83], v[84:87]\n"
"ds_read_b128 a[248:251], v241 offset:128\n"
"s_waitcnt lgkmcnt(0)\n"
"v_mfma_f32_16x16x32_bf16 v[4:7], a[248:251], a[88:91], v[4:7]\n"
"v_mfma_f32_16x16x32_bf16 v[12:15], a[248:251], a[96:99], v[12:15]\n"
"v_mfma_f32_16x16x32_bf16 v[20:23], a[248:251], a[104:107], v[20:23]\n"
"v_mfma_f32_16x16x32_bf16 v[28:31], a[248:251], a[12:15], v[28:31]\n"
"ds_read_b128 a[252:255], v243 offset:128\n"
"s_waitcnt lgkmcnt(0)\n"
"v_mfma_f32_16x16x32_bf16 v[60:63], a[252:255], a[88:91], v[60:63]\n"
"v_mfma_f32_16x16x32_bf16 v[68:71], a[252:255], a[96:99], v[68:71]\n"
"v_mfma_f32_16x16x32_bf16 v[76:79], a[252:255], a[104:107], v[76:79]\n"
"v_mfma_f32_16x16x32_bf16 v[84:87], a[252:255], a[12:15], v[84:87]\n"
"ds_read_b128 a[224:227], v240 offset:256\n"
"s_waitcnt lgkmcnt(0)\n"
"v_mfma_f32_16x16x32_bf16 v[4:7], a[224:227], a[112:115], v[4:7]\n"
"v_mfma_f32_16x16x32_bf16 v[12:15], a[224:227], a[120:123], v[12:15]\n"
"v_mfma_f32_16x16x32_bf16 v[20:23], a[224:227], a[128:131], v[20:23]\n"
"v_mfma_f32_16x16x32_bf16 v[28:31], a[224:227], a[136:139], v[28:31]\n"
"ds_read_b128 a[228:231], v242 offset:256\n"
"s_waitcnt lgkmcnt(0)\n"
"v_mfma_f32_16x16x32_bf16 v[60:63], a[228:231], a[112:115], v[60:63]\n"
"v_mfma_f32_16x16x32_bf16 v[68:71], a[228:231], a[120:123], v[68:71]\n"
"v_mfma_f32_16x16x32_bf16 v[76:79], a[228:231], a[128:131], v[76:79]\n"
"v_mfma_f32_16x16x32_bf16 v[84:87], a[228:231], a[136:139], v[84:87]\n"
"ds_read_b128 a[232:235], v241 offset:256\n"
"s_waitcnt lgkmcnt(0)\n"
"v_mfma_f32_16x16x32_bf16 v[4:7], a[232:235], a[144:147], v[4:7]\n"
"v_mfma_f32_16x16x32_bf16 v[12:15], a[232:235], a[152:155], v[12:15]\n"
"v_mfma_f32_16x16x32_bf16 v[20:23], a[232:235], a[160:163], v[20:23]\n"
"v_mfma_f32_16x16x32_bf16 v[28:31], a[232:235], a[20:23], v[28:31]\n"
"ds_read_b128 a[236:239], v243 offset:256\n"
"s_waitcnt lgkmcnt(0)\n"
"v_mfma_f32_16x16x32_bf16 v[60:63], a[236:239], a[144:147], v[60:63]\n"
"v_mfma_f32_16x16x32_bf16 v[68:71], a[236:239], a[152:155], v[68:71]\n"
"v_mfma_f32_16x16x32_bf16 v[76:79], a[236:239], a[160:163], v[76:79]\n"
"v_mfma_f32_16x16x32_bf16 v[84:87], a[236:239], a[20:23], v[84:87]\n"
"ds_read_b128 a[240:243], v240 offset:384\n"
"s_waitcnt lgkmcnt(0)\n"
"v_mfma_f32_16x16x32_bf16 v[4:7], a[240:243], a[168:171], v[4:7]\n"
"v_mfma_f32_16x16x32_bf16 v[12:15], a[240:243], a[176:179], v[12:15]\n"
"v_mfma_f32_16x16x32_bf16 v[20:23], a[240:243], a[184:187], v[20:23]\n"
"v_mfma_f32_16x16x32_bf16 v[28:31], a[240:243], a[192:195], v[28:31]\n"
"ds_read_b128 a[244:247], v242 offset:384\n"
"s_waitcnt lgkmcnt(0)\n"
"v_mfma_f32_16x16x32_bf16 v[60:63], a[244:247], a[168:171], v[60:63]\n"
"v_mfma_f32_16x16x32_bf16 v[68:71], a[244:247], a[176:179], v[68:71]\n"
"v_mfma_f32_16x16x32_bf16 v[76:79], a[244:247], a[184:187], v[76:79]\n"
"v_mfma_f32_16x16x32_bf16 v[84:87], a[244:247], a[192:195], v[84:87]\n"
"ds_read_b128 a[248:251], v241 offset:384\n"
"s_waitcnt lgkmcnt(0)\n"
"v_mfma_f32_16x16x32_bf16 v[4:7], a[248:251], a[200:203], v[4:7]\n"
"v_mfma_f32_16x16x32_bf16 v[12:15], a[248:251], a[208:211], v[12:15]\n"
"v_mfma_f32_16x16x32_bf16 v[20:23], a[248:251], a[216:219], v[20:23]\n"
"v_mfma_f32_16x16x32_bf16 v[28:31], a[248:251], a[28:31], v[28:31]\n"
"ds_read_b128 a[252:255], v243 offset:384\n"
"s_waitcnt lgkmcnt(0)\n"
"v_mfma_f32_16x16x32_bf16 v[60:63], a[252:255], a[200:203], v[60:63]\n"
"v_mfma_f32_16x16x32_bf16 v[68:71], a[252:255], a[208:211], v[68:71]\n"
"v_mfma_f32_16x16x32_bf16 v[76:79], a[252:255], a[216:219], v[76:79]\n"
"v_mfma_f32_16x16x32_bf16 v[84:87], a[252:255], a[28:31], v[84:87]\n"
"v_max3_f32 v249, v249, v4, v5\n"
"v_max3_f32 v249, v249, v6, v7\n"
"s_nop 2\n"
"v_max3_f32 v249, v249, v60, v61\n"
"v_max3_f32 v249, v249, v62, v63\n"
"v_max3_f32 v250, v250, v12, v13\n"
"v_max3_f32 v250, v250, v14, v15\n"
"v_max3_f32 v250, v250, v68, v69\n"
"v_max3_f32 v250, v250, v70, v71\n"
"v_max3_f32 v251, v251, v20, v21\n"
"v_max3_f32 v251, v251, v22, v23\n"
"v_max3_f32 v251, v251, v76, v77\n"
"v_max3_f32 v251, v251, v78, v79\n"
"v_max3_f32 v252, v252, v28, v29\n"
"v_max3_f32 v252, v252, v30, v31\n"
"v_max3_f32 v252, v252, v84, v85\n"
"v_max3_f32 v252, v252, v86, v87\n"
"s_barrier\n"
"s_add_u32 s80, s80, 16384\n"
"s_cmp_lt_u32 s80, 262144\n"
"s_nop 4\n"
"s_cbranch_scc1 FLOOP_%=\n"
"s_mov_b32 s11, 2\n"
"s_mov_b32 s85, 0x7f800000\n"
"s_nop 7\n"
"s_branch START_%=\n"
"END_%=:\n"
"s_waitcnt vmcnt(0) lgkmcnt(0)\n"

      :: "s"(__builtin_amdgcn_kernarg_segment_ptr()), "s"(blockIdx.x), "s"(w)
      : "v0","v1","v2","v3","v4","v5","v6","v7","v8","v9","v10","v11","v12","v13","v14","v15","v16","v17","v18","v19","v20","v21","v22","v23","v24","v25","v26","v27","v28","v29","v30","v31","v32","v33","v34","v35","v36","v37","v38","v39","v40","v41","v42","v43","v44","v45","v46","v47","v48","v49","v50","v51","v52","v53","v54","v55","v56","v57","v58","v59","v60","v61","v62","v63","v64","v65","v66","v67","v68","v69","v70","v71","v72","v73","v74","v75","v76","v77","v78","v79","v80","v81","v82","v83","v84","v85","v86","v87","v88","v89","v90","v91","v92","v93","v94","v95","v96","v97","v98","v99","v100","v101","v102","v103","v104","v105","v106","v107","v108","v109","v110","v111","v112","v113","v114","v115","v116","v117","v118","v119","v120","v121","v122","v123","v124","v125","v126","v127","v128","v129","v130","v131","v132","v133","v134","v135","v136","v137","v138","v139","v140","v141","v142","v143","v144","v145","v146","v147","v148","v149","v150","v151","v152","v153","v154","v155","v156","v157","v158","v159","v160","v161","v162","v163","v164","v165","v166","v167","v168","v169","v170","v171","v172","v173","v174","v175","v176","v177","v178","v179","v180","v181","v182","v183","v184","v185","v186","v187","v188","v189","v190","v191","v192","v193","v194","v195","v196","v197","v198","v199","v200","v201","v202","v203","v204","v205","v206","v207","v208","v209","v210","v211","v212","v213","v214","v215","v216","v217","v218","v219","v220","v221","v222","v223","v224","v225","v226","v227","v228","v229","v230","v231","v232","v233","v234","v235","v236","v237","v238","v239","v240","v241","v242","v243","v244","v245","v246","v247","v248","v249","v250","v251","v252","v253","v254","v255","a0","a1","a2","a3","a4","a5","a6","a7","a8","a9","a10","a11","a12","a13","a14","a15","a16","a17","a18","a19","a20","a21","a22","a23","a24","a25","a26","a27","a28","a29","a30","a31","a32","a33","a34","a35","a36","a37","a38","a39","a40","a41","a42","a43","a44","a45","a46","a47","a48","a49","a50","a51","a52","a53","a54","a55","a56","a57","a58","a59","a60","a61","a62","a63","a64","a65","a66","a67","a68","a69","a70","a71","a72","a73","a74","a75","a76","a77","a78","a79","a80","a81","a82","a83","a84","a85","a86","a87","a88","a89","a90","a91","a92","a93","a94","a95","a96","a97","a98","a99","a100","a101","a102","a103","a104","a105","a106","a107","a108","a109","a110","a111","a112","a113","a114","a115","a116","a117","a118","a119","a120","a121","a122","a123","a124","a125","a126","a127","a128","a129","a130","a131","a132","a133","a134","a135","a136","a137","a138","a139","a140","a141","a142","a143","a144","a145","a146","a147","a148","a149","a150","a151","a152","a153","a154","a155","a156","a157","a158","a159","a160","a161","a162","a163","a164","a165","a166","a167","a168","a169","a170","a171","a172","a173","a174","a175","a176","a177","a178","a179","a180","a181","a182","a183","a184","a185","a186","a187","a188","a189","a190","a191","a192","a193","a194","a195","a196","a197","a198","a199","a200","a201","a202","a203","a204","a205","a206","a207","a208","a209","a210","a211","a212","a213","a214","a215","a216","a217","a218","a219","a220","a221","a222","a223","a224","a225","a226","a227","a228","a229","a230","a231","a232","a233","a234","a235","a236","a237","a238","a239","a240","a241","a242","a243","a244","a245","a246","a247","a248","a249","a250","a251","a252","a253","a254","a255","s4","s5","s6","s7","s8","s9","s10","s11","s12","s13","s14","s15","s16","s17","s18","s19","s20","s21","s22","s23","s24","s25","s26","s27","s28","s29","s30","s31","s34","s35","s36","s37","s38","s39","s40","s41","s42","s43","s44","s45","s46","s47","s48","s49","s50","s51","s52","s53","s54","s55","s56","s57","s58","s59","s60","s61","s62","s63","s64","s65","s66","s67","s68","s69","s70","s71","s72","s73","s74","s75","s76","s77","s78","s79","s80","s81","s82","s83","s84","s85","s86","s87","s88","s89","s90","s91","s92","s93","s94","s95","s96","s97","s98","s99","vcc","scc","memory");
}

extern "C" void kernel_launch(void* const* d_in, const int* in_sizes, int n_in, void* d_out, int out_size, void* d_ws, size_t ws_size,
                              hipStream_t stream) {
  (void)hipFuncSetAttribute((const void*)concat_heads_fused_kernel, hipFuncAttributeMaxDynamicSharedMemorySize, 163840);
  concat_heads_fused_kernel<<<256, 256, 163840, stream>>>(
      (const char*)d_in[0], (const char*)d_in[1], (const char*)d_in[2], (const char*)d_in[3], (const char*)d_in[4],
      (const char*)d_in[5], (const char*)d_in[6], (const char*)d_in[7], (const char*)d_in[8], (const char*)d_in[9],
      (char*)d_out, (char*)d_ws);
}
```
